```python
import torch

def _softmax(x, axis=-1):
    return torch.softmax(x, dim=axis)

def _gelu(x):
    return torch.nn.functional.gelu(x, approximate='tanh')

def _silu(x):
    return torch.nn.functional.silu(x)

def _sigmoid(x):
    return torch.sigmoid(x)

def _layernorm(x, axis=-1, eps=1e-05):
    m = x.mean(dim=axis, keepdim=True)
    v = x.var(dim=axis, keepdim=True, unbiased=False)
    return (x - m) / torch.sqrt(v + eps)

def _scatter_add(tgt, idx, src, axis=0):
    return tgt.clone().index_add_(axis, idx, src)

def _segment_sum(src, seg, n):
    out = torch.zeros((n,) + src.shape[1:], dtype=src.dtype)
    return out.index_add_(0, seg, src)

def _pad(x, pw):
    flat = tuple((v for pair in reversed(pw) for v in pair))
    return torch.nn.functional.pad(x, flat)
kernel_name = 'interleave_layers__memory_aug_recurrent_mem_tr__glu_gated_geglu__prompt'

def setup_inputs():

    _w = lambda a: (lambda t: t.to(torch.bfloat16) if t.is_floating_point() else t)(torch.as_tensor(a))

    def _A_setup():
        B, S, Dm = (1, 2048, 512)
        H, Dh = (32, 128)
        M, seg = (8, 32)
        seg = min(seg, S)
        S = S // seg * seg
        return {'x': _w((lambda *s: torch.randn(*s, dtype=torch.bfloat16) * 0.05)(B, S, Dm)), 'mem0': _w((lambda *s: torch.randn(*s, dtype=torch.bfloat16) * 0.4)(B, M, Dm)), 'Wq': _w((lambda *s: torch.randn(*s, dtype=torch.bfloat16) * 0.8)(Dm, H, Dh) * Dm ** (-0.5)), 'Wk': _w((lambda *s: torch.randn(*s, dtype=torch.bfloat16) * 0.8)(Dm, H, Dh) * Dm ** (-0.5)), 'Wv': _w((lambda *s: torch.randn(*s, dtype=torch.bfloat16) * 0.4)(Dm, H, Dh) * Dm ** (-0.5)), 'Wo': _w((lambda *s: torch.randn(*s, dtype=torch.bfloat16) * 0.4)(H, Dh, Dm) * (H * Dh) ** (-0.5)), 'seg_len': seg}
    _A_in = _A_setup()

    def _B_setup():
        B, S, Dm, Df = (1, 2048, 512, 1792)
        return {'x': _w((lambda *s: torch.randn(*s, dtype=torch.bfloat16) * 0.05)(B, S, Dm)), 'w_gate': _w((lambda *s: torch.randn(*s, dtype=torch.bfloat16) * 0.4)(Dm, Df) * Dm ** (-0.5)), 'w_up': _w((lambda *s: torch.randn(*s, dtype=torch.bfloat16) * 0.4)(Dm, Df) * Dm ** (-0.5)), 'w_down': _w((lambda *s: torch.randn(*s, dtype=torch.bfloat16) * 0.4)(Df, Dm) * Df ** (-0.5))}
    _B_in = _B_setup()
    return {'A_x': _A_in['x'], 'A_mem0': _A_in['mem0'], 'A_Wq': _A_in['Wq'], 'A_Wk': _A_in['Wk'], 'A_Wv': _A_in['Wv'], 'A_Wo': _A_in['Wo'], 'A_seg_len': _A_in['seg_len'], 'B_x': _B_in['x'], 'B_w_gate': _B_in['w_gate'], 'B_w_up': _B_in['w_up'], 'B_w_down': _B_in['w_down']}

def reference(A_x, A_mem0, A_Wq, A_Wk, A_Wv, A_Wo, A_seg_len, B_x, B_w_gate, B_w_up, B_w_down):

    def _A_ref(x, mem0, Wq, Wk, Wv, Wo, seg_len):
        B, S, Dm = x.shape
        H, Dh = (Wq.shape[1], Wq.shape[2])
        M = mem0.shape[1]
        scale = Dh ** (-0.5)
        n_seg = S // seg_len
        L = M + seg_len
        idx = torch.arange(L)
        mask = (idx[:, None] < M) | (idx[:, None] >= idx[None, :])
        mem = mem0
        outs = []
        for s in range(n_seg):
            xs = x[:, s * seg_len:(s + 1) * seg_len, :].clone()
            h = torch.concatenate([mem, xs], axis=1)
            q = torch.einsum('btd,dhe->bhte', h, Wq)
            k = torch.einsum('btd,dhe->bhte', h, Wk)
            v = torch.einsum('btd,dhe->bhte', h, Wv)
            scores = torch.einsum('bhqe,bhke->bhqk', q, k) * scale
            scores = torch.where(mask[None, None], scores, -1000000000.0)
            p = _softmax(scores, axis=-1)
            o = torch.einsum('bhqk,bhke->bhqe', p, v)
            o = torch.einsum('bhte,hed->btd', o, Wo)
            mem = o[:, :M, :].clone()
            outs.append(o[:, M:, :])
        return torch.concatenate(outs, axis=1)

    def _B_ref(x, w_gate, w_up, w_down):
        g = torch.einsum('bsd,df->bsf', x, w_gate)
        u = torch.einsum('bsd,df->bsf', x, w_up)
        h = _gelu(g) * u
        return torch.einsum('bsf,fd->bsd', h, w_down)
    x = A_x
    x = x + _A_ref(_layernorm(x, axis=-1), A_mem0, A_Wq, A_Wk, A_Wv, A_Wo, A_seg_len)
    x = x + _B_ref(_layernorm(x, axis=-1), B_w_gate, B_w_up, B_w_down)
    x = x + _A_ref(_layernorm(x, axis=-1), A_mem0, A_Wq, A_Wk, A_Wv, A_Wo, A_seg_len)
    x = x + _B_ref(_layernorm(x, axis=-1), B_w_gate, B_w_up, B_w_down)
    return x
```

```cpp
#include <hip/hip_runtime.h>
#include <hip/hip_bf16.h>
typedef unsigned short u16;
__device__ __forceinline__ float b2f(u16 b){ return __uint_as_float(((unsigned)b)<<16); }
__device__ __forceinline__ u16 f2b(float f){ __hip_bfloat16 h = __float2bfloat16(f); return *(u16*)&h; }
__device__ __forceinline__ float rb(float f){ return b2f(f2b(f)); }

constexpr int S=2048, D=512, H=32, DH=128, HD=4096, M=8, SEG=32, DF=1792;

__global__ void ln_k(const u16* x, u16* y){
  int row=blockIdx.x; int t=threadIdx.x;
  float v[8]; float s=0;
  for(int i=0;i<8;i++){ v[i]=b2f(x[row*D+t*8+i]); s+=v[i]; }
  for(int o=32;o>=1;o>>=1) s+=__shfl_xor(s,o);
  float m=s/D; float q=0;
  for(int i=0;i<8;i++){ float d=v[i]-m; q+=d*d; }
  for(int o=32;o>=1;o>>=1) q+=__shfl_xor(q,o);
  float var=q/D; float r=rsqrtf(var+1e-5f);
  for(int i=0;i<8;i++) y[row*D+t*8+i]=f2b((v[i]-m)*r);
}
template<int MODE>
__global__ void gemm_k(const u16* A, int lda, const u16* B, int ldb, u16* C, int ldc, const u16* R, int Mr, int K){
  __shared__ float As[64][33]; __shared__ float Bs[32][65];
  int tx=threadIdx.x&15, ty=threadIdx.x>>4; int bm=blockIdx.y*64, bn=blockIdx.x*64;
  float acc[4][4]={};
  for(int k0=0;k0<K;k0+=32){
    for(int i=threadIdx.x;i<64*32;i+=256){ int r=i>>5,c=i&31; As[r][c]=(bm+r<Mr)?b2f(A[(size_t)(bm+r)*lda+k0+c]):0.f; }
    for(int i=threadIdx.x;i<32*64;i+=256){ int r=i>>6,c=i&63; Bs[r][c]=b2f(B[(size_t)(k0+r)*ldb+bn+c]); }
    __syncthreads();
    for(int k=0;k<32;k++){
      float a[4],b[4];
      for(int i=0;i<4;i++) a[i]=As[ty*4+i][k];
      for(int j=0;j<4;j++) b[j]=Bs[k][tx*4+j];
      for(int i=0;i<4;i++) for(int j=0;j<4;j++) acc[i][j]+=a[i]*b[j];
    }
    __syncthreads();
  }
  for(int i=0;i<4;i++) for(int j=0;j<4;j++){ int r=bm+ty*4+i,c=bn+tx*4+j; if(r<Mr){
    float v=acc[i][j]; if(MODE==1) v=b2f(R[(size_t)r*ldc+c])+rb(v); C[(size_t)r*ldc+c]=f2b(v);} }
}
__global__ void attn_k(const u16* Qx,const u16* Kx,const u16* Vx,const u16* qm,const u16* km,const u16* vm,u16* Om,u16* Ox,int seg0,int memrows){
  int h=blockIdx.x; int seg=seg0+blockIdx.y; __shared__ float k[40][129], v[40][128], q[32][129], p[32][41];
  const u16* kmm=km+(size_t)seg*M*HD; const u16* vmm=vm+(size_t)seg*M*HD;
  for(int i=threadIdx.x;i<40*128;i+=blockDim.x){ int r=i>>7,e=i&127;
    k[r][e]= r<8? b2f(kmm[r*HD+h*DH+e]) : b2f(Kx[(size_t)(seg*SEG+r-8)*HD+h*DH+e]);
    v[r][e]= r<8? b2f(vmm[r*HD+h*DH+e]) : b2f(Vx[(size_t)(seg*SEG+r-8)*HD+h*DH+e]); }
  int nq=memrows?8:32;
  for(int i=threadIdx.x;i<nq*128;i+=blockDim.x){ int r=i>>7,e=i&127; q[r][e]= memrows? b2f(qm[r*HD+h*DH+e]) : b2f(Qx[(size_t)(seg*SEG+r)*HD+h*DH+e]); }
  __syncthreads();
  for(int i=threadIdx.x;i<nq*40;i+=blockDim.x){ int r=i/40,c=i%40; float s=0; for(int e=0;e<128;e++) s+=q[r][e]*k[c][e];
    s=s*0.08838834764831845f; bool ok= memrows || (r+8>=c); p[r][c]= ok? s : -1e9f; }
  __syncthreads();
  if(threadIdx.x<nq){ int r=threadIdx.x; float m=-3e38f; for(int c=0;c<40;c++) m=fmaxf(m,p[r][c]); float s=0; for(int c=0;c<40;c++){ float e=__expf(p[r][c]-m); p[r][c]=e; s+=e;} for(int c=0;c<40;c++) p[r][c]=rb(p[r][c]/s); }
  __syncthreads();
  for(int i=threadIdx.x;i<nq*128;i+=blockDim.x){ int r=i>>7,e=i&127; float s=0; for(int c=0;c<40;c++) s+=p[r][c]*v[c][e];
    if(memrows) Om[r*HD+h*DH+e]=f2b(s); else Ox[(size_t)(seg*SEG+r)*HD+h*DH+e]=f2b(s); }
}
__global__ void geglu_k(const u16* g,const u16* u,u16* hh,int n){ int i=blockIdx.x*256+threadIdx.x; if(i<n){ float x=b2f(g[i]); float t=tanhf(0.7978845608028654f*(x+0.044715f*x*x*x)); float ge=rb(0.5f*x*(1.f+t)); hh[i]=f2b(ge*b2f(u[i])); } }

extern "C" void kernel_launch(void* const* d_in, const int* in_sizes, int n_in, void* d_out, int out_size, void* d_ws, size_t ws_size, hipStream_t stream){
  const u16* x0=(const u16*)d_in[0]; const u16* mem0=(const u16*)d_in[1]; const u16* Wq=(const u16*)d_in[2]; const u16* Wk=(const u16*)d_in[3]; const u16* Wv=(const u16*)d_in[4]; const u16* Wo=(const u16*)d_in[5];
  const u16* Wg=(const u16*)d_in[8]; const u16* Wu=(const u16*)d_in[9]; const u16* Wd=(const u16*)d_in[10];
  u16* ws=(u16*)d_ws; size_t o=0; auto al=[&](size_t n){ u16* p=ws+o; o+=n; return p; };
  u16* xn=al(S*D); u16* Q=al((size_t)S*HD); u16* K=al((size_t)S*HD); u16* V=al((size_t)S*HD); u16* O=al((size_t)S*HD);
  u16* km=al((size_t)64*M*HD); u16* vm=al((size_t)64*M*HD); u16* qm=al(M*HD); u16* om=al(M*HD); u16* mem=al(2*M*D);
  u16* xa=al(S*D); u16* xb=al(S*D); u16* g=al((size_t)S*DF); u16* u=al((size_t)S*DF); u16* hh=al((size_t)S*DF);
  const u16* xin=x0;
  for(int l=0;l<2;l++){
    ln_k<<<S,64,0,stream>>>(xin,xn);
    gemm_k<0><<<dim3(HD/64,S/64),256,0,stream>>>(xn,D,Wq,HD,Q,HD,nullptr,S,D);
    gemm_k<0><<<dim3(HD/64,S/64),256,0,stream>>>(xn,D,Wk,HD,K,HD,nullptr,S,D);
    gemm_k<0><<<dim3(HD/64,S/64),256,0,stream>>>(xn,D,Wv,HD,V,HD,nullptr,S,D);
    const u16* mc=mem0;
    for(int s=0;s<64;s++){
      gemm_k<0><<<dim3(HD/64,1),256,0,stream>>>(mc,D,Wq,HD,qm,HD,nullptr,M,D);
      gemm_k<0><<<dim3(HD/64,1),256,0,stream>>>(mc,D,Wk,HD,km+(size_t)s*M*HD,HD,nullptr,M,D);
      gemm_k<0><<<dim3(HD/64,1),256,0,stream>>>(mc,D,Wv,HD,vm+(size_t)s*M*HD,HD,nullptr,M,D);
      if(s<63){
        attn_k<<<dim3(H,1),256,0,stream>>>(Q,K,V,qm,km,vm,om,O,s,1);
        u16* mn=mem+(s&1)*M*D;
        gemm_k<0><<<dim3(D/64,1),256,0,stream>>>(om,HD,Wo,D,mn,D,nullptr,M,HD);
        mc=mn;
      }
    }
    attn_k<<<dim3(H,64),256,0,stream>>>(Q,K,V,qm,km,vm,om,O,0,0);
    gemm_k<1><<<dim3(D/64,S/64),256,0,stream>>>(O,HD,Wo,D,xa,D,xin,S,HD);
    ln_k<<<S,64,0,stream>>>(xa,xn);
    gemm_k<0><<<dim3(DF/64,S/64),256,0,stream>>>(xn,D,Wg,DF,g,DF,nullptr,S,D);
    gemm_k<0><<<dim3(DF/64,S/64),256,0,stream>>>(xn,D,Wu,DF,u,DF,nullptr,S,D);
    geglu_k<<<(S*DF+255)/256,256,0,stream>>>(g,u,hh,S*DF);
    u16* xo = l==0? xb : (u16*)d_out;
    gemm_k<1><<<dim3(D/64,S/64),256,0,stream>>>(hh,DF,Wd,D,xo,D,xa,S,DF);
    xin=xb;
  }
}
```

```cpp
#include <hip/hip_runtime.h>
#include <stdint.h>

typedef unsigned short u16;
typedef unsigned char u8;
typedef __bf16 bf16x2 __attribute__((ext_vector_type(2)));
typedef float f32x2 __attribute__((ext_vector_type(2)));
typedef unsigned v2u __attribute__((ext_vector_type(2)));
typedef unsigned v4u __attribute__((ext_vector_type(4)));
typedef unsigned v8u __attribute__((ext_vector_type(8)));
typedef unsigned long long u64;
#define GAS __attribute__((address_space(1)))
#define LAS __attribute__((address_space(3)))
#define DI __device__ __forceinline__
#define UNI(x) ((unsigned)__builtin_amdgcn_readfirstlane(x))

constexpr int SEQ = 2048, DM = 512, NH = 32, DH = 128, HD = 4096, NSEG = 64, DF = 1792;
constexpr float SPAN_CYC = 1850.f;
constexpr unsigned SENT = 0xFFFFFFFFu, OOB = 0x80000000u;
constexpr unsigned g_ex = 0xaf005500, g_ex2 = 3 | (10u << 8) | (40u << 16), g_dly = 10 | (8u << 16), g_stag = 0x54014046, g_dly2 = 54 | (47u << 16), g_dly3 = 54 | (30u << 16) | (104u << 24);

constexpr unsigned MB = 1u << 20;
constexpr unsigned O_COMM = 0, O_CTL = 2 * MB, O_RDY = O_CTL + 128 * 1024, O_EPOCH = O_RDY + 16384, O_PROBE = O_CTL + 256 * 1024, O_MEM0 = O_CTL + 512 * 1024;
constexpr unsigned O_XN0 = 4 * MB, O_XN = 8 * MB, O_X2 = 12 * MB, O_XM = 16 * MB, O_XNM = 20 * MB, O_KM = 24 * MB, O_VM = 32 * MB, O_PD = 40 * MB,
                   O_PW = 48 * MB, O_HF = 64 * MB, O_KF = 80 * MB, O_VF = 112 * MB, O_OF = 144 * MB, O_INB = 176 * MB, WS_BYTES = 210 * MB;
constexpr int F_KV = 0, F_QA = 32, F_WO = 48, F_GU = 64, F_DN = 80, F_R1 = 88, F_XN = 92, F_R = 128, F_GO = 160, F_G2 = 168;
constexpr int LDS_BYTES = 160 * 1024, L_DUMP = LDS_BYTES - 5120;
__host__ __device__ constexpr unsigned mp(unsigned o) { return ((o >> 13) << 14) | (o & 8191u); }
__host__ __device__ constexpr unsigned nearo(unsigned o, unsigned q13) { return o | (((o >> 8) ^ q13) & 0x2000u); }
__host__ __device__ constexpr int nloc(int rho) { return 16 * ((rho >> 2) & 1) + (rho & 3) + 4 * (rho >> 3); }
constexpr unsigned O_ABORT = O_CTL + mp(100 * 128);
__host__ __device__ constexpr unsigned fr(unsigned h, unsigned w) { return F_R + 64 * (h >> 3) + 4 * (h & 7) + w; }
DI unsigned flag(unsigned idx, unsigned q13) { return nearo(O_CTL + mp(idx * 128u), q13); }
constexpr unsigned O_STAMP = 0, NREC = WS_BYTES;

struct Params {
  const u16 *x, *mem0, *wq, *wk, *wv, *wo, *wg, *wu, *wd;
  u16* out;
  u8* ws;
  unsigned dly, stag, dly2, dly3, ex, ex2;
};

DI float b2f(unsigned b) { return __uint_as_float(b << 16); }
DI unsigned pk(float a, float b) {
  f32x2 v = {a, b};
  return __builtin_bit_cast(unsigned, __builtin_convertvector(v, bf16x2));
}

constexpr u64 STEP = 0x9E3779B97F4A7C15ull;
DI void st128(u8* ws, unsigned off, v4u v) { asm volatile("global_store_dwordx4 %0, %1, %2 sc1\n\ts_nop 1" ::"v"(off), "v"(v), "s"(ws) : "memory"); }
#define ADJ_UP 16
#define ADJ_DN 2
#define ADJ_MIN 0
#define RAMP_N 10
#define RAMP_B 3.f
#define WIN 3
#define SPAN0 20.f
#define B_FIRST 12
#define L_FIRST 5
#define START1 210
#define G1L1 120u
#define REL0 62
#define REL1 58
#define TOUCH(a) asm volatile("s_mov_b32 m0, %1\n\ts_nop 0\n\tglobal_load_lds_dword %0, off" ::"v"(a), "s"(tl) : "memory")
DI unsigned setup(const Params& p, LAS unsigned* lds, unsigned x, v2u& tag) {
  const __amdgpu_buffer_rsrc_t rs = __builtin_amdgcn_make_buffer_rsrc(p.ws, 0, NREC, 0x00020000);
  const unsigned tid = threadIdx.x, lane = tid & 63, w = UNI(tid >> 6), b = blockIdx.x, l16 = lane * 16;
  const unsigned t0 = (unsigned)__builtin_amdgcn_s_memrealtime();
  u64 t64 = 0;
  if (b >= 184) t64 = *(const GAS u64*)(p.ws + O_EPOCH);
  if (b >= 192) {
    const unsigned d = b - 192, row = w * 8 + (lane & 7), c8 = lane >> 3;
    v4u xv[8];
#pragma unroll
    for (int j = 0; j < 8; ++j) xv[j] = *(const GAS v4u*)(p.x + (d * 32 + row) * DM + (c8 + 8 * j) * 8);
    const unsigned rho = lane & 15, g = lane >> 4;
    v4u a = *(const GAS v4u*)(p.mem0 + (2 * (rho >> 2) + (rho & 1)) * DM + 64 * (d & 7) + 16 * g + 8 * ((rho >> 1) & 1));
#pragma unroll
    for (int i = 0; i < 6; ++i) st128(p.ws, (i < 4 ? O_COMM + MB + d * 16384 + w * 4096 : O_INB + d * 8192 + w * 2048 - 4096) + i * 1024 + l16, v4u{SENT, SENT, SENT, SENT});
    if (w < 2) st128(p.ws, O_CTL + d * 2048 + w * 1024 + l16, v4u{0u, 0u, 0u, 0u});
    asm volatile("" : "+v"(a), "+v"(xv[0]), "+v"(xv[7]));
    float v[64], s = 0.f, q = 0.f;
#pragma unroll
    for (int i = 0; i < 32; ++i) { v[2 * i] = b2f(xv[i >> 2][i & 3] & 0xffffu); v[2 * i + 1] = b2f(xv[i >> 2][i & 3] >> 16); s += v[2 * i] + v[2 * i + 1]; }
#pragma unroll
    for (int o = 8; o < 64; o <<= 1) s += __shfl_xor(s, o);
    const float mean = s * (1.f / DM);
#pragma unroll
    for (int i = 0; i < 64; ++i) { v[i] -= mean; q += v[i] * v[i]; }
#pragma unroll
    for (int o = 8; o < 64; o <<= 1) q += __shfl_xor(q, o);
    const float r = rsqrtf(q * (1.f / DM) + 1e-5f);
    const unsigned o = nearo(O_XN0 + d * 65536, 0) + (c8 >> 1) * 1024 + (c8 & 1) * 512 + row * 16;
#pragma unroll
    for (int j = 0; j < 8; ++j)
      st128(p.ws, o + mp(j * 4096), v4u{pk(v[8 * j] * r, v[8 * j + 1] * r), pk(v[8 * j + 2] * r, v[8 * j + 3] * r), pk(v[8 * j + 4] * r, v[8 * j + 5] * r), pk(v[8 * j + 6] * r, v[8 * j + 7] * r)});
    if (w == 2 && d < 8) {
      v4u t = {(a.x & 0xffffu) | (a.z << 16), (a.x >> 16) | (a.z & 0xffff0000u), (a.y & 0xffffu) | (a.w << 16), (a.y >> 16) | (a.w & 0xffff0000u)};
      t.x = min(t.x, SENT - 1u); t.y = min(t.y, SENT - 1u); t.z = min(t.z, SENT - 1u); t.w = min(t.w, SENT - 1u);
      st128(p.ws, O_MEM0 + d * 1024 + l16, t);
    }
  } else if (b >= 184) {
    GAS unsigned* a = (GAS unsigned*)(p.ws + O_PROBE) + ((b - 184) * 4 + w) * 64;
    unsigned best[2] = {~0u, ~0u};
    for (int i = 0; i <= (int)((p.stag >> 12) & 15u); ++i) {
      const unsigned side = (i ^ w) & 1;
      GAS unsigned* q = a + (i == 0 ? lane : side) * 2048;
      if (lane < (i == 0 ? 2u : 1u)) {
        u64 t0, t1; unsigned v; const unsigned z = 0;
        asm volatile("s_memtime %0\n\ts_waitcnt lgkmcnt(0)\n\tglobal_atomic_or %2, %3, %4, off sc0 sc1\n\ts_waitcnt vmcnt(0)\n\ts_memtime %1\n\ts_waitcnt lgkmcnt(0)"
                     : "=&s"(t0), "=&s"(t1), "=&v"(v) : "v"(q), "v"(z) : "memory");
        const unsigned d = i == 0 ? ~0u : (unsigned)(t1 - t0);
        if (side) best[1] = min(best[1], d); else best[0] = min(best[0], d);
      }
    }
    if (lane == 0) { lds[w] = best[0]; lds[4 + w] = best[1]; }
  }
  if (b >= 184) {
    asm volatile("s_waitcnt vmcnt(0) lgkmcnt(0)\n\ts_barrier" ::: "memory");
    if (tid < 16) {
      const unsigned b0 = min(min(lds[0], lds[1]), min(lds[2], lds[3])), b1 = min(min(lds[4], lds[5]), min(lds[6], lds[7]));
      const unsigned vote = b < 192 && (b1 < b0) != (x >= 4) ? 8u : 0u;
      __builtin_amdgcn_raw_buffer_store_b64(v2u{(unsigned)(t64 + STEP), ((unsigned)((t64 + STEP) >> 32) & ~255u) | vote | ((x - b) & 7u)}, rs, ((b - 184) * 16 + tid) * 8, O_RDY, 16);
    }
    asm volatile("s_barrier" ::: "memory");
  }
  {
    const unsigned u = (b >> 3) & 7, xi = x & 3, tl = (unsigned)(uintptr_t)lds + L_DUMP;
    if (x < 4) {
      const size_t o = (size_t)((u & 3) * 16 + (tid >> 4)) * HD + xi * 1024 + (tid & 15) * 64;
      if (b < 192 && (p.ex & 0x10000u)) {
        if (b >= 160) {
#pragma unroll
          for (int i = 0; i < 8; ++i) TOUCH((i & 1 ? p.wv : p.wk) + o + (size_t)((i | 1) * 64) * HD);
        }
      } else if (b < 192) {
#pragma unroll
        for (int i = 0; i < 4; ++i) TOUCH((i & 1 ? p.wv : p.wk) + o + (size_t)((4 * (i >> 1) + 2 * (u >> 2) + 1) * 64) * HD);
        if (b >= 160) {
#pragma unroll
          for (int i = 0; i < 8; ++i) TOUCH(p.wq + o + (size_t)(((2 * i & 7) + (i >> 2)) * 64) * HD);
        }
      } else {
#pragma unroll
        for (int i = 0; i < 4; ++i) TOUCH(p.wo + (size_t)(xi * 1024 + u * 128 + i * 32) * DM + tid * 64);
      }
    }
    const unsigned due = (b < 160 && x < 4 ? p.ex & 255u : p.stag & 255u) * 10u;
    if (x >= 4 && b < 192 && (p.ex & 0xff00u)) {
      while ((unsigned)__builtin_amdgcn_s_memrealtime() - t0 < ((p.ex >> 8) & 255u) * 10u) __builtin_amdgcn_s_sleep(4);
      const unsigned o = min((xi * 8 + u) * 256 + tid, 7167u) * 64;
      TOUCH(p.wg + o); TOUCH(p.wu + o); TOUCH(p.wg + o + 7168 * 64); TOUCH(p.wu + o + 7168 * 64); TOUCH(p.wd + o); TOUCH(p.wd + o + 7168 * 64);
    }
    while ((unsigned)__builtin_amdgcn_s_memrealtime() - t0 < due) __builtin_amdgcn_s_sleep(4);
  }
  if (b < 160) return 0;
  if (b < 184) t64 = *(const GAS u64*)(p.ws + O_EPOCH);
  tag = v2u{(unsigned)(t64 + STEP), (unsigned)((t64 + STEP) >> 32)};
#pragma unroll 1
  for (unsigned spins = 0; spins < (1u << 16); ++spins) {
    asm volatile("" ::: "memory");
    const v2u r = __builtin_amdgcn_raw_buffer_load_b64(rs, lane * 128, O_RDY, 16), r2 = __builtin_amdgcn_raw_buffer_load_b64(rs, l16, O_RDY + 8192, 16);
    if (!__any(((r.x ^ tag.x) | (r2.x ^ tag.x) | (((r.y ^ tag.y) | (r2.y ^ tag.y)) >> 8)) != 0u))
      return ((UNI(r.y) + b) & 7u) | (__popcll(__ballot(r.y & 8u)) > 4 ? 0u : 8u);
    __builtin_amdgcn_s_sleep(10);
  }
  return ~0u;
}

#define ROLE_CLOB "memory", "vcc", "scc", "v255", "a255", "s99"
struct Ctx { unsigned lb, l, q, slot; };
DI v2u ptr2(const void* p) { const u64 a = (u64)p; return v2u{UNI((unsigned)a), UNI((unsigned)(a >> 32))}; }

constexpr int RL_MEM = 0, RL_X = 8192, RL_QK = 16384, RL_O = RL_QK + 8192, RL_W = RL_O + 2048, RL_END = RL_W + 131072;
static_assert(RL_END <= L_DUMP && RL_W % 128 == 0 && RL_O % 16 == 0, "lds");
DI void role_R(const Params& p, const Ctx& c, unsigned x) {
  const unsigned r7 = (blockIdx.x >> 3) & 7, h = 8 * (x & 3) + r7;
  const unsigned tid = threadIdx.x, lane = tid & 63, w = UNI(tid >> 6);
  const unsigned n = lane & 15, g = lane >> 4, lb = c.lb, l16 = lane * 16, qq = (lane >> 2) & 3, pp = lane & 3;
  const unsigned comm = O_COMM;
  const v8u va = {l16,
                  lb + RL_W + w * 32768 + l16,
                  ((n >> 2) * 4 + g) * 256 + (n & 3) * 16,
                  g * 65536 + n * 16,
                  (g & 1) * 1024 + (g >> 1) * 512 + (16 * (n >> 3) + (n & 7)) * 16,
                  (g >> 1) * 1024 + (g & 1) * 512 + ((n & 3) + 8 * (n >> 2)) * 16,
                  (n & 2) == 0 ? (g & 1) * 256 + (g >> 1) * 128 + (2 * (n >> 2) + (n & 1)) * 16 : OOB,
                  lb + RL_QK + w * 2048 + ((n >> 2) & 1) * 272 + ((n >> 3) * 4 + (n & 3)) * 32 + g * 8};
  const v8u vb = {lb + RL_QK + w * 2048 + (g >> 1) * 544 + ((g & 1) * 4 + (n >> 2)) * 32 + (n & 3) * 8,
                  lb + RL_QK + (g >> 1) * 544 + ((g & 1) * 4 + (n >> 2)) * 32 + (n & 3) * 8,
                  lb + RL_O + (2 * w + ((n & 2) ? 0 : 1)) * 256 + (n ^ 2 ^ (g & 2)) * 16 + (g & 1) * 8,
                  lb + RL_O + (lane ^ (2 * (g & 1))) * 16,
                  (lane & 2) ? 0xEEEEu : 0x4444u,
                  lb + RL_MEM + w * 2048 + l16, lb + RL_MEM + (1 ^ w) * 2048 + l16, lb + RL_MEM + (2 ^ w) * 2048 + l16};
  const v4u vc = {lb + RL_MEM + (3 ^ w) * 2048 + l16, (n & 3) == 0 ? 0x00003F80u : ((n & 3) == 1 ? 0x3F800000u : 0u), n < 4 ? g * 16 + n * 4 : OOB, lane == 0 ? 0u : OOB};
  const unsigned vd = n * 16 + g * 4;
  const unsigned sr = lane >> 3, ch = lane & 7;
  const v4u ve = {((sr & 4) | ((sr & 1) << 1) | ((sr >> 1) & 1)) * 8192 + (ch ^ (2 * (sr >> 2))) * 16, sr * 1024 + (ch ^ (sr >> 2)) * 16,
                  lb + RL_W + w * 32768 + (16 * (g >> 1) + 4 * qq + (g & 1)) * 128 + ((2 * (w & 1)) ^ qq) * 32 + pp * 8,
                  lb + RL_W + w * 32768 + (16 * (g >> 1) + 8 * (qq >> 1) + 4 * (g & 1) + (qq & 1)) * 128 + pp * 32 + (g & 1) * 16};
  const unsigned col = h * DH + 64 * (w >> 1);
  const v2u pq = ptr2(p.wq + col), pkk = ptr2(p.wk + col), pv = ptr2(p.wv + col), po = ptr2(p.wo + (size_t)h * DH * DM + 128 * w);
  const v8u sw = {pq.x, pq.y, pkk.x, pkk.y, pv.x, pv.y, po.x, po.y};
  v2u sx = {2 * w, p.ex >> 24};
  v8u sa = {UNI((r7 >> 2) * 65536 + w * 16384 + (r7 & 3) * 64), UNI((4 * r7 + w) * 256), UNI(comm + MB + w * 2048), UNI(comm + MB + (4 * r7 + w) * 64),
            UNI(O_KF + r7 * 16384 + w * 2048), 0u, UNI(O_KM + mp(r7 * 2048) + w * 512), 0u};
  const v4u sb = {UNI(flag(F_KV + r7, 0)), UNI(flag(fr(r7, w), 0)), O_ABORT, UNI(O_MEM0 + w * 2048)};
  const v2u sg = {blockIdx.x, x};
  v4u sc = {0u, 0u, UNI(lb + RL_W + w * 32768), UNI(O_STAMP + c.slot * 8192 + w * 2048)};
  const v2u sd = {0u, p.dly};
  const v4u se = {p.dly2 + (B_FIRST << 16), comm + MB, comm + MB + h % 6 * 4, 0u};
  asm volatile(
    "s_mov_b64 exec, -1\ns_and_b32 s37, s37, 0xffff\ns_mov_b32 s39, 0x20000\ns_nop 4\n"
    "s_mov_b32 s70, 0x224000\n"
    "s_load_dwordx2 s[72:73], s[36:37], s70\n"
    "s_mov_b32 s78, 0\n"
    "s_mov_b32 s79, 0x220000\n"
    "L_R_PRO%=:\n"
    "v_xor_b32 v23, 16, v27\n"
    "v_xor_b32 v28, 64, v24\n"
    "v_xor_b32 v29, 32, v26\n"
    "s_and_b32 s70, s28, 2\n"
    "s_lshl_b32 s70, s70, 15\n"
    "s_sub_u32 s70, 0x8000, s70\n"
    "v_add_u32 v30, s70, v26\n"
    "v_add_u32 v31, s70, v29\n"
    "s_xor_b32 s70, s28, 0\n"
    "s_lshl_b32 s70, s70, 19\n"
    "s_add_u32 s30, s22, s70\n"
    "s_addc_u32 s31, s23, 0\n"
    "s_add_u32 m0, s54, 0\n"
    "s_nop 0\n"
    "global_load_lds_dwordx4 v24, s[30:31] \n"
    "s_add_u32 s30, s30, 65536\n"
    "s_addc_u32 s31, s31, 0\n"
    "s_add_u32 m0, s54, 1024\n"
    "s_nop 0\n"
    "global_load_lds_dwordx4 v28, s[30:31] \n"
    "s_add_u32 s30, s30, 65536\n"
    "s_addc_u32 s31, s31, 0\n"
    "s_add_u32 m0, s54, 2048\n"
    "s_nop 0\n"
    "global_load_lds_dwordx4 v24, s[30:31] \n"
    "s_add_u32 s30, s30, 65536\n"
    "s_addc_u32 s31, s31, 0\n"
    "s_add_u32 m0, s54, 3072\n"
    "s_nop 0\n"
    "global_load_lds_dwordx4 v28, s[30:31] \n"
    "s_add_u32 s30, s30, 65536\n"
    "s_addc_u32 s31, s31, 0\n"
    "s_add_u32 m0, s54, 4096\n"
    "s_nop 0\n"
    "global_load_lds_dwordx4 v24, s[30:31] \n"
    "s_add_u32 s30, s30, 65536\n"
    "s_addc_u32 s31, s31, 0\n"
    "s_add_u32 m0, s54, 5120\n"
    "s_nop 0\n"
    "global_load_lds_dwordx4 v28, s[30:31] \n"
    "s_add_u32 s30, s30, 65536\n"
    "s_addc_u32 s31, s31, 0\n"
    "s_add_u32 m0, s54, 6144\n"
    "s_nop 0\n"
    "global_load_lds_dwordx4 v24, s[30:31] \n"
    "s_add_u32 s30, s30, 65536\n"
    "s_addc_u32 s31, s31, 0\n"
    "s_add_u32 m0, s54, 7168\n"
    "s_nop 0\n"
    "global_load_lds_dwordx4 v28, s[30:31] \n"
    "s_xor_b32 s70, s28, 0\n"
    "s_lshl_b32 s70, s70, 19\n"
    "s_add_u32 s30, s24, s70\n"
    "s_addc_u32 s31, s25, 0\n"
    "s_add_u32 m0, s54, 8192\n"
    "s_nop 0\n"
    "global_load_lds_dwordx4 v24, s[30:31] \n"
    "s_add_u32 s30, s30, 65536\n"
    "s_addc_u32 s31, s31, 0\n"
    "s_add_u32 m0, s54, 9216\n"
    "s_nop 0\n"
    "global_load_lds_dwordx4 v28, s[30:31] \n"
    "s_add_u32 s30, s30, 65536\n"
    "s_addc_u32 s31, s31, 0\n"
    "s_add_u32 m0, s54, 10240\n"
    "s_nop 0\n"
    "global_load_lds_dwordx4 v24, s[30:31] \n"
    "s_add_u32 s30, s30, 65536\n"
    "s_addc_u32 s31, s31, 0\n"
    "s_add_u32 m0, s54, 11264\n"
    "s_nop 0\n"
    "global_load_lds_dwordx4 v28, s[30:31] \n"
    "s_add_u32 s30, s30, 65536\n"
    "s_addc_u32 s31, s31, 0\n"
    "s_add_u32 m0, s54, 12288\n"
    "s_nop 0\n"
    "global_load_lds_dwordx4 v24, s[30:31] \n"
    "s_add_u32 s30, s30, 65536\n"
    "s_addc_u32 s31, s31, 0\n"
    "s_add_u32 m0, s54, 13312\n"
    "s_nop 0\n"
    "global_load_lds_dwordx4 v28, s[30:31] \n"
    "s_add_u32 s30, s30, 65536\n"
    "s_addc_u32 s31, s31, 0\n"
    "s_add_u32 m0, s54, 14336\n"
    "s_nop 0\n"
    "global_load_lds_dwordx4 v24, s[30:31] \n"
    "s_add_u32 s30, s30, 65536\n"
    "s_addc_u32 s31, s31, 0\n"
    "s_add_u32 m0, s54, 15360\n"
    "s_nop 0\n"
    "global_load_lds_dwordx4 v28, s[30:31] \n"
    "s_xor_b32 s70, s28, 4\n"
    "s_lshl_b32 s70, s70, 19\n"
    "s_add_u32 s30, s22, s70\n"
    "s_addc_u32 s31, s23, 0\n"
    "s_add_u32 m0, s54, 16384\n"
    "s_nop 0\n"
    "global_load_lds_dwordx4 v24, s[30:31] \n"
    "s_add_u32 s30, s30, 65536\n"
    "s_addc_u32 s31, s31, 0\n"
    "s_add_u32 m0, s54, 17408\n"
    "s_nop 0\n"
    "global_load_lds_dwordx4 v28, s[30:31] \n"
    "s_add_u32 s30, s30, 65536\n"
    "s_addc_u32 s31, s31, 0\n"
    "s_add_u32 m0, s54, 18432\n"
    "s_nop 0\n"
    "global_load_lds_dwordx4 v24, s[30:31] \n"
    "s_add_u32 s30, s30, 65536\n"
    "s_addc_u32 s31, s31, 0\n"
    "s_add_u32 m0, s54, 19456\n"
    "s_nop 0\n"
    "global_load_lds_dwordx4 v28, s[30:31] \n"
    "s_add_u32 s30, s30, 65536\n"
    "s_addc_u32 s31, s31, 0\n"
    "s_add_u32 m0, s54, 20480\n"
    "s_nop 0\n"
    "global_load_lds_dwordx4 v24, s[30:31] \n"
    "s_add_u32 s30, s30, 65536\n"
    "s_addc_u32 s31, s31, 0\n"
    "s_add_u32 m0, s54, 21504\n"
    "s_nop 0\n"
    "global_load_lds_dwordx4 v28, s[30:31] \n"
    "s_add_u32 s30, s30, 65536\n"
    "s_addc_u32 s31, s31, 0\n"
    "s_add_u32 m0, s54, 22528\n"
    "s_nop 0\n"
    "global_load_lds_dwordx4 v24, s[30:31] \n"
    "s_add_u32 s30, s30, 65536\n"
    "s_addc_u32 s31, s31, 0\n"
    "s_add_u32 m0, s54, 23552\n"
    "s_nop 0\n"
    "global_load_lds_dwordx4 v28, s[30:31] \n"
    "s_xor_b32 s70, s28, 4\n"
    "s_lshl_b32 s70, s70, 19\n"
    "s_add_u32 s30, s24, s70\n"
    "s_addc_u32 s31, s25, 0\n"
    "s_add_u32 m0, s54, 24576\n"
    "s_nop 0\n"
    "global_load_lds_dwordx4 v24, s[30:31] \n"
    "s_add_u32 s30, s30, 65536\n"
    "s_addc_u32 s31, s31, 0\n"
    "s_add_u32 m0, s54, 25600\n"
    "s_nop 0\n"
    "global_load_lds_dwordx4 v28, s[30:31] \n"
    "s_add_u32 s30, s30, 65536\n"
    "s_addc_u32 s31, s31, 0\n"
    "s_add_u32 m0, s54, 26624\n"
    "s_nop 0\n"
    "global_load_lds_dwordx4 v24, s[30:31] \n"
    "s_add_u32 s30, s30, 65536\n"
    "s_addc_u32 s31, s31, 0\n"
    "s_add_u32 m0, s54, 27648\n"
    "s_nop 0\n"
    "global_load_lds_dwordx4 v28, s[30:31] \n"
    "s_add_u32 s30, s30, 65536\n"
    "s_addc_u32 s31, s31, 0\n"
    "s_add_u32 m0, s54, 28672\n"
    "s_nop 0\n"
    "global_load_lds_dwordx4 v24, s[30:31] \n"
    "s_add_u32 s30, s30, 65536\n"
    "s_addc_u32 s31, s31, 0\n"
    "s_add_u32 m0, s54, 29696\n"
    "s_nop 0\n"
    "global_load_lds_dwordx4 v28, s[30:31] \n"
    "s_add_u32 s30, s30, 65536\n"
    "s_addc_u32 s31, s31, 0\n"
    "s_add_u32 m0, s54, 30720\n"
    "s_nop 0\n"
    "global_load_lds_dwordx4 v24, s[30:31] \n"
    "s_add_u32 s30, s30, 65536\n"
    "s_addc_u32 s31, s31, 0\n"
    "s_add_u32 m0, s54, 31744\n"
    "s_nop 0\n"
    "global_load_lds_dwordx4 v28, s[30:31] \n"
    "s_waitcnt vmcnt(24) lgkmcnt(0)\n"
    "s_barrier\n"
    "ds_read_b64_tr_b16 v[72:73], v26 offset:0\n"
    "ds_read_b64_tr_b16 v[74:75], v26 offset:256\n"
    "ds_read_b64_tr_b16 v[76:77], v26 offset:4096\n"
    "ds_read_b64_tr_b16 v[78:79], v26 offset:4352\n"
    "ds_read_b64_tr_b16 v[80:81], v29 offset:0\n"
    "ds_read_b64_tr_b16 v[82:83], v29 offset:256\n"
    "ds_read_b64_tr_b16 v[84:85], v29 offset:4096\n"
    "ds_read_b64_tr_b16 v[86:87], v29 offset:4352\n"
    "ds_read_b64_tr_b16 a[88:89], v30 offset:0\n"
    "ds_read_b64_tr_b16 a[90:91], v30 offset:256\n"
    "ds_read_b64_tr_b16 a[92:93], v30 offset:4096\n"
    "ds_read_b64_tr_b16 a[94:95], v30 offset:4352\n"
    "ds_read_b64_tr_b16 a[128:129], v31 offset:0\n"
    "ds_read_b64_tr_b16 a[130:131], v31 offset:256\n"
    "ds_read_b64_tr_b16 a[132:133], v31 offset:4096\n"
    "ds_read_b64_tr_b16 a[134:135], v31 offset:4352\n"
    "s_waitcnt vmcnt(16) lgkmcnt(0)\n"
    "s_barrier\n"
    "s_xor_b32 s70, s28, 1\n"
    "s_lshl_b32 s70, s70, 19\n"
    "s_add_u32 s30, s22, s70\n"
    "s_addc_u32 s31, s23, 0\n"
    "s_add_u32 m0, s54, 0\n"
    "s_nop 0\n"
    "global_load_lds_dwordx4 v24, s[30:31] \n"
    "s_add_u32 s30, s30, 65536\n"
    "s_addc_u32 s31, s31, 0\n"
    "s_add_u32 m0, s54, 1024\n"
    "s_nop 0\n"
    "global_load_lds_dwordx4 v28, s[30:31] \n"
    "s_add_u32 s30, s30, 65536\n"
    "s_addc_u32 s31, s31, 0\n"
    "s_add_u32 m0, s54, 2048\n"
    "s_nop 0\n"
    "global_load_lds_dwordx4 v24, s[30:31] \n"
    "s_add_u32 s30, s30, 65536\n"
    "s_addc_u32 s31, s31, 0\n"
    "s_add_u32 m0, s54, 3072\n"
    "s_nop 0\n"
    "global_load_lds_dwordx4 v28, s[30:31] \n"
    "s_add_u32 s30, s30, 65536\n"
    "s_addc_u32 s31, s31, 0\n"
    "s_add_u32 m0, s54, 4096\n"
    "s_nop 0\n"
    "global_load_lds_dwordx4 v24, s[30:31] \n"
    "s_add_u32 s30, s30, 65536\n"
    "s_addc_u32 s31, s31, 0\n"
    "s_add_u32 m0, s54, 5120\n"
    "s_nop 0\n"
    "global_load_lds_dwordx4 v28, s[30:31] \n"
    "s_add_u32 s30, s30, 65536\n"
    "s_addc_u32 s31, s31, 0\n"
    "s_add_u32 m0, s54, 6144\n"
    "s_nop 0\n"
    "global_load_lds_dwordx4 v24, s[30:31] \n"
    "s_add_u32 s30, s30, 65536\n"
    "s_addc_u32 s31, s31, 0\n"
    "s_add_u32 m0, s54, 7168\n"
    "s_nop 0\n"
    "global_load_lds_dwordx4 v28, s[30:31] \n"
    "ds_read_b64_tr_b16 a[160:161], v26 offset:8192\n"
    "ds_read_b64_tr_b16 a[162:163], v26 offset:8448\n"
    "ds_read_b64_tr_b16 a[164:165], v26 offset:12288\n"
    "ds_read_b64_tr_b16 a[166:167], v26 offset:12544\n"
    "ds_read_b64_tr_b16 a[224:225], v29 offset:8192\n"
    "ds_read_b64_tr_b16 a[226:227], v29 offset:8448\n"
    "ds_read_b64_tr_b16 a[228:229], v29 offset:12288\n"
    "ds_read_b64_tr_b16 a[230:231], v29 offset:12544\n"
    "ds_read_b64_tr_b16 a[176:177], v30 offset:8192\n"
    "ds_read_b64_tr_b16 a[178:179], v30 offset:8448\n"
    "ds_read_b64_tr_b16 a[180:181], v30 offset:12288\n"
    "ds_read_b64_tr_b16 a[182:183], v30 offset:12544\n"
    "ds_read_b64_tr_b16 a[240:241], v31 offset:8192\n"
    "ds_read_b64_tr_b16 a[242:243], v31 offset:8448\n"
    "ds_read_b64_tr_b16 a[244:245], v31 offset:12288\n"
    "ds_read_b64_tr_b16 a[246:247], v31 offset:12544\n"
    "s_waitcnt vmcnt(16) lgkmcnt(0)\n"
    "s_barrier\n"
    "s_xor_b32 s70, s28, 1\n"
    "s_lshl_b32 s70, s70, 19\n"
    "s_add_u32 s30, s24, s70\n"
    "s_addc_u32 s31, s25, 0\n"
    "s_add_u32 m0, s54, 8192\n"
    "s_nop 0\n"
    "global_load_lds_dwordx4 v24, s[30:31] \n"
    "s_add_u32 s30, s30, 65536\n"
    "s_addc_u32 s31, s31, 0\n"
    "s_add_u32 m0, s54, 9216\n"
    "s_nop 0\n"
    "global_load_lds_dwordx4 v28, s[30:31] \n"
    "s_add_u32 s30, s30, 65536\n"
    "s_addc_u32 s31, s31, 0\n"
    "s_add_u32 m0, s54, 10240\n"
    "s_nop 0\n"
    "global_load_lds_dwordx4 v24, s[30:31] \n"
    "s_add_u32 s30, s30, 65536\n"
    "s_addc_u32 s31, s31, 0\n"
    "s_add_u32 m0, s54, 11264\n"
    "s_nop 0\n"
    "global_load_lds_dwordx4 v28, s[30:31] \n"
    "s_add_u32 s30, s30, 65536\n"
    "s_addc_u32 s31, s31, 0\n"
    "s_add_u32 m0, s54, 12288\n"
    "s_nop 0\n"
    "global_load_lds_dwordx4 v24, s[30:31] \n"
    "s_add_u32 s30, s30, 65536\n"
    "s_addc_u32 s31, s31, 0\n"
    "s_add_u32 m0, s54, 13312\n"
    "s_nop 0\n"
    "global_load_lds_dwordx4 v28, s[30:31] \n"
    "s_add_u32 s30, s30, 65536\n"
    "s_addc_u32 s31, s31, 0\n"
    "s_add_u32 m0, s54, 14336\n"
    "s_nop 0\n"
    "global_load_lds_dwordx4 v24, s[30:31] \n"
    "s_add_u32 s30, s30, 65536\n"
    "s_addc_u32 s31, s31, 0\n"
    "s_add_u32 m0, s54, 15360\n"
    "s_nop 0\n"
    "global_load_lds_dwordx4 v28, s[30:31] \n"
    "ds_read_b64_tr_b16 v[112:113], v26 offset:16384\n"
    "ds_read_b64_tr_b16 v[114:115], v26 offset:16640\n"
    "ds_read_b64_tr_b16 v[116:117], v26 offset:20480\n"
    "ds_read_b64_tr_b16 v[118:119], v26 offset:20736\n"
    "ds_read_b64_tr_b16 v[120:121], v29 offset:16384\n"
    "ds_read_b64_tr_b16 v[122:123], v29 offset:16640\n"
    "ds_read_b64_tr_b16 v[124:125], v29 offset:20480\n"
    "ds_read_b64_tr_b16 v[126:127], v29 offset:20736\n"
    "ds_read_b64_tr_b16 a[104:105], v30 offset:16384\n"
    "ds_read_b64_tr_b16 a[106:107], v30 offset:16640\n"
    "ds_read_b64_tr_b16 a[108:109], v30 offset:20480\n"
    "ds_read_b64_tr_b16 a[110:111], v30 offset:20736\n"
    "ds_read_b64_tr_b16 a[144:145], v31 offset:16384\n"
    "ds_read_b64_tr_b16 a[146:147], v31 offset:16640\n"
    "ds_read_b64_tr_b16 a[148:149], v31 offset:20480\n"
    "ds_read_b64_tr_b16 a[150:151], v31 offset:20736\n"
    "s_waitcnt vmcnt(16) lgkmcnt(0)\n"
    "s_barrier\n"
    "s_xor_b32 s70, s28, 5\n"
    "s_lshl_b32 s70, s70, 19\n"
    "s_add_u32 s30, s22, s70\n"
    "s_addc_u32 s31, s23, 0\n"
    "s_add_u32 m0, s54, 16384\n"
    "s_nop 0\n"
    "global_load_lds_dwordx4 v24, s[30:31] \n"
    "s_add_u32 s30, s30, 65536\n"
    "s_addc_u32 s31, s31, 0\n"
    "s_add_u32 m0, s54, 17408\n"
    "s_nop 0\n"
    "global_load_lds_dwordx4 v28, s[30:31] \n"
    "s_add_u32 s30, s30, 65536\n"
    "s_addc_u32 s31, s31, 0\n"
    "s_add_u32 m0, s54, 18432\n"
    "s_nop 0\n"
    "global_load_lds_dwordx4 v24, s[30:31] \n"
    "s_add_u32 s30, s30, 65536\n"
    "s_addc_u32 s31, s31, 0\n"
    "s_add_u32 m0, s54, 19456\n"
    "s_nop 0\n"
    "global_load_lds_dwordx4 v28, s[30:31] \n"
    "s_add_u32 s30, s30, 65536\n"
    "s_addc_u32 s31, s31, 0\n"
    "s_add_u32 m0, s54, 20480\n"
    "s_nop 0\n"
    "global_load_lds_dwordx4 v24, s[30:31] \n"
    "s_add_u32 s30, s30, 65536\n"
    "s_addc_u32 s31, s31, 0\n"
    "s_add_u32 m0, s54, 21504\n"
    "s_nop 0\n"
    "global_load_lds_dwordx4 v28, s[30:31] \n"
    "s_add_u32 s30, s30, 65536\n"
    "s_addc_u32 s31, s31, 0\n"
    "s_add_u32 m0, s54, 22528\n"
    "s_nop 0\n"
    "global_load_lds_dwordx4 v24, s[30:31] \n"
    "s_add_u32 s30, s30, 65536\n"
    "s_addc_u32 s31, s31, 0\n"
    "s_add_u32 m0, s54, 23552\n"
    "s_nop 0\n"
    "global_load_lds_dwordx4 v28, s[30:31] \n"
    "ds_read_b64_tr_b16 a[192:193], v26 offset:24576\n"
    "ds_read_b64_tr_b16 a[194:195], v26 offset:24832\n"
    "ds_read_b64_tr_b16 a[196:197], v26 offset:28672\n"
    "ds_read_b64_tr_b16 a[198:199], v26 offset:28928\n"
    "ds_read_b64_tr_b16 v[128:129], v29 offset:24576\n"
    "ds_read_b64_tr_b16 v[130:131], v29 offset:24832\n"
    "ds_read_b64_tr_b16 v[132:133], v29 offset:28672\n"
    "ds_read_b64_tr_b16 v[134:135], v29 offset:28928\n"
    "ds_read_b64_tr_b16 a[208:209], v30 offset:24576\n"
    "ds_read_b64_tr_b16 a[210:211], v30 offset:24832\n"
    "ds_read_b64_tr_b16 a[212:213], v30 offset:28672\n"
    "ds_read_b64_tr_b16 a[214:215], v30 offset:28928\n"
    "ds_read_b64_tr_b16 v[144:145], v31 offset:24576\n"
    "ds_read_b64_tr_b16 v[146:147], v31 offset:24832\n"
    "ds_read_b64_tr_b16 v[148:149], v31 offset:28672\n"
    "ds_read_b64_tr_b16 v[150:151], v31 offset:28928\n"
    "s_waitcnt vmcnt(16) lgkmcnt(0)\n"
    "s_barrier\n"
    "s_xor_b32 s70, s28, 5\n"
    "s_lshl_b32 s70, s70, 19\n"
    "s_add_u32 s30, s24, s70\n"
    "s_addc_u32 s31, s25, 0\n"
    "s_add_u32 m0, s54, 24576\n"
    "s_nop 0\n"
    "global_load_lds_dwordx4 v24, s[30:31] \n"
    "s_add_u32 s30, s30, 65536\n"
    "s_addc_u32 s31, s31, 0\n"
    "s_add_u32 m0, s54, 25600\n"
    "s_nop 0\n"
    "global_load_lds_dwordx4 v28, s[30:31] \n"
    "s_add_u32 s30, s30, 65536\n"
    "s_addc_u32 s31, s31, 0\n"
    "s_add_u32 m0, s54, 26624\n"
    "s_nop 0\n"
    "global_load_lds_dwordx4 v24, s[30:31] \n"
    "s_add_u32 s30, s30, 65536\n"
    "s_addc_u32 s31, s31, 0\n"
    "s_add_u32 m0, s54, 27648\n"
    "s_nop 0\n"
    "global_load_lds_dwordx4 v28, s[30:31] \n"
    "s_add_u32 s30, s30, 65536\n"
    "s_addc_u32 s31, s31, 0\n"
    "s_add_u32 m0, s54, 28672\n"
    "s_nop 0\n"
    "global_load_lds_dwordx4 v24, s[30:31] \n"
    "s_add_u32 s30, s30, 65536\n"
    "s_addc_u32 s31, s31, 0\n"
    "s_add_u32 m0, s54, 29696\n"
    "s_nop 0\n"
    "global_load_lds_dwordx4 v28, s[30:31] \n"
    "s_add_u32 s30, s30, 65536\n"
    "s_addc_u32 s31, s31, 0\n"
    "s_add_u32 m0, s54, 30720\n"
    "s_nop 0\n"
    "global_load_lds_dwordx4 v24, s[30:31] \n"
    "s_add_u32 s30, s30, 65536\n"
    "s_addc_u32 s31, s31, 0\n"
    "s_add_u32 m0, s54, 31744\n"
    "s_nop 0\n"
    "global_load_lds_dwordx4 v28, s[30:31] \n"
    "ds_read_b64_tr_b16 a[80:81], v26 offset:0\n"
    "ds_read_b64_tr_b16 a[82:83], v26 offset:256\n"
    "ds_read_b64_tr_b16 a[84:85], v26 offset:4096\n"
    "ds_read_b64_tr_b16 a[86:87], v26 offset:4352\n"
    "ds_read_b64_tr_b16 a[120:121], v29 offset:0\n"
    "ds_read_b64_tr_b16 a[122:123], v29 offset:256\n"
    "ds_read_b64_tr_b16 a[124:125], v29 offset:4096\n"
    "ds_read_b64_tr_b16 a[126:127], v29 offset:4352\n"
    "ds_read_b64_tr_b16 v[96:97], v30 offset:0\n"
    "ds_read_b64_tr_b16 v[98:99], v30 offset:256\n"
    "ds_read_b64_tr_b16 v[100:101], v30 offset:4096\n"
    "ds_read_b64_tr_b16 v[102:103], v30 offset:4352\n"
    "ds_read_b64_tr_b16 v[104:105], v31 offset:0\n"
    "ds_read_b64_tr_b16 v[106:107], v31 offset:256\n"
    "ds_read_b64_tr_b16 v[108:109], v31 offset:4096\n"
    "ds_read_b64_tr_b16 v[110:111], v31 offset:4352\n"
    "s_waitcnt vmcnt(16) lgkmcnt(0)\n"
    "s_barrier\n"
    "s_xor_b32 s70, s28, 0\n"
    "s_lshl_b32 s70, s70, 19\n"
    "s_add_u32 s30, s20, s70\n"
    "s_addc_u32 s31, s21, 0\n"
    "s_add_u32 m0, s54, 0\n"
    "s_nop 0\n"
    "global_load_lds_dwordx4 v24, s[30:31] \n"
    "s_add_u32 s30, s30, 65536\n"
    "s_addc_u32 s31, s31, 0\n"
    "s_add_u32 m0, s54, 1024\n"
    "s_nop 0\n"
    "global_load_lds_dwordx4 v28, s[30:31] \n"
    "s_add_u32 s30, s30, 65536\n"
    "s_addc_u32 s31, s31, 0\n"
    "s_add_u32 m0, s54, 2048\n"
    "s_nop 0\n"
    "global_load_lds_dwordx4 v24, s[30:31] \n"
    "s_add_u32 s30, s30, 65536\n"
    "s_addc_u32 s31, s31, 0\n"
    "s_add_u32 m0, s54, 3072\n"
    "s_nop 0\n"
    "global_load_lds_dwordx4 v28, s[30:31] \n"
    "s_add_u32 s30, s30, 65536\n"
    "s_addc_u32 s31, s31, 0\n"
    "s_add_u32 m0, s54, 4096\n"
    "s_nop 0\n"
    "global_load_lds_dwordx4 v24, s[30:31] \n"
    "s_add_u32 s30, s30, 65536\n"
    "s_addc_u32 s31, s31, 0\n"
    "s_add_u32 m0, s54, 5120\n"
    "s_nop 0\n"
    "global_load_lds_dwordx4 v28, s[30:31] \n"
    "s_add_u32 s30, s30, 65536\n"
    "s_addc_u32 s31, s31, 0\n"
    "s_add_u32 m0, s54, 6144\n"
    "s_nop 0\n"
    "global_load_lds_dwordx4 v24, s[30:31] \n"
    "s_add_u32 s30, s30, 65536\n"
    "s_addc_u32 s31, s31, 0\n"
    "s_add_u32 m0, s54, 7168\n"
    "s_nop 0\n"
    "global_load_lds_dwordx4 v28, s[30:31] \n"
    "ds_read_b64_tr_b16 a[168:169], v26 offset:8192\n"
    "ds_read_b64_tr_b16 a[170:171], v26 offset:8448\n"
    "ds_read_b64_tr_b16 a[172:173], v26 offset:12288\n"
    "ds_read_b64_tr_b16 a[174:175], v26 offset:12544\n"
    "ds_read_b64_tr_b16 a[232:233], v29 offset:8192\n"
    "ds_read_b64_tr_b16 a[234:235], v29 offset:8448\n"
    "ds_read_b64_tr_b16 a[236:237], v29 offset:12288\n"
    "ds_read_b64_tr_b16 a[238:239], v29 offset:12544\n"
    "ds_read_b64_tr_b16 a[184:185], v30 offset:8192\n"
    "ds_read_b64_tr_b16 a[186:187], v30 offset:8448\n"
    "ds_read_b64_tr_b16 a[188:189], v30 offset:12288\n"
    "ds_read_b64_tr_b16 a[190:191], v30 offset:12544\n"
    "ds_read_b64_tr_b16 a[248:249], v31 offset:8192\n"
    "ds_read_b64_tr_b16 a[250:251], v31 offset:8448\n"
    "ds_read_b64_tr_b16 a[252:253], v31 offset:12288\n"
    "ds_read_b64_tr_b16 a[254:255], v31 offset:12544\n"
    "s_waitcnt vmcnt(16) lgkmcnt(0)\n"
    "s_barrier\n"
    "s_xor_b32 s70, s28, 4\n"
    "s_lshl_b32 s70, s70, 19\n"
    "s_add_u32 s30, s20, s70\n"
    "s_addc_u32 s31, s21, 0\n"
    "s_add_u32 m0, s54, 8192\n"
    "s_nop 0\n"
    "global_load_lds_dwordx4 v24, s[30:31] \n"
    "s_add_u32 s30, s30, 65536\n"
    "s_addc_u32 s31, s31, 0\n"
    "s_add_u32 m0, s54, 9216\n"
    "s_nop 0\n"
    "global_load_lds_dwordx4 v28, s[30:31] \n"
    "s_add_u32 s30, s30, 65536\n"
    "s_addc_u32 s31, s31, 0\n"
    "s_add_u32 m0, s54, 10240\n"
    "s_nop 0\n"
    "global_load_lds_dwordx4 v24, s[30:31] \n"
    "s_add_u32 s30, s30, 65536\n"
    "s_addc_u32 s31, s31, 0\n"
    "s_add_u32 m0, s54, 11264\n"
    "s_nop 0\n"
    "global_load_lds_dwordx4 v28, s[30:31] \n"
    "s_add_u32 s30, s30, 65536\n"
    "s_addc_u32 s31, s31, 0\n"
    "s_add_u32 m0, s54, 12288\n"
    "s_nop 0\n"
    "global_load_lds_dwordx4 v24, s[30:31] \n"
    "s_add_u32 s30, s30, 65536\n"
    "s_addc_u32 s31, s31, 0\n"
    "s_add_u32 m0, s54, 13312\n"
    "s_nop 0\n"
    "global_load_lds_dwordx4 v28, s[30:31] \n"
    "s_add_u32 s30, s30, 65536\n"
    "s_addc_u32 s31, s31, 0\n"
    "s_add_u32 m0, s54, 14336\n"
    "s_nop 0\n"
    "global_load_lds_dwordx4 v24, s[30:31] \n"
    "s_add_u32 s30, s30, 65536\n"
    "s_addc_u32 s31, s31, 0\n"
    "s_add_u32 m0, s54, 15360\n"
    "s_nop 0\n"
    "global_load_lds_dwordx4 v28, s[30:31] \n"
    "ds_read_b64_tr_b16 a[96:97], v26 offset:16384\n"
    "ds_read_b64_tr_b16 a[98:99], v26 offset:16640\n"
    "ds_read_b64_tr_b16 a[100:101], v26 offset:20480\n"
    "ds_read_b64_tr_b16 a[102:103], v26 offset:20736\n"
    "ds_read_b64_tr_b16 a[136:137], v29 offset:16384\n"
    "ds_read_b64_tr_b16 a[138:139], v29 offset:16640\n"
    "ds_read_b64_tr_b16 a[140:141], v29 offset:20480\n"
    "ds_read_b64_tr_b16 a[142:143], v29 offset:20736\n"
    "ds_read_b64_tr_b16 a[112:113], v30 offset:16384\n"
    "ds_read_b64_tr_b16 a[114:115], v30 offset:16640\n"
    "ds_read_b64_tr_b16 a[116:117], v30 offset:20480\n"
    "ds_read_b64_tr_b16 a[118:119], v30 offset:20736\n"
    "ds_read_b64_tr_b16 a[152:153], v31 offset:16384\n"
    "ds_read_b64_tr_b16 a[154:155], v31 offset:16640\n"
    "ds_read_b64_tr_b16 a[156:157], v31 offset:20480\n"
    "ds_read_b64_tr_b16 a[158:159], v31 offset:20736\n"
    "s_waitcnt vmcnt(16) lgkmcnt(0)\n"
    "s_barrier\n"
    "s_xor_b32 s70, s28, 5\n"
    "s_lshl_b32 s70, s70, 19\n"
    "s_add_u32 s30, s20, s70\n"
    "s_addc_u32 s31, s21, 0\n"
    "s_add_u32 m0, s54, 16384\n"
    "s_nop 0\n"
    "global_load_lds_dwordx4 v24, s[30:31] \n"
    "s_add_u32 s30, s30, 65536\n"
    "s_addc_u32 s31, s31, 0\n"
    "s_add_u32 m0, s54, 17408\n"
    "s_nop 0\n"
    "global_load_lds_dwordx4 v28, s[30:31] \n"
    "s_add_u32 s30, s30, 65536\n"
    "s_addc_u32 s31, s31, 0\n"
    "s_add_u32 m0, s54, 18432\n"
    "s_nop 0\n"
    "global_load_lds_dwordx4 v24, s[30:31] \n"
    "s_add_u32 s30, s30, 65536\n"
    "s_addc_u32 s31, s31, 0\n"
    "s_add_u32 m0, s54, 19456\n"
    "s_nop 0\n"
    "global_load_lds_dwordx4 v28, s[30:31] \n"
    "s_add_u32 s30, s30, 65536\n"
    "s_addc_u32 s31, s31, 0\n"
    "s_add_u32 m0, s54, 20480\n"
    "s_nop 0\n"
    "global_load_lds_dwordx4 v24, s[30:31] \n"
    "s_add_u32 s30, s30, 65536\n"
    "s_addc_u32 s31, s31, 0\n"
    "s_add_u32 m0, s54, 21504\n"
    "s_nop 0\n"
    "global_load_lds_dwordx4 v28, s[30:31] \n"
    "s_add_u32 s30, s30, 65536\n"
    "s_addc_u32 s31, s31, 0\n"
    "s_add_u32 m0, s54, 22528\n"
    "s_nop 0\n"
    "global_load_lds_dwordx4 v24, s[30:31] \n"
    "s_add_u32 s30, s30, 65536\n"
    "s_addc_u32 s31, s31, 0\n"
    "s_add_u32 m0, s54, 23552\n"
    "s_nop 0\n"
    "global_load_lds_dwordx4 v28, s[30:31] \n"
    "ds_read_b64_tr_b16 a[200:201], v26 offset:24576\n"
    "ds_read_b64_tr_b16 a[202:203], v26 offset:24832\n"
    "ds_read_b64_tr_b16 a[204:205], v26 offset:28672\n"
    "ds_read_b64_tr_b16 a[206:207], v26 offset:28928\n"
    "ds_read_b64_tr_b16 v[136:137], v29 offset:24576\n"
    "ds_read_b64_tr_b16 v[138:139], v29 offset:24832\n"
    "ds_read_b64_tr_b16 v[140:141], v29 offset:28672\n"
    "ds_read_b64_tr_b16 v[142:143], v29 offset:28928\n"
    "ds_read_b64_tr_b16 a[216:217], v30 offset:24576\n"
    "ds_read_b64_tr_b16 a[218:219], v30 offset:24832\n"
    "ds_read_b64_tr_b16 a[220:221], v30 offset:28672\n"
    "ds_read_b64_tr_b16 a[222:223], v30 offset:28928\n"
    "ds_read_b64_tr_b16 v[152:153], v31 offset:24576\n"
    "ds_read_b64_tr_b16 v[154:155], v31 offset:24832\n"
    "ds_read_b64_tr_b16 v[156:157], v31 offset:28672\n"
    "ds_read_b64_tr_b16 v[158:159], v31 offset:28928\n"
    "s_waitcnt vmcnt(16) lgkmcnt(0)\n"
    "s_barrier\n"
    "s_xor_b32 s70, s28, 1\n"
    "s_lshl_b32 s70, s70, 19\n"
    "s_add_u32 s30, s20, s70\n"
    "s_addc_u32 s31, s21, 0\n"
    "s_add_u32 m0, s54, 24576\n"
    "s_nop 0\n"
    "global_load_lds_dwordx4 v24, s[30:31] \n"
    "s_add_u32 s30, s30, 65536\n"
    "s_addc_u32 s31, s31, 0\n"
    "s_add_u32 m0, s54, 25600\n"
    "s_nop 0\n"
    "global_load_lds_dwordx4 v28, s[30:31] \n"
    "s_add_u32 s30, s30, 65536\n"
    "s_addc_u32 s31, s31, 0\n"
    "s_add_u32 m0, s54, 26624\n"
    "s_nop 0\n"
    "global_load_lds_dwordx4 v24, s[30:31] \n"
    "s_add_u32 s30, s30, 65536\n"
    "s_addc_u32 s31, s31, 0\n"
    "s_add_u32 m0, s54, 27648\n"
    "s_nop 0\n"
    "global_load_lds_dwordx4 v28, s[30:31] \n"
    "s_add_u32 s30, s30, 65536\n"
    "s_addc_u32 s31, s31, 0\n"
    "s_add_u32 m0, s54, 28672\n"
    "s_nop 0\n"
    "global_load_lds_dwordx4 v24, s[30:31] \n"
    "s_add_u32 s30, s30, 65536\n"
    "s_addc_u32 s31, s31, 0\n"
    "s_add_u32 m0, s54, 29696\n"
    "s_nop 0\n"
    "global_load_lds_dwordx4 v28, s[30:31] \n"
    "s_add_u32 s30, s30, 65536\n"
    "s_addc_u32 s31, s31, 0\n"
    "s_add_u32 m0, s54, 30720\n"
    "s_nop 0\n"
    "global_load_lds_dwordx4 v24, s[30:31] \n"
    "s_add_u32 s30, s30, 65536\n"
    "s_addc_u32 s31, s31, 0\n"
    "s_add_u32 m0, s54, 31744\n"
    "s_nop 0\n"
    "global_load_lds_dwordx4 v28, s[30:31] \n"
    "ds_read_b64_tr_b16 v[56:57], v26 offset:0\n"
    "ds_read_b64_tr_b16 v[58:59], v26 offset:256\n"
    "ds_read_b64_tr_b16 v[60:61], v26 offset:4096\n"
    "ds_read_b64_tr_b16 v[62:63], v26 offset:4352\n"
    "ds_read_b64_tr_b16 v[64:65], v29 offset:0\n"
    "ds_read_b64_tr_b16 v[66:67], v29 offset:256\n"
    "ds_read_b64_tr_b16 v[68:69], v29 offset:4096\n"
    "ds_read_b64_tr_b16 v[70:71], v29 offset:4352\n"
    "ds_read_b64_tr_b16 a[8:9], v30 offset:0\n"
    "ds_read_b64_tr_b16 a[10:11], v30 offset:256\n"
    "ds_read_b64_tr_b16 a[12:13], v30 offset:4096\n"
    "ds_read_b64_tr_b16 a[14:15], v30 offset:4352\n"
    "ds_read_b64_tr_b16 a[48:49], v31 offset:0\n"
    "ds_read_b64_tr_b16 a[50:51], v31 offset:256\n"
    "ds_read_b64_tr_b16 a[52:53], v31 offset:4096\n"
    "ds_read_b64_tr_b16 a[54:55], v31 offset:4352\n"
    "s_waitcnt vmcnt(16) lgkmcnt(0)\n"
    "s_barrier\n"
    "s_add_u32 s30, s26, 0\n"
    "s_addc_u32 s31, s27, 0\n"
    "s_add_u32 m0, s54, 0\n"
    "s_nop 0\n"
    "global_load_lds_dwordx4 v25, s[30:31] \n"
    "s_add_u32 s30, s30, 8192\n"
    "s_addc_u32 s31, s31, 0\n"
    "s_add_u32 m0, s54, 1024\n"
    "s_nop 0\n"
    "global_load_lds_dwordx4 v25, s[30:31] \n"
    "s_add_u32 s30, s30, 8192\n"
    "s_addc_u32 s31, s31, 0\n"
    "s_add_u32 m0, s54, 2048\n"
    "s_nop 0\n"
    "global_load_lds_dwordx4 v25, s[30:31] \n"
    "s_add_u32 s30, s30, 8192\n"
    "s_addc_u32 s31, s31, 0\n"
    "s_add_u32 m0, s54, 3072\n"
    "s_nop 0\n"
    "global_load_lds_dwordx4 v25, s[30:31] \n"
    "s_add_u32 s30, s30, 8192\n"
    "s_addc_u32 s31, s31, 0\n"
    "s_add_u32 m0, s54, 4096\n"
    "s_nop 0\n"
    "global_load_lds_dwordx4 v25, s[30:31] \n"
    "s_add_u32 s30, s30, 8192\n"
    "s_addc_u32 s31, s31, 0\n"
    "s_add_u32 m0, s54, 5120\n"
    "s_nop 0\n"
    "global_load_lds_dwordx4 v25, s[30:31] \n"
    "s_add_u32 s30, s30, 8192\n"
    "s_addc_u32 s31, s31, 0\n"
    "s_add_u32 m0, s54, 6144\n"
    "s_nop 0\n"
    "global_load_lds_dwordx4 v25, s[30:31] \n"
    "s_add_u32 s30, s30, 8192\n"
    "s_addc_u32 s31, s31, 0\n"
    "s_add_u32 m0, s54, 7168\n"
    "s_nop 0\n"
    "global_load_lds_dwordx4 v25, s[30:31] \n"
    "ds_read_b64_tr_b16 v[32:33], v26 offset:8192\n"
    "ds_read_b64_tr_b16 v[34:35], v26 offset:8448\n"
    "ds_read_b64_tr_b16 v[36:37], v26 offset:12288\n"
    "ds_read_b64_tr_b16 v[38:39], v26 offset:12544\n"
    "ds_read_b64_tr_b16 v[40:41], v29 offset:8192\n"
    "ds_read_b64_tr_b16 v[42:43], v29 offset:8448\n"
    "ds_read_b64_tr_b16 v[44:45], v29 offset:12288\n"
    "ds_read_b64_tr_b16 v[46:47], v29 offset:12544\n"
    "ds_read_b64_tr_b16 a[24:25], v30 offset:8192\n"
    "ds_read_b64_tr_b16 a[26:27], v30 offset:8448\n"
    "ds_read_b64_tr_b16 a[28:29], v30 offset:12288\n"
    "ds_read_b64_tr_b16 a[30:31], v30 offset:12544\n"
    "ds_read_b64_tr_b16 a[64:65], v31 offset:8192\n"
    "ds_read_b64_tr_b16 a[66:67], v31 offset:8448\n"
    "ds_read_b64_tr_b16 a[68:69], v31 offset:12288\n"
    "ds_read_b64_tr_b16 a[70:71], v31 offset:12544\n"
    "s_waitcnt vmcnt(16) lgkmcnt(0)\n"
    "s_barrier\n"
    "s_add_u32 s30, s26, 65536\n"
    "s_addc_u32 s31, s27, 0\n"
    "s_add_u32 m0, s54, 8192\n"
    "s_nop 0\n"
    "global_load_lds_dwordx4 v25, s[30:31] \n"
    "s_add_u32 s30, s30, 8192\n"
    "s_addc_u32 s31, s31, 0\n"
    "s_add_u32 m0, s54, 9216\n"
    "s_nop 0\n"
    "global_load_lds_dwordx4 v25, s[30:31] \n"
    "s_add_u32 s30, s30, 8192\n"
    "s_addc_u32 s31, s31, 0\n"
    "s_add_u32 m0, s54, 10240\n"
    "s_nop 0\n"
    "global_load_lds_dwordx4 v25, s[30:31] \n"
    "s_add_u32 s30, s30, 8192\n"
    "s_addc_u32 s31, s31, 0\n"
    "s_add_u32 m0, s54, 11264\n"
    "s_nop 0\n"
    "global_load_lds_dwordx4 v25, s[30:31] \n"
    "s_add_u32 s30, s30, 8192\n"
    "s_addc_u32 s31, s31, 0\n"
    "s_add_u32 m0, s54, 12288\n"
    "s_nop 0\n"
    "global_load_lds_dwordx4 v25, s[30:31] \n"
    "s_add_u32 s30, s30, 8192\n"
    "s_addc_u32 s31, s31, 0\n"
    "s_add_u32 m0, s54, 13312\n"
    "s_nop 0\n"
    "global_load_lds_dwordx4 v25, s[30:31] \n"
    "s_add_u32 s30, s30, 8192\n"
    "s_addc_u32 s31, s31, 0\n"
    "s_add_u32 m0, s54, 14336\n"
    "s_nop 0\n"
    "global_load_lds_dwordx4 v25, s[30:31] \n"
    "s_add_u32 s30, s30, 8192\n"
    "s_addc_u32 s31, s31, 0\n"
    "s_add_u32 m0, s54, 15360\n"
    "s_nop 0\n"
    "global_load_lds_dwordx4 v25, s[30:31] \n"
    "ds_read_b64_tr_b16 v[48:49], v26 offset:16384\n"
    "ds_read_b64_tr_b16 v[50:51], v26 offset:16640\n"
    "ds_read_b64_tr_b16 v[52:53], v26 offset:20480\n"
    "ds_read_b64_tr_b16 v[54:55], v26 offset:20736\n"
    "ds_read_b64_tr_b16 v[88:89], v29 offset:16384\n"
    "ds_read_b64_tr_b16 v[90:91], v29 offset:16640\n"
    "ds_read_b64_tr_b16 v[92:93], v29 offset:20480\n"
    "ds_read_b64_tr_b16 v[94:95], v29 offset:20736\n"
    "ds_read_b64_tr_b16 a[32:33], v30 offset:16384\n"
    "ds_read_b64_tr_b16 a[34:35], v30 offset:16640\n"
    "ds_read_b64_tr_b16 a[36:37], v30 offset:20480\n"
    "ds_read_b64_tr_b16 a[38:39], v30 offset:20736\n"
    "ds_read_b64_tr_b16 a[72:73], v31 offset:16384\n"
    "ds_read_b64_tr_b16 a[74:75], v31 offset:16640\n"
    "ds_read_b64_tr_b16 a[76:77], v31 offset:20480\n"
    "ds_read_b64_tr_b16 a[78:79], v31 offset:20736\n"
    "s_waitcnt vmcnt(16) lgkmcnt(0)\n"
    "s_barrier\n"
    "s_add_u32 s30, s26, 128\n"
    "s_addc_u32 s31, s27, 0\n"
    "s_add_u32 m0, s54, 16384\n"
    "s_nop 0\n"
    "global_load_lds_dwordx4 v25, s[30:31] \n"
    "s_add_u32 s30, s30, 8192\n"
    "s_addc_u32 s31, s31, 0\n"
    "s_add_u32 m0, s54, 17408\n"
    "s_nop 0\n"
    "global_load_lds_dwordx4 v25, s[30:31] \n"
    "s_add_u32 s30, s30, 8192\n"
    "s_addc_u32 s31, s31, 0\n"
    "s_add_u32 m0, s54, 18432\n"
    "s_nop 0\n"
    "global_load_lds_dwordx4 v25, s[30:31] \n"
    "s_add_u32 s30, s30, 8192\n"
    "s_addc_u32 s31, s31, 0\n"
    "s_add_u32 m0, s54, 19456\n"
    "s_nop 0\n"
    "global_load_lds_dwordx4 v25, s[30:31] \n"
    "s_add_u32 s30, s30, 8192\n"
    "s_addc_u32 s31, s31, 0\n"
    "s_add_u32 m0, s54, 20480\n"
    "s_nop 0\n"
    "global_load_lds_dwordx4 v25, s[30:31] \n"
    "s_add_u32 s30, s30, 8192\n"
    "s_addc_u32 s31, s31, 0\n"
    "s_add_u32 m0, s54, 21504\n"
    "s_nop 0\n"
    "global_load_lds_dwordx4 v25, s[30:31] \n"
    "s_add_u32 s30, s30, 8192\n"
    "s_addc_u32 s31, s31, 0\n"
    "s_add_u32 m0, s54, 22528\n"
    "s_nop 0\n"
    "global_load_lds_dwordx4 v25, s[30:31] \n"
    "s_add_u32 s30, s30, 8192\n"
    "s_addc_u32 s31, s31, 0\n"
    "s_add_u32 m0, s54, 23552\n"
    "s_nop 0\n"
    "global_load_lds_dwordx4 v25, s[30:31] \n"
    "ds_read_b64_tr_b16 a[0:1], v26 offset:24576\n"
    "ds_read_b64_tr_b16 a[2:3], v26 offset:24832\n"
    "ds_read_b64_tr_b16 a[4:5], v26 offset:28672\n"
    "ds_read_b64_tr_b16 a[6:7], v26 offset:28928\n"
    "ds_read_b64_tr_b16 a[40:41], v29 offset:24576\n"
    "ds_read_b64_tr_b16 a[42:43], v29 offset:24832\n"
    "ds_read_b64_tr_b16 a[44:45], v29 offset:28672\n"
    "ds_read_b64_tr_b16 a[46:47], v29 offset:28928\n"
    "ds_read_b64_tr_b16 a[16:17], v30 offset:24576\n"
    "ds_read_b64_tr_b16 a[18:19], v30 offset:24832\n"
    "ds_read_b64_tr_b16 a[20:21], v30 offset:28672\n"
    "ds_read_b64_tr_b16 a[22:23], v30 offset:28928\n"
    "ds_read_b64_tr_b16 a[56:57], v31 offset:24576\n"
    "ds_read_b64_tr_b16 a[58:59], v31 offset:24832\n"
    "ds_read_b64_tr_b16 a[60:61], v31 offset:28672\n"
    "ds_read_b64_tr_b16 a[62:63], v31 offset:28928\n"
    "s_waitcnt vmcnt(16) lgkmcnt(0)\n"
    "s_barrier\n"
    "v_lshlrev_b32 v28, 3, v0\n"
    "v_add_u32 v30, 0x2000, v0\n"
    "buffer_load_dwordx2 v[28:29], v28, s[36:39], s79 offen sc1\n"
    "buffer_load_dwordx2 v[30:31], v30, s[36:39], s79 offen sc1\n"
    "s_add_u32 s30, s26, 65664\n"
    "s_addc_u32 s31, s27, 0\n"
    "s_add_u32 m0, s54, 24576\n"
    "s_nop 0\n"
    "global_load_lds_dwordx4 v25, s[30:31] \n"
    "s_add_u32 s30, s30, 8192\n"
    "s_addc_u32 s31, s31, 0\n"
    "s_add_u32 m0, s54, 25600\n"
    "s_nop 0\n"
    "global_load_lds_dwordx4 v25, s[30:31] \n"
    "s_add_u32 s30, s30, 8192\n"
    "s_addc_u32 s31, s31, 0\n"
    "s_add_u32 m0, s54, 26624\n"
    "s_nop 0\n"
    "global_load_lds_dwordx4 v25, s[30:31] \n"
    "s_add_u32 s30, s30, 8192\n"
    "s_addc_u32 s31, s31, 0\n"
    "s_add_u32 m0, s54, 27648\n"
    "s_nop 0\n"
    "global_load_lds_dwordx4 v25, s[30:31] \n"
    "s_add_u32 s30, s30, 8192\n"
    "s_addc_u32 s31, s31, 0\n"
    "s_add_u32 m0, s54, 28672\n"
    "s_nop 0\n"
    "global_load_lds_dwordx4 v25, s[30:31] \n"
    "s_add_u32 s30, s30, 8192\n"
    "s_addc_u32 s31, s31, 0\n"
    "s_add_u32 m0, s54, 29696\n"
    "s_nop 0\n"
    "global_load_lds_dwordx4 v25, s[30:31] \n"
    "s_add_u32 s30, s30, 8192\n"
    "s_addc_u32 s31, s31, 0\n"
    "s_add_u32 m0, s54, 30720\n"
    "s_nop 0\n"
    "global_load_lds_dwordx4 v25, s[30:31] \n"
    "s_add_u32 s30, s30, 8192\n"
    "s_addc_u32 s31, s31, 0\n"
    "s_add_u32 m0, s54, 31744\n"
    "s_nop 0\n"
    "global_load_lds_dwordx4 v25, s[30:31] \n"
    "ds_read_b64_tr_b16 v[160:161], v27 offset:0\n"
    "ds_read_b64_tr_b16 v[162:163], v27 offset:256\n"
    "ds_read_b64_tr_b16 v[164:165], v27 offset:4096\n"
    "ds_read_b64_tr_b16 v[166:167], v27 offset:4352\n"
    "ds_read_b64_tr_b16 v[176:177], v27 offset:8\n"
    "ds_read_b64_tr_b16 v[178:179], v27 offset:264\n"
    "ds_read_b64_tr_b16 v[180:181], v27 offset:4104\n"
    "ds_read_b64_tr_b16 v[182:183], v27 offset:4360\n"
    "ds_read_b64_tr_b16 v[192:193], v23 offset:0\n"
    "ds_read_b64_tr_b16 v[194:195], v23 offset:256\n"
    "ds_read_b64_tr_b16 v[196:197], v23 offset:4096\n"
    "ds_read_b64_tr_b16 v[198:199], v23 offset:4352\n"
    "ds_read_b64_tr_b16 v[208:209], v23 offset:8\n"
    "ds_read_b64_tr_b16 v[210:211], v23 offset:264\n"
    "ds_read_b64_tr_b16 v[212:213], v23 offset:4104\n"
    "ds_read_b64_tr_b16 v[214:215], v23 offset:4360\n"
    "s_mov_b32 s60, 0\n"
    "s_mov_b32 s61, 0xb000000\n"
    "s_mov_b32 s62, s51\n"
    "s_mov_b32 s64, 0\n"
    "s_mov_b32 s65, 0\n"
    "s_mov_b32 s68, 0x3e0293ee\n"
    "s_mov_b32 s69, 0x3e0293ee\n"
    "s_mov_b32 s58, 0x3f803f80\n"
    "s_mov_b32 s59, 0x3f803f80\n"
    "s_and_b32 s18, s57, 0xffff\n"
    "s_lshr_b32 s19, s57, 16\n"
    "s_lshr_b32 s11, s12, 16\n"
    "s_and_b32 s7, s4, 255\n"
    "s_and_b32 s10, s12, 0xffff\n"
    "s_sub_u32 s10, s10, s7\n"
    "s_add_u32 s7, s7, 88\n"
    "s_bfe_u32 s5, s4, 0x80008\n"
    "s_lshr_b32 s15, s4, 16\n"
    "s_mov_b32 s2, 0\n"
    "s_waitcnt vmcnt(18)\n"
    "ds_read_b64_tr_b16 v[168:169], v27 offset:8192\n"
    "ds_read_b64_tr_b16 v[170:171], v27 offset:8448\n"
    "ds_read_b64_tr_b16 v[172:173], v27 offset:12288\n"
    "ds_read_b64_tr_b16 v[174:175], v27 offset:12544\n"
    "ds_read_b64_tr_b16 v[184:185], v27 offset:8200\n"
    "ds_read_b64_tr_b16 v[186:187], v27 offset:8456\n"
    "ds_read_b64_tr_b16 v[188:189], v27 offset:12296\n"
    "ds_read_b64_tr_b16 v[190:191], v27 offset:12552\n"
    "ds_read_b64_tr_b16 v[200:201], v23 offset:8192\n"
    "ds_read_b64_tr_b16 v[202:203], v23 offset:8448\n"
    "ds_read_b64_tr_b16 v[204:205], v23 offset:12288\n"
    "ds_read_b64_tr_b16 v[206:207], v23 offset:12544\n"
    "ds_read_b64_tr_b16 v[216:217], v23 offset:8200\n"
    "ds_read_b64_tr_b16 v[218:219], v23 offset:8456\n"
    "ds_read_b64_tr_b16 v[220:221], v23 offset:12296\n"
    "ds_read_b64_tr_b16 v[222:223], v23 offset:12552\n"
    "s_waitcnt vmcnt(8)\n"
    "s_memrealtime s[8:9]\n"
    "s_branch L_R_GCHK%=\n"
    "L_R_GATE%=:\n"
    "s_sleep 10\n"
    "s_add_u32 s78, s78, 1\n"
    "s_cmp_lt_u32 s78, 0x10000\n"
    "s_cbranch_scc1 L_R_GLD%=\n"
    "s_endpgm\n"
    "L_R_GLD%=:\n"
    "v_lshlrev_b32 v28, 3, v0\n"
    "v_add_u32 v30, 0x2000, v0\n"
    "buffer_load_dwordx2 v[28:29], v28, s[36:39], s79 offen sc1\n"
    "buffer_load_dwordx2 v[30:31], v30, s[36:39], s79 offen sc1\n"
    "s_waitcnt vmcnt(0)\n"
    "L_R_GCHK%=:\n"
    "s_waitcnt lgkmcnt(0)\n"
    "s_add_u32 s76, s72, 0x7F4A7C15\n"
    "s_addc_u32 s77, s73, 0x9E3779B9\n"
    "s_andn2_b32 s77, s77, 255\n"
    "v_xor_b32 v28, s76, v28\n"
    "v_xor_b32 v30, s76, v30\n"
    "v_xor_b32 v29, s77, v29\n"
    "v_xor_b32 v31, s77, v31\n"
    "v_or_b32 v28, v28, v30\n"
    "v_or_b32 v30, v29, v31\n"
    "v_lshrrev_b32 v30, 8, v30\n"
    "v_or_b32 v28, v28, v30\n"
    "v_cmp_ne_u32 vcc, 0, v28\n"
    "s_cbranch_vccnz L_R_GATE%=\n"
    "v_readfirstlane_b32 s70, v29\n"
    "v_and_b32 v28, 8, v29\n"
    "v_cmp_ne_u32 vcc, 0, v28\n"
    "s_bcnt1_i32_b64 s76, vcc\n"
    "s_cmp_gt_u32 s76, 4\n"
    "s_cselect_b32 s77, 0, 4\n"
    "s_add_u32 s71, s70, s74\n"
    "s_xor_b32 s77, s77, s71\n"
    "s_and_b32 s77, s77, 4\n"
    "s_lshl_b32 s77, s77, 11\n"
    "s_and_b32 s76, s71, 3\n"
    "s_and_b32 s70, s75, 3\n"
    "s_and_b32 s75, s71, 7\n"
    "s_sub_i32 s70, s76, s70\n"
    "s_cmp_lg_u32 s70, 0\n"
    "s_cbranch_scc1 L_R_REDO%=\n"
    "s_lshl_b32 s70, s76, 10\n"
    "s_add_u32 s48, s48, s70\n"
    "s_xor_b32 s48, s48, s77\n"
    "s_xor_b32 s14, s14, s77\n"
    "s_add_u32 s70, s8, s7\n"
    "s_add_u32 s70, s70, s29\n"
    "s_bitset0_b32 s70, 31\n"
    "v_mov_b32 v28, s70\n"
    "s_cmp_eq_u32 s28, 0\n"
    "s_cselect_b32 s70, s14, s38\n"
    "buffer_store_dword v28, v19, s[36:39], s70 offen sc1\n"
    "ds_write_b128 v1, v[32:35] offset:8192\n"
    "ds_write_b128 v1, v[36:39] offset:9216\n"
    "ds_write_b128 v1, v[40:43] offset:10240\n"
    "ds_write_b128 v1, v[44:47] offset:11264\n"
    "ds_write_b128 v1, v[48:51] offset:12288\n"
    "ds_write_b128 v1, v[52:55] offset:13312\n"
    "ds_write_b128 v1, v[88:91] offset:14336\n"
    "ds_write_b128 v1, v[92:95] offset:15360\n"
    "ds_read_b64_tr_b16 v[224:225], v27 offset:16384\n"
    "ds_read_b64_tr_b16 v[226:227], v27 offset:16640\n"
    "ds_read_b64_tr_b16 v[228:229], v27 offset:20480\n"
    "ds_read_b64_tr_b16 v[230:231], v27 offset:20736\n"
    "ds_read_b64_tr_b16 v[240:241], v27 offset:16392\n"
    "ds_read_b64_tr_b16 v[242:243], v27 offset:16648\n"
    "ds_read_b64_tr_b16 v[244:245], v27 offset:20488\n"
    "ds_read_b64_tr_b16 v[246:247], v27 offset:20744\n"
    "ds_read_b64_tr_b16 v[32:33], v23 offset:16384\n"
    "ds_read_b64_tr_b16 v[34:35], v23 offset:16640\n"
    "ds_read_b64_tr_b16 v[36:37], v23 offset:20480\n"
    "ds_read_b64_tr_b16 v[38:39], v23 offset:20736\n"
    "ds_read_b64_tr_b16 v[40:41], v23 offset:16392\n"
    "ds_read_b64_tr_b16 v[42:43], v23 offset:16648\n"
    "ds_read_b64_tr_b16 v[44:45], v23 offset:20488\n"
    "ds_read_b64_tr_b16 v[46:47], v23 offset:20744\n"
    "s_lshl_b32 s70, s76, 17\n"
    "s_add_u32 s40, s40, s70\n"
    "s_lshl_b32 s70, s76, 14\n"
    "s_add_u32 s41, s41, s70\n"
    "s_lshl_b32 s70, s76, 11\n"
    "s_add_u32 s43, s43, s70\n"
    "s_lshl_b32 s70, s76, 17\n"
    "s_add_u32 s44, s44, s70\n"
    "s_lshl_b32 s70, s76, 15\n"
    "s_add_u32 s46, s46, s70\n"
    "s_lshl_b32 s70, s76, 14\n"
    "s_add_u32 s49, s49, s70\n"
    "s_xor_b32 s61, s61, s77\n"
    "s_xor_b32 s42, s42, s77\n"
    "s_xor_b32 s43, s43, s77\n"
    "s_xor_b32 s49, s49, s77\n"
    "s_xor_b32 s13, s13, s77\n"
    "s_xor_b32 s56, s56, s77\n"
    "s_add_u32 s43, s43, 0x4000\n"
    "s_lshr_b32 s52, s44, 8\n"
    "s_xor_b32 s52, s52, s56\n"
    "s_and_b32 s52, s52, 0x2000\n"
    "s_or_b32 s52, s52, s44\n"
    "s_add_u32 s45, s52, 0x2000000\n"
    "s_waitcnt vmcnt(1) lgkmcnt(0)\n"
    "buffer_load_dword v21, off, s[36:39], s48 sc1\n"
    "ds_write_b128 v1, v[96:99] offset:16384\n"
    "ds_write_b128 v1, v[100:103] offset:17408\n"
    "ds_write_b128 v1, v[104:107] offset:18432\n"
    "ds_write_b128 v1, v[108:111] offset:19456\n"
    "ds_write_b128 v1, v[112:115] offset:20480\n"
    "ds_write_b128 v1, v[116:119] offset:21504\n"
    "ds_write_b128 v1, v[120:123] offset:22528\n"
    "ds_write_b128 v1, v[124:127] offset:23552\n"
    "ds_read_b64_tr_b16 v[232:233], v27 offset:24576\n"
    "ds_read_b64_tr_b16 v[234:235], v27 offset:24832\n"
    "ds_read_b64_tr_b16 v[236:237], v27 offset:28672\n"
    "ds_read_b64_tr_b16 v[238:239], v27 offset:28928\n"
    "ds_read_b64_tr_b16 v[248:249], v27 offset:24584\n"
    "ds_read_b64_tr_b16 v[250:251], v27 offset:24840\n"
    "ds_read_b64_tr_b16 v[252:253], v27 offset:28680\n"
    "ds_read_b64_tr_b16 v[254:255], v27 offset:28936\n"
    "ds_read_b64_tr_b16 v[48:49], v23 offset:24576\n"
    "ds_read_b64_tr_b16 v[50:51], v23 offset:24832\n"
    "ds_read_b64_tr_b16 v[52:53], v23 offset:28672\n"
    "ds_read_b64_tr_b16 v[54:55], v23 offset:28928\n"
    "ds_read_b64_tr_b16 v[88:89], v23 offset:24584\n"
    "ds_read_b64_tr_b16 v[90:91], v23 offset:24840\n"
    "ds_read_b64_tr_b16 v[92:93], v23 offset:28680\n"
    "ds_read_b64_tr_b16 v[94:95], v23 offset:28936\n"
    "s_waitcnt vmcnt(0)\n"
    "v_readfirstlane_b32 s70, v21\n"
    "buffer_load_dwordx4 v[24:27], v0, s[36:39], s62 offen sc1\n"
    "buffer_load_dwordx4 v[28:31], v0, s[36:39], s62 offen offset:1024 sc1\n"
    "s_cmp_lg_u32 s70, 0\n"
    "s_cselect_b32 s63, 1, 0\n"
    "s_cselect_b32 s52, s52, s48\n"
    "s_cselect_b32 s45, s45, s51\n"
    "s_waitcnt lgkmcnt(15)\n"
    "buffer_load_dwordx4 v[112:115], v4, s[36:39], s52 offen sc1\n"
    "buffer_load_dwordx4 v[116:119], v4, s[36:39], s52 offen offset:128 sc1\n"
    "buffer_load_dwordx4 v[120:123], v5, s[36:39], s45 offen sc1\n"
    "buffer_load_dwordx4 v[124:127], v5, s[36:39], s45 offen offset:64 sc1\n"
    "ds_write_b128 v1, v[56:59] offset:0\n"
    "ds_write_b128 v1, v[60:63] offset:1024\n"
    "ds_write_b128 v1, v[64:67] offset:2048\n"
    "ds_write_b128 v1, v[68:71] offset:3072\n"
    "ds_write_b128 v1, v[72:75] offset:4096\n"
    "ds_write_b128 v1, v[76:79] offset:5120\n"
    "ds_write_b128 v1, v[80:83] offset:6144\n"
    "ds_write_b128 v1, v[84:87] offset:7168\n"
    "s_waitcnt lgkmcnt(8)\n"
    "ds_write_b128 v1, v[32:35] offset:24576\n"
    "ds_write_b128 v1, v[36:39] offset:25600\n"
    "ds_write_b128 v1, v[40:43] offset:26624\n"
    "ds_write_b128 v1, v[44:47] offset:27648\n"
    "ds_write_b128 v1, v[48:51] offset:28672\n"
    "ds_write_b128 v1, v[52:55] offset:29696\n"
    "ds_write_b128 v1, v[88:91] offset:30720\n"
    "ds_write_b128 v1, v[92:95] offset:31744\n"
    "s_waitcnt lgkmcnt(0)\n"
    "s_memrealtime s[8:9]\n"
    "v_mov_b64 v[88:89], 0\n"
    "v_mov_b64 v[90:91], 0\n"
    "v_mov_b64 v[92:93], 0\n"
    "v_mov_b64 v[94:95], 0\n"
    "v_mov_b64 v[96:97], 0\n"
    "v_mov_b64 v[98:99], 0\n"
    "v_mov_b64 v[100:101], 0\n"
    "v_mov_b64 v[102:103], 0\n"
    "v_mov_b64 v[104:105], 0\n"
    "v_mov_b64 v[106:107], 0\n"
    "v_mov_b64 v[108:109], 0\n"
    "v_mov_b64 v[110:111], 0\n"
    "s_waitcnt vmcnt(0) lgkmcnt(0)\n"
    "s_mov_b32 s6, s8\n"
    "s_add_u32 s70, s8, s7\n"
    "s_add_u32 s70, s70, 32\n"
    "s_bitset0_b32 s70, 31\n"
    "v_mov_b32 v21, s70\n"
    "s_add_u32 s70, s14, 0x1f80\n"
    "s_cmp_eq_u32 s28, 0\n"
    "s_cselect_b32 s70, s70, s38\n"
    "buffer_store_dword v21, v19, s[36:39], s70 offen sc1\n"
    "s_branch L_R_GOTB%=\n"
    "L_R_POLLB%=:\n"
    "buffer_load_dwordx4 v[24:27], v0, s[36:39], s62 offen sc1\n"
    "buffer_load_dwordx4 v[28:31], v0, s[36:39], s62 offen offset:1024 sc1\n"
    "s_add_u32 s43, s43, 0x4000\n"
    "s_add_u32 s61, s61, 0x80000\n"
    "s_and_b32 s70, s61, 0x180000\n"
    "s_cselect_b32 s70, 0, 0x2000\n"
    "s_xor_b32 s61, s61, s70\n"
    "s_add_u32 s44, s44, 0x80000\n"
    "s_lshr_b32 s52, s44, 8\n"
    "s_xor_b32 s52, s52, s56\n"
    "s_and_b32 s52, s52, 0x2000\n"
    "s_or_b32 s52, s52, s44\n"
    "s_add_u32 s45, s52, 0x2000000\n"
    "s_waitcnt vmcnt(3)\n"
    "v_readfirstlane_b32 s67, v23\n"
    "s_min_u32 s67, s67, 63\n"
    "s_cmp_gt_u32 s67, s60\n"
    "s_cselect_b32 s63, 1, 0\n"
    "s_cselect_b32 s52, s52, s48\n"
    "s_cselect_b32 s45, s45, s51\n"
    "s_cmp_eq_u32 s60, 63\n"
    "s_cselect_b32 s63, 1, s63\n"
    "s_cmp_eq_u32 s63, 0\n"
    "s_cselect_b32 s2, -1, s2\n"
    "s_sleep 3\n"
    "buffer_load_dwordx4 v[112:115], v4, s[36:39], s52 offen sc1\n"
    "buffer_load_dwordx4 v[116:119], v4, s[36:39], s52 offen offset:128 sc1\n"
    "buffer_load_dwordx4 v[120:123], v5, s[36:39], s45 offen sc1\n"
    "buffer_load_dwordx4 v[124:127], v5, s[36:39], s45 offen offset:64 sc1\n"
    "buffer_store_dword v22, v19, s[36:39], s49 offen sc1\n"
    "s_waitcnt vmcnt(6)\n"
    "L_R_GOTB%=:\n"
    "v_smfmac_f32_16x16x64_bf16 v[88:91], v[24:27], v[56:63], v12\n"
    "ds_write_b128 v13, v[24:27]\n"
    "v_max3_u32 v21, v24, v25, v26\n"
    "s_waitcnt vmcnt(5)\n"
    "v_smfmac_f32_16x16x64_bf16 v[92:95], v[24:27], v[64:71], v12\n"
    "ds_write_b128 v13, v[28:31] offset:1024\n"
    "v_max3_u32 v21, v21, v27, v28\n"
    "v_smfmac_f32_16x16x64_bf16 v[96:99], v[24:27], v[72:79], v12\n"
    "s_memrealtime s[8:9]\n"
    "v_max3_u32 v22, v29, v30, v31\n"
    "v_smfmac_f32_16x16x64_bf16 v[100:103], v[24:27], v[80:87], v12\n"
    "v_max_u32 v21, v21, v22\n"
    "v_smfmac_f32_16x16x64_bf16 v[88:91], v[28:31], a[0:7], v12\n"
    "v_cmp_eq_u32 vcc, -1, v21\n"
    "v_smfmac_f32_16x16x64_bf16 v[92:95], v[28:31], a[40:47], v12\n"
    "v_smfmac_f32_16x16x64_bf16 v[96:99], v[28:31], a[80:87], v12\n"
    "s_cbranch_vccnz L_R_FAILB%=\n"
    "L_R_CONTB%=:\n"
    "s_waitcnt lgkmcnt(0)\n"
    "s_barrier\n"
    "ds_read_b128 v[32:35], v14 offset:0\n"
    "v_smfmac_f32_16x16x64_bf16 v[100:103], v[28:31], a[120:127], v12\n"
    "ds_read_b128 v[36:39], v14 offset:1024\n"
    "s_sub_i32 s9, s8, s6\n"
    "v_smfmac_f32_16x16x64_bf16 v[104:107], v[24:27], a[160:167], v12\n"
    "ds_read_b128 v[40:43], v15 offset:0\n"
    "s_sub_i32 s70, s9, s10\n"
    "v_smfmac_f32_16x16x64_bf16 v[108:111], v[24:27], a[224:231], v12\n"
    "ds_read_b128 v[56:59], v1 offset:8192\n"
    "s_mov_b32 vcc_lo, 0xcccccccc\n"
    "v_smfmac_f32_16x16x64_bf16 v[104:107], v[28:31], a[168:175], v12\n"
    "ds_read_b128 v[60:63], v1 offset:9216\n"
    "s_mov_b32 vcc_hi, 0xcccccccc\n"
    "v_smfmac_f32_16x16x64_bf16 v[108:111], v[28:31], a[232:239], v12\n"
    "ds_read_b128 v[48:51], v16 offset:0\n"
    "s_waitcnt lgkmcnt(5)\n"
    "v_smfmac_f32_16x16x64_bf16 v[88:91], v[32:35], a[8:15], v12\n"
    "ds_read_b128 v[52:55], v16 offset:1024\n"
    "s_cmp_lt_u32 s70, s2\n"
    "v_smfmac_f32_16x16x64_bf16 v[92:95], v[32:35], a[48:55], v12\n"
    "ds_read_b128 v[72:75], v1 offset:12288\n"
    "s_waitcnt lgkmcnt(6)\n"
    "v_smfmac_f32_16x16x64_bf16 v[88:91], v[36:39], a[16:23], v12\n"
    "ds_read_b128 v[76:79], v1 offset:13312\n"
    "s_cbranch_scc1 L_R_ANN%=\n"
    "L_R_CONTN%=:\n"
    "v_smfmac_f32_16x16x64_bf16 v[92:95], v[36:39], a[56:63], v12\n"
    "ds_read_b128 v[44:47], v15 offset:1024\n"
    "s_waitcnt lgkmcnt(5)\n"
    "v_smfmac_f32_16x16x64_bf16 v[88:91], v[40:43], v[56:63], v12\n"
    "ds_read_b128 v[64:67], v1 offset:10240\n"
    "s_waitcnt lgkmcnt(5)\n"
    "v_smfmac_f32_16x16x64_bf16 v[88:91], v[48:51], a[24:31], v12\n"
    "ds_read_b128 v[68:71], v1 offset:11264\n"
    "s_waitcnt lgkmcnt(5)\n"
    "v_smfmac_f32_16x16x64_bf16 v[88:91], v[52:55], a[32:39], v12\n"
    "ds_read_b128 v[80:83], v1 offset:14336\n"
    "s_waitcnt lgkmcnt(3)\n"
    "v_smfmac_f32_16x16x64_bf16 v[88:91], v[44:47], v[72:79], v12\n"
    "ds_read_b128 v[84:87], v1 offset:15360\n"
    "v_smfmac_f32_16x16x64_bf16 v[92:95], v[48:51], a[64:71], v12\n"
    "ds_read_b128 v[56:59], v1 offset:16384\n"
    "s_waitcnt lgkmcnt(3)\n"
    "v_smfmac_f32_16x16x64_bf16 v[92:95], v[52:55], a[72:79], v12\n"
    "ds_read_b128 v[60:63], v1 offset:17408\n"
    "v_add_f32 v88, v88, v90\n"
    "v_smfmac_f32_16x16x64_bf16 v[92:95], v[40:43], v[64:71], v12\n"
    "s_waitcnt lgkmcnt(2)\n"
    "v_add_f32 v89, v89, v91\n"
    "v_smfmac_f32_16x16x64_bf16 v[92:95], v[44:47], v[80:87], v12\n"
    "ds_read_b128 v[72:75], v1 offset:20480\n"
    "v_cvt_pk_bf16_f32 v88, v88, v89\n"
    "v_smfmac_f32_16x16x64_bf16 v[96:99], v[32:35], a[88:95], v12\n"
    "ds_write_b32 v7, v88 offset:4\n"
    "v_smfmac_f32_16x16x64_bf16 v[96:99], v[44:47], a[96:103], v12\n"
    "ds_read_b128 v[76:79], v1 offset:21504\n"
    "v_add_f32 v92, v92, v94\n"
    "v_add_f32 v93, v93, v95\n"
    "v_cvt_pk_bf16_f32 v89, v92, v93\n"
    "v_smfmac_f32_16x16x64_bf16 v[96:99], v[48:51], a[104:111], v12\n"
    "ds_write_b32 v7, v89 offset:548\n"
    "ds_read_b64_tr_b16 v[92:93], v8 offset:0\n"
    "v_smfmac_f32_16x16x64_bf16 v[96:99], v[52:55], a[112:119], v12\n"
    "ds_read_b64_tr_b16 v[94:95], v8 offset:272\n"
    "s_waitcnt lgkmcnt(6)\n"
    "v_smfmac_f32_16x16x64_bf16 v[96:99], v[36:39], v[56:63], v12\n"
    "ds_read_b128 v[64:67], v1 offset:18432\n"
    "s_waitcnt lgkmcnt(4)\n"
    "v_smfmac_f32_16x16x64_bf16 v[96:99], v[40:43], v[72:79], v12\n"
    "ds_read_b128 v[68:71], v1 offset:19456\n"
    "v_smfmac_f32_16x16x64_bf16 v[100:103], v[32:35], a[128:135], v12\n"
    "ds_read_b128 v[80:83], v1 offset:22528\n"
    "s_cmp_eq_u32 s63, 0\n"
    "v_smfmac_f32_16x16x64_bf16 v[100:103], v[44:47], a[136:143], v12\n"
    "ds_read_b128 v[84:87], v1 offset:23552\n"
    "v_add_f32 v96, v96, v98\n"
    "v_smfmac_f32_16x16x64_bf16 v[100:103], v[48:51], a[144:151], v12\n"
    "s_waitcnt vmcnt(3)\n"
    "s_cbranch_scc1 L_R_LATEKV%=\n"
    "L_R_CONTK%=:\n"
    "s_waitcnt lgkmcnt(4)\n"
    "v_mfma_f32_16x16x32_bf16 v[24:27], v[112:115], v[92:95], 0\n"
    "v_add_f32 v97, v97, v99\n"
    "v_cvt_pk_bf16_f32 v96, v96, v97\n"
    "v_mfma_f32_16x16x32_bf16 v[28:31], v[116:119], v[92:95], 0\n"
    "ds_write_b32 v7, v96 offset:0\n"
    "v_smfmac_f32_16x16x64_bf16 v[100:103], v[52:55], a[152:159], v12\n"
    "s_waitcnt lgkmcnt(1)\n"
    "v_smfmac_f32_16x16x64_bf16 v[100:103], v[36:39], v[64:71], v12\n"
    "v_cndmask_b32_dpp v24, v28, v24, vcc quad_perm:[2,3,0,1] row_mask:0xf bank_mask:0xf\n"
    "v_cndmask_b32_dpp v25, v29, v25, vcc quad_perm:[2,3,0,1] row_mask:0xf bank_mask:0xf\n"
    "v_smfmac_f32_16x16x64_bf16 v[100:103], v[40:43], v[80:87], v12\n"
    "ds_write_b64 v13, v[24:25] offset:8192\n"
    "v_cndmask_b32_dpp v26, v30, v26, vcc quad_perm:[2,3,0,1] row_mask:0xf bank_mask:0xf\n"
    "v_cndmask_b32_dpp v27, v31, v27, vcc quad_perm:[2,3,0,1] row_mask:0xf bank_mask:0xf\n"
    "v_smfmac_f32_16x16x64_bf16 v[104:107], v[32:35], a[176:183], v12\n"
    "ds_write_b64 v13, v[26:27] offset:8200\n"
    "v_mov_b32 v98, 0\n"
    "v_add_f32 v100, v100, v102\n"
    "v_add_f32 v101, v101, v103\n"
    "v_cvt_pk_bf16_f32 v97, v100, v101\n"
    "v_smfmac_f32_16x16x64_bf16 v[108:111], v[32:35], a[240:247], v12\n"
    "ds_write_b32 v7, v97 offset:544\n"
    "v_smfmac_f32_16x16x64_bf16 v[104:107], v[36:39], a[184:191], v12\n"
    "v_mov_b64 v[96:97], 0\n"
    "v_smfmac_f32_16x16x64_bf16 v[108:111], v[36:39], a[248:255], v12\n"
    "v_mov_b64 v[32:33], s[58:59]\n"
    "v_smfmac_f32_16x16x64_bf16 v[104:107], v[40:43], a[192:199], v12\n"
    "v_mov_b64 v[34:35], s[58:59]\n"
    "v_smfmac_f32_16x16x64_bf16 v[108:111], v[40:43], v[128:135], v12\n"
    "s_waitcnt lgkmcnt(0)\n"
    "s_barrier\n"
    "ds_read_b64_tr_b16 v[56:57], v9 offset:0\n"
    "ds_read_b64_tr_b16 v[58:59], v9 offset:272\n"
    "v_smfmac_f32_16x16x64_bf16 v[104:107], v[44:47], a[200:207], v12\n"
    "ds_read_b64_tr_b16 v[60:61], v9 offset:2048\n"
    "ds_read_b64_tr_b16 v[62:63], v9 offset:2320\n"
    "v_smfmac_f32_16x16x64_bf16 v[108:111], v[44:47], v[136:143], v12\n"
    "ds_read_b128 v[72:75], v14 offset:8192\n"
    "ds_read_b64_tr_b16 v[64:65], v9 offset:4096\n"
    "v_smfmac_f32_16x16x64_bf16 v[104:107], v[48:51], a[208:215], v12\n"
    "ds_read_b64_tr_b16 v[66:67], v9 offset:4368\n"
    "ds_read_b64_tr_b16 v[68:69], v9 offset:6144\n"
    "s_waitcnt lgkmcnt(6)\n"
    "v_mfma_f32_16x16x32_bf16 v[28:31], v[56:59], v[56:59], 0\n"
    "ds_read_b64_tr_b16 v[70:71], v9 offset:6416\n"
    "s_waitcnt lgkmcnt(5)\n"
    "v_mfma_f32_16x16x32_bf16 v[28:31], v[60:63], v[60:63], v[28:31]\n"
    "ds_read_b128 v[76:79], v15 offset:8192\n"
    "s_waitcnt lgkmcnt(5)\n"
    "v_smfmac_f32_16x16x64_bf16 v[108:111], v[48:51], v[144:151], v12\n"
    "ds_read_b128 v[80:83], v16 offset:8192\n"
    "v_add_f32 v24, v24, v72\n"
    "s_waitcnt lgkmcnt(4)\n"
    "v_mfma_f32_16x16x32_bf16 v[28:31], v[64:67], v[64:67], v[28:31]\n"
    "s_waitcnt lgkmcnt(2)\n"
    "v_add_f32 v25, v25, v73\n"
    "v_add_f32 v26, v26, v74\n"
    "v_mfma_f32_16x16x32_bf16 v[28:31], v[68:71], v[68:71], v[28:31]\n"
    "s_waitcnt lgkmcnt(1)\n"
    "v_add_f32 v27, v27, v75\n"
    "v_add_f32 v24, v24, v76\n"
    "v_add_f32 v25, v25, v77\n"
    "v_add_f32 v26, v26, v78\n"
    "v_add_f32 v27, v27, v79\n"
    "s_waitcnt lgkmcnt(0)\n"
    "v_add_f32 v24, v24, v80\n"
    "v_add_f32 v25, v25, v81\n"
    "v_add_f32 v26, v26, v82\n"
    "v_add_f32 v27, v27, v83\n"
    "v_max3_f32 v84, v24, v25, v26\n"
    "v_max_f32 v84, v84, v27\n"
    "v_max_f32 v85, v28, v29\n"
    "s_waitcnt vmcnt(1)\n"
    "v_max_f32_dpp v84, v84, v84 quad_perm:[2,3,0,1] row_mask:0xf bank_mask:0xf\n"
    "v_max_f32_dpp v30, v85, v84 quad_perm:[2,3,2,3] row_mask:0xf bank_mask:0xf\n"
    "v_max_f32_dpp v31, v85, v84 quad_perm:[2,3,2,3] row_mask:0xf bank_mask:0xf\n"
    "v_smfmac_f32_16x16x64_bf16 v[104:107], v[52:55], a[216:223], v12\n"
    "ds_read_b128 v[60:63], v1 offset:25600\n"
    "v_permlane16_swap_b32 v30, v31\n"
    "v_max_f32 v84, v30, v31\n"
    "v_max_f32 v31, v30, v31\n"
    "v_smfmac_f32_16x16x64_bf16 v[108:111], v[52:55], v[152:159], v12\n"
    "ds_read_b128 v[64:67], v1 offset:26624\n"
    "v_permlane32_swap_b32 v84, v31\n"
    "v_max_f32 v30, v84, v31\n"
    "v_mul_f32 v30, 0xbe0293ee, v30\n"
    "v_pk_fma_f32 v[24:25], v[24:25], s[68:69], v[30:31] op_sel_hi:[1,1,0]\n"
    "v_pk_fma_f32 v[26:27], v[26:27], s[68:69], v[30:31] op_sel_hi:[1,1,0]\n"
    "v_pk_fma_f32 v[28:29], v[28:29], s[68:69], v[30:31] op_sel_hi:[1,1,0]\n"
    "v_exp_f32 v24, v24\n"
    "v_exp_f32 v25, v25\n"
    "v_exp_f32 v26, v26\n"
    "v_exp_f32 v27, v27\n"
    "v_cvt_pk_bf16_f32 v88, v24, v25\n"
    "v_cvt_pk_bf16_f32 v89, v26, v27\n"
    "v_exp_f32 v28, v28\n"
    "v_mov_b32_dpp v90, v88 quad_perm:[2,3,0,1] row_mask:0xf bank_mask:0xf\n"
    "v_mov_b32_dpp v91, v89 quad_perm:[2,3,0,1] row_mask:0xf bank_mask:0xf\n"
    "v_exp_f32 v29, v29\n"
    "v_add_f32 v104, v104, v106\n"
    "v_mfma_f32_16x16x32_bf16 v[112:115], v[120:123], v[88:91], 0\n"
    "s_cmp_eq_u32 s60, 63\n"
    "v_cvt_pk_bf16_f32 v96, v28, v29\n"
    "v_add_f32 v105, v105, v107\n"
    "v_mfma_f32_16x16x32_bf16 v[116:119], v[124:127], v[88:91], 0\n"
    "s_cbranch_scc1 L_R_TAIL%=\n"
    "v_add_f32 v108, v108, v110\n"
    "v_add_f32 v109, v109, v111\n"
    "v_cvt_pk_bf16_f32 v104, v104, v105\n"
    "v_cvt_pk_bf16_f32 v105, v108, v109\n"
    "v_add_f32 v100, v24, v25\n"
    "v_add_f32 v101, v26, v27\n"
    "v_mfma_f32_16x16x16_bf16 v[112:115], v[104:105], v[96:97], v[112:115]\n"
    "v_cvt_pk_bf16_f32 v99, v28, v29\n"
    "v_add_f32 v100, v100, v101\n"
    "v_add_f32 v102, v28, v29\n"
    "v_mfma_f32_16x16x16_bf16 v[116:119], v[104:105], v[98:99], v[116:119]\n"
    "v_add_f32_dpp v100, v100, v100 quad_perm:[2,3,0,1] row_mask:0xf bank_mask:0xf\n"
    "v_add_f32_dpp v101, v102, v100 quad_perm:[2,3,2,3] row_mask:0xf bank_mask:0xf\n"
    "v_add_f32_dpp v100, v102, v100 quad_perm:[2,3,2,3] row_mask:0xf bank_mask:0xf\n"
    "v_mov_b64 v[24:25], 0\n"
    "v_mov_b64 v[26:27], 0\n"
    "v_permlane16_swap_b32 v100, v101\n"
    "v_add_f32 v102, v100, v101\n"
    "v_add_f32 v100, v100, v101\n"
    "v_cndmask_b32_dpp v112, v116, v112, vcc quad_perm:[2,3,0,1] row_mask:0xf bank_mask:0xf\n"
    "v_cndmask_b32_dpp v113, v117, v113, vcc quad_perm:[2,3,0,1] row_mask:0xf bank_mask:0xf\n"
    "v_permlane32_swap_b32 v100, v102\n"
    "v_add_f32 v100, v100, v102\n"
    "v_rcp_f32 v100, v100\n"
    "v_cndmask_b32_dpp v114, v118, v114, vcc quad_perm:[2,3,0,1] row_mask:0xf bank_mask:0xf\n"
    "v_cndmask_b32_dpp v115, v119, v115, vcc quad_perm:[2,3,0,1] row_mask:0xf bank_mask:0xf\n"
    "v_pk_mul_f32 v[112:113], v[112:113], v[100:101] op_sel_hi:[1,0]\n"
    "v_pk_mul_f32 v[114:115], v[114:115], v[100:101] op_sel_hi:[1,0]\n"
    "v_cvt_pk_bf16_f32 v112, v112, v113\n"
    "v_cvt_pk_bf16_f32 v113, v114, v115\n"
    "ds_write_b64 v10, v[112:113]\n"
    "v_mov_b64 v[28:29], 0\n"
    "v_mov_b64 v[30:31], 0\n"
    "s_add_u32 s66, s40, s61\n"
    "v_mov_b64 v[32:33], 0\n"
    "s_add_u32 s71, s66, 0x1000\n"
    "v_mov_b64 v[34:35], 0\n"
    "v_mov_b64 v[36:37], 0\n"
    "v_mov_b64 v[38:39], 0\n"
    "s_waitcnt lgkmcnt(0)\n"
    "v_mov_b64 v[40:41], 0\n"
    "s_barrier\n"
    "ds_read_b128 v[88:91], v11\n"
    "v_mov_b64 v[42:43], 0\n"
    "ds_read_b128 v[92:95], v11 offset:1024\n"
    "v_mov_b64 v[44:45], 0\n"
    "ds_read_b128 v[56:59], v1 offset:24576\n"
    "v_mov_b64 v[46:47], 0\n"
    "s_nop 1\n"
    "v_mov_b64 v[48:49], 0\n"
    "s_waitcnt lgkmcnt(2)\n"
    "v_smfmac_f32_16x16x64_bf16 v[24:27], v[88:91], v[160:167], v12\n"
    "ds_read_b128 v[72:75], v1 offset:28672\n"
    "v_mov_b64 v[50:51], 0\n"
    "s_waitcnt lgkmcnt(2)\n"
    "v_smfmac_f32_16x16x64_bf16 v[24:27], v[92:95], v[168:175], v12\n"
    "ds_read_b128 v[76:79], v1 offset:29696\n"
    "v_mov_b64 v[52:53], 0\n"
    "v_smfmac_f32_16x16x64_bf16 v[28:31], v[88:91], v[176:183], v12\n"
    "ds_read_b128 v[68:71], v1 offset:27648\n"
    "v_mov_b64 v[54:55], 0\n"
    "v_smfmac_f32_16x16x64_bf16 v[28:31], v[92:95], v[184:191], v12\n"
    "ds_read_b128 v[80:83], v1 offset:30720\n"
    "v_add_f32 v24, v24, v26\n"
    "v_smfmac_f32_16x16x64_bf16 v[32:35], v[88:91], v[192:199], v12\n"
    "ds_read_b128 v[84:87], v1 offset:31744\n"
    "v_add_f32 v25, v25, v27\n"
    "v_smfmac_f32_16x16x64_bf16 v[32:35], v[92:95], v[200:207], v12\n"
    "v_add_f32 v28, v28, v30\n"
    "v_cvt_pk_bf16_f32 v96, v24, v28\n"
    "v_smfmac_f32_16x16x64_bf16 v[36:39], v[88:91], v[208:215], v12\n"
    "v_add_f32 v29, v29, v31\n"
    "v_cvt_pk_bf16_f32 v97, v25, v29\n"
    "v_smfmac_f32_16x16x64_bf16 v[36:39], v[92:95], v[216:223], v12\n"
    "ds_read_b64_tr_b16 v[112:113], v8 offset:0\n"
    "v_add_f32 v32, v32, v34\n"
    "v_smfmac_f32_16x16x64_bf16 v[40:43], v[88:91], v[224:231], v12\n"
    "ds_read_b64_tr_b16 v[114:115], v8 offset:272\n"
    "v_add_f32 v33, v33, v35\n"
    "v_smfmac_f32_16x16x64_bf16 v[40:43], v[92:95], v[232:239], v12\n"
    "v_add_f32 v36, v36, v38\n"
    "v_cvt_pk_bf16_f32 v98, v32, v36\n"
    "v_smfmac_f32_16x16x64_bf16 v[44:47], v[88:91], v[240:247], v12\n"
    "v_add_f32 v37, v37, v39\n"
    "v_cvt_pk_bf16_f32 v99, v33, v37\n"
    "v_smfmac_f32_16x16x64_bf16 v[44:47], v[92:95], v[248:255], v12\n"
    "s_waitcnt lgkmcnt(0)\n"
    "buffer_store_dwordx4 v[96:99], v2, s[36:39], s66 offen sc1\n"
    "v_add_f32 v40, v40, v42\n"
    "v_smfmac_f32_16x16x64_bf16 v[48:51], v[88:91], v[56:63], v12\n"
    "v_add_f32 v41, v41, v43\n"
    "v_smfmac_f32_16x16x64_bf16 v[48:51], v[92:95], v[72:79], v12\n"
    "v_add_f32 v44, v44, v46\n"
    "v_smfmac_f32_16x16x64_bf16 v[52:55], v[88:91], v[64:71], v12\n"
    "s_load_dwordx8 s[28:35], s[36:37], s13\n"
    "v_add_f32 v45, v45, v47\n"
    "v_smfmac_f32_16x16x64_bf16 v[52:55], v[92:95], v[80:87], v12\n"
    "v_cvt_pk_bf16_f32 v100, v40, v44\n"
    "v_cvt_pk_bf16_f32 v101, v41, v45\n"
    "v_add_f32 v48, v48, v50\n"
    "v_add_f32 v49, v49, v51\n"
    "s_add_u32 s60, s60, 1\n"
    "s_lshr_b32 s53, s46, 8\n"
    "v_add_f32 v52, v52, v54\n"
    "v_add_f32 v53, v53, v55\n"
    "v_cvt_pk_bf16_f32 v102, v48, v52\n"
    "v_cvt_pk_bf16_f32 v103, v49, v53\n"
    "buffer_store_dwordx4 v[100:103], v2, s[36:39], s71 offen sc1\n"
    "s_memrealtime s[96:97]\n"
    "s_sleep 1\n"
    "s_lshr_b32 s53, s46, 8\n"
    "s_xor_b32 s53, s53, s56\n"
    "s_and_b32 s53, s53, 0x2000\n"
    "s_or_b32 s53, s53, s46\n"
    "s_add_u32 s47, s53, 0x800000\n"
    "s_add_u32 s46, s46, 0x20000\n"
    "buffer_store_dwordx4 v[112:115], v6, s[36:39], s53 offen sc1\n"
    "buffer_store_dword v104, v20, s[36:39], s47 offen sc1\n"
    "buffer_store_dword v105, v20, s[36:39], s47 offen offset:256 sc1\n"
    "s_add_u32 s20, s41, s61\n"
    "s_xor_b32 s24, s20, 0x40000\n"
    "s_max_i32 s8, s9, s10\n"
    "s_add_u32 s6, s6, s8\n"
    "s_add_u32 s6, s6, s7\n"
    "s_waitcnt lgkmcnt(0)\n"
    "s_sub_i32 s70, s6, s96\n"
    "s_max_i32 s28, s28, s29\n"
    "s_max_i32 s28, s28, s30\n"
    "s_max_i32 s28, s28, s31\n"
    "s_max_i32 s28, s28, s32\n"
    "s_max_i32 s28, s28, s33\n"
    "s_max_i32 s28, s28, s34\n"
    "s_add_u32 s13, s13, 128\n"
    "s_add_u32 s14, s14, 128\n"
    "s_sub_i32 s3, s28, s96\n"
    "s_bfe_i32 s3, s3, 0x1f0000\n"
    "s_add_u32 s16, s3, 100\n"
    "s_cmp_lt_u32 s16, 300\n"
    "s_cselect_b32 s70, s3, s70\n"
    "s_and_b32 s3, s35, 255\n"
    "s_lshr_b32 s16, s35, 8\n"
    "s_cmp_lt_u32 s16, 256\n"
    "s_cselect_b32 s7, s3, s7\n"
    "s_cselect_b32 s11, s16, s11\n"
    "s_add_u32 s6, s96, s70\n"
    "s_add_u32 s6, s6, s11\n"
    "s_mov_b32 s2, s15\n"
    "s_max_i32 s70, s70, s18\n"
    "s_add_u32 s16, s96, s70\n"
    "s_cmp_eq_u32 s60, 1\n"
    "s_cbranch_scc1 L_R_RE%=\n"
    "L_R_REX%=:\n"
    "v_mov_b32 v22, s60\n"
    "L_R_WA%=:\n"
    "s_memrealtime s[98:99]\n"
    "s_waitcnt lgkmcnt(0)\n"
    "s_sub_i32 s70, s98, s16\n"
    "s_cmp_lt_i32 s70, 0\n"
    "s_cbranch_scc1 L_R_WA%=\n"
    "L_R_POLLA%=:\n"
    "buffer_load_dwordx4 v[24:27], v3, s[36:39], s20 offen sc1\n"
    "buffer_load_dwordx4 v[28:31], v3, s[36:39], s24 offen sc1\n"
    "v_bfe_u32 v21, v3, 6, 2\n"
    "v_cmp_eq_u32 vcc, 0, v21\n"
    "s_nop 1\n"
    "v_cndmask_b32_e64 v32, 0, v17, vcc\n"
    "v_cmp_eq_u32 vcc, 1, v21\n"
    "s_nop 1\n"
    "v_cndmask_b32_e64 v33, 0, v17, vcc\n"
    "v_cmp_eq_u32 vcc, 2, v21\n"
    "s_nop 1\n"
    "v_cndmask_b32_e64 v34, 0, v17, vcc\n"
    "v_cmp_eq_u32 vcc, 3, v21\n"
    "s_nop 1\n"
    "v_cndmask_b32_e64 v35, 0, v17, vcc\n"
    "v_mov_b64 v[88:89], 0\n"
    "v_mov_b64 v[90:91], 0\n"
    "v_mov_b64 v[92:93], 0\n"
    "v_mov_b64 v[94:95], 0\n"
    "v_mov_b64 v[96:97], 0\n"
    "v_mov_b64 v[98:99], 0\n"
    "v_mov_b64 v[100:101], 0\n"
    "v_mov_b64 v[102:103], 0\n"
    "v_mov_b64 v[104:105], 0\n"
    "v_mov_b64 v[106:107], 0\n"
    "v_mov_b64 v[108:109], 0\n"
    "v_mov_b64 v[110:111], 0\n"
    "ds_read_b128 v[56:59], v1 offset:0\n"
    "ds_read_b128 v[60:63], v1 offset:1024\n"
    "ds_read_b128 v[64:67], v1 offset:2048\n"
    "ds_read_b128 v[68:71], v1 offset:3072\n"
    "ds_read_b128 v[72:75], v1 offset:4096\n"
    "ds_read_b128 v[76:79], v1 offset:5120\n"
    "ds_read_b128 v[80:83], v1 offset:6144\n"
    "ds_read_b128 v[84:87], v1 offset:7168\n"
    "buffer_load_dword v23, off, s[36:39], s48 sc1\n"
    "s_cmp_ge_u32 s60, 63\n"
    "s_cbranch_scc1 L_R_EG%=\n"
    "L_R_EGX%=:\n"
    "s_add_u32 s42, s42, 0x4000\n"
    "s_mov_b32 s62, s42\n"
    "s_waitcnt vmcnt(2)\n"
    "L_R_GOTA%=:\n"
    "v_mfma_f32_16x16x32_bf16 v[36:39], v[32:35], v[24:27], 0\n"
    "s_waitcnt vmcnt(1)\n"
    "v_mfma_f32_16x16x32_bf16 v[36:39], v[32:35], v[28:31], v[36:39]\n"
    "s_nop 7\n"
    "v_add_f32_dpp v36, v36, v36 row_ror:4 row_mask:0xf bank_mask:0xf\n"
    "v_add_f32_dpp v37, v37, v37 row_ror:4 row_mask:0xf bank_mask:0xf\n"
    "s_nop 0\n"
    "v_add_f32_dpp v36, v36, v36 row_ror:8 row_mask:0xf bank_mask:0xf\n"
    "v_add_f32_dpp v37, v37, v37 row_ror:8 row_mask:0xf bank_mask:0xf\n"
    "v_cvt_pk_bf16_f32 v40, v36, v37\n"
    "v_cmpx_o_f32 vcc, v36, v36\n"
    "buffer_store_dword v40, v18, s[36:39], s43 offen sc1\n"
    "s_mov_b64 exec, -1\n"
    "s_cmp_eq_u64 vcc, -1\n"
    "s_cbranch_scc0 L_R_FAILA%=\n"
    "L_R_CONTA%=:\n"
    "s_memrealtime s[96:97]\n"
    "s_waitcnt lgkmcnt(0)\n"
    "s_sub_i32 s70, s6, s96\n"
    "s_add_i32 s3, s70, s5\n"
    "s_cmp_gt_i32 s3, s19\n"
    "s_cselect_b32 s2, s2, 0\n"
    "s_max_i32 s70, s70, s19\n"
    "s_add_u32 s17, s96, s70\n"
    "L_R_WB%=:\n"
    "s_memrealtime s[98:99]\n"
    "s_waitcnt lgkmcnt(0)\n"
    "s_sub_i32 s70, s98, s17\n"
    "s_cmp_lt_i32 s70, 0\n"
    "s_cbranch_scc1 L_R_WB%=\n"
    "s_branch L_R_POLLB%=\n"
    "L_R_ANN%=:\n"
    "s_cmp_eq_u32 s63, 0\n"
    "s_cbranch_scc1 L_R_MID%=\n"
    "s_add_u32 s10, s10, 2\n"
    "s_add_u32 s70, s8, s7\n"
    "s_bitset0_b32 s70, 31\n"
    "v_mov_b32 v21, s70\n"
    "buffer_store_dword v21, v19, s[36:39], s14 offen sc1\n"
    "s_branch L_R_CONTN%=\n"
    "L_R_MID%=:\n"
    "s_waitcnt vmcnt(4)\n"
    "v_readfirstlane_b32 s70, v112\n"
    "s_cmp_gt_u32 s70, s60\n"
    "s_cbranch_scc0 L_R_CONTN%=\n"
    "s_lshr_b32 s52, s44, 8\n"
    "s_xor_b32 s52, s52, s56\n"
    "s_and_b32 s52, s52, 0x2000\n"
    "s_or_b32 s52, s52, s44\n"
    "s_add_u32 s45, s52, 0x2000000\n"
    "s_mov_b32 s63, 1\n"
    "buffer_load_dwordx4 v[112:115], v4, s[36:39], s52 offen sc1\n"
    "buffer_load_dwordx4 v[116:119], v4, s[36:39], s52 offen offset:128 sc1\n"
    "buffer_load_dwordx4 v[120:123], v5, s[36:39], s45 offen sc1\n"
    "buffer_load_dwordx4 v[124:127], v5, s[36:39], s45 offen offset:64 sc1\n"
    "s_add_u32 s9, s9, 34\n"
    "s_add_u32 s70, s9, s6\n"
    "s_add_u32 s70, s70, s7\n"
    "s_bitset0_b32 s70, 31\n"
    "v_mov_b32 v21, s70\n"
    "s_and_b32 s70, s51, 0x1800\n"
    "s_cselect_b32 s70, s38, s14\n"
    "buffer_store_dword v21, v19, s[36:39], s70 offen sc1\n"
    "s_branch L_R_CONTN%=\n"
    "L_R_EG%=:\n"
    "s_waitcnt vmcnt(3)\n"
    "buffer_store_dword v22, v19, s[36:39], s49 offen sc1\n"
    "s_waitcnt vmcnt(1)\n"
    "s_branch L_R_EGX%=\n"
    "L_R_RE%=:\n"
    "s_sub_u32 s17, s16, 32\n"
    "L_R_WR%=:\n"
    "s_memrealtime s[98:99]\n"
    "s_waitcnt lgkmcnt(0)\n"
    "s_sub_i32 s70, s98, s17\n"
    "s_cmp_lt_i32 s70, 0\n"
    "s_cbranch_scc1 L_R_WR%=\n"
    "s_add_u32 s70, s13, 0x1f00\n"
    "s_load_dwordx8 s[28:35], s[36:37], s70\n"
    "s_waitcnt lgkmcnt(0)\n"
    "s_max_i32 s28, s28, s29\n"
    "s_max_i32 s28, s28, s30\n"
    "s_max_i32 s28, s28, s31\n"
    "s_max_i32 s28, s28, s32\n"
    "s_max_i32 s28, s28, s33\n"
    "s_sub_i32 s3, s28, s16\n"
    "s_bitset0_b32 s3, 31\n"
    "s_cmp_lt_u32 s3, 200\n"
    "s_cselect_b32 s3, s3, 0\n"
    "s_add_u32 s16, s16, s3\n"
    "s_add_u32 s6, s16, s11\n"
    "s_branch L_R_REX%=\n"
    "L_R_FAILB%=:\n"
    "s_nop 7\n"
    "s_cmp_lg_u32 s65, 0\n"
    "s_cbranch_scc1 L_R_CONTB%=\n"
    "buffer_load_dwordx4 v[24:27], v0, s[36:39], s62 offen sc1\n"
    "buffer_load_dwordx4 v[28:31], v0, s[36:39], s62 offen offset:1024 sc1\n"
    "v_mov_b64 v[88:89], 0\n"
    "v_mov_b64 v[90:91], 0\n"
    "v_mov_b64 v[92:93], 0\n"
    "v_mov_b64 v[94:95], 0\n"
    "v_mov_b64 v[96:97], 0\n"
    "v_mov_b64 v[98:99], 0\n"
    "v_mov_b64 v[100:101], 0\n"
    "v_mov_b64 v[102:103], 0\n"
    "s_sub_u32 s2, s63, 1\n"
    "s_add_u32 s64, s64, 1\n"
    "s_and_b32 s70, s64, 255\n"
    "s_cbranch_scc1 L_R_REB%=\n"
    "buffer_load_dword v21, off, s[36:39], s50 sc1\n"
    "s_waitcnt vmcnt(0)\n"
    "v_readfirstlane_b32 s65, v21\n"
    "s_cmp_lt_u32 s64, 0x40000\n"
    "s_cbranch_scc1 L_R_REB%=\n"
    "s_mov_b32 s65, 1\n"
    "v_mov_b32 v21, 1\n"
    "buffer_store_dword v21, off, s[36:39], s50 sc1\n"
    "s_branch L_R_REB%=\n"
    "L_R_REB%=:\n"
    "s_waitcnt vmcnt(0)\n"
    "s_branch L_R_GOTB%=\n"
    "L_R_FAILA%=:\n"
    "s_nop 7\n"
    "s_cmp_lg_u32 s65, 0\n"
    "s_cbranch_scc1 L_R_FORCEA%=\n"
    "v_max3_u32 v21, v24, v25, v26\n"
    "v_max3_u32 v21, v21, v27, v28\n"
    "v_max3_u32 v21, v21, v29, v30\n"
    "v_max_u32 v21, v21, v31\n"
    "v_cmp_eq_u32 vcc, -1, v21\n"
    "s_cbranch_vccz L_R_FORCEA%=\n"
    "s_mov_b32 s2, 0\n"
    "buffer_load_dwordx4 v[24:27], v3, s[36:39], s20 offen sc1\n"
    "buffer_load_dwordx4 v[28:31], v3, s[36:39], s24 offen sc1\n"
    "s_add_u32 s64, s64, 1\n"
    "s_and_b32 s70, s64, 255\n"
    "s_cbranch_scc1 L_R_REA%=\n"
    "buffer_load_dword v21, off, s[36:39], s50 sc1\n"
    "s_waitcnt vmcnt(0)\n"
    "v_readfirstlane_b32 s65, v21\n"
    "s_cmp_lt_u32 s64, 0x40000\n"
    "s_cbranch_scc1 L_R_REA%=\n"
    "s_mov_b32 s65, 1\n"
    "v_mov_b32 v21, 1\n"
    "buffer_store_dword v21, off, s[36:39], s50 sc1\n"
    "s_branch L_R_REA%=\n"
    "L_R_REA%=:\n"
    "s_waitcnt vmcnt(0)\n"
    "s_branch L_R_GOTA%=\n"
    "L_R_FORCEA%=:\n"
    "buffer_store_dword v40, v18, s[36:39], s43 offen sc1\n"
    "s_branch L_R_CONTA%=\n"
    "L_R_LATEKV%=:\n"
    "buffer_load_dword v21, off, s[36:39], s48 sc1\n"
    "s_waitcnt vmcnt(0)\n"
    "v_readfirstlane_b32 s70, v21\n"
    "s_cmp_gt_u32 s70, s60\n"
    "s_cbranch_scc0 L_R_FAILK%=\n"
    "L_R_KVOK%=:\n"
    "s_lshr_b32 s52, s44, 8\n"
    "s_xor_b32 s52, s52, s56\n"
    "s_and_b32 s52, s52, 0x2000\n"
    "s_or_b32 s52, s52, s44\n"
    "s_add_u32 s45, s52, 0x2000000\n"
    "buffer_load_dwordx4 v[112:115], v4, s[36:39], s52 offen sc1\n"
    "buffer_load_dwordx4 v[116:119], v4, s[36:39], s52 offen offset:128 sc1\n"
    "buffer_load_dwordx4 v[120:123], v5, s[36:39], s45 offen sc1\n"
    "buffer_load_dwordx4 v[124:127], v5, s[36:39], s45 offen offset:64 sc1\n"
    "s_memrealtime s[8:9]\n"
    "s_waitcnt lgkmcnt(0)\n"
    "s_add_u32 s70, s8, s7\n"
    "s_bitset0_b32 s70, 31\n"
    "s_sub_i32 s9, s8, s6\n"
    "v_mov_b32 v21, s70\n"
    "s_cmp_eq_u32 s60, 0\n"
    "s_cselect_b32 s3, 0x1f80, 0\n"
    "s_add_u32 s3, s3, s14\n"
    "s_and_b32 s70, s51, 0x1800\n"
    "s_cselect_b32 s70, s38, s3\n"
    "buffer_store_dword v21, v19, s[36:39], s70 offen sc1\n"
    "s_waitcnt vmcnt(1)\n"
    "s_branch L_R_CONTK%=\n"
    "L_R_FAILK%=:\n"
    "s_nop 7\n"
    "s_cmp_lg_u32 s65, 0\n"
    "s_cbranch_scc1 L_R_KVOK%=\n"
    "s_add_u32 s64, s64, 1\n"
    "s_sleep 2\n"
    "s_and_b32 s70, s64, 255\n"
    "s_cbranch_scc1 L_R_LATEKV%=\n"
    "buffer_load_dword v21, off, s[36:39], s50 sc1\n"
    "s_waitcnt vmcnt(0)\n"
    "v_readfirstlane_b32 s65, v21\n"
    "s_cmp_lt_u32 s64, 0x40000\n"
    "s_cbranch_scc1 L_R_LATEKV%=\n"
    "s_mov_b32 s65, 1\n"
    "v_mov_b32 v21, 1\n"
    "buffer_store_dword v21, off, s[36:39], s50 sc1\n"
    "s_branch L_R_LATEKV%=\n"
    "L_R_REDO%=:\n"
    "s_ashr_i32 s71, s70, 31\n"
    "s_lshl_b32 s76, s70, 11\n"
    "s_add_u32 s20, s20, s76\n"
    "s_addc_u32 s21, s21, s71\n"
    "s_lshl_b32 s76, s70, 11\n"
    "s_add_u32 s22, s22, s76\n"
    "s_addc_u32 s23, s23, s71\n"
    "s_lshl_b32 s76, s70, 11\n"
    "s_add_u32 s24, s24, s76\n"
    "s_addc_u32 s25, s25, s71\n"
    "s_lshl_b32 s76, s70, 20\n"
    "s_add_u32 s26, s26, s76\n"
    "s_addc_u32 s27, s27, s71\n"
    "s_waitcnt vmcnt(0) lgkmcnt(0)\n"
    "s_barrier\n"
    "s_branch L_R_PRO%=\n"
    "L_R_TAIL%=:\n"
    "s_waitcnt lgkmcnt(0)\n"
    "v_add_f32 v108, v108, v110\n"
    "v_add_f32 v109, v109, v111\n"
    "v_cvt_pk_bf16_f32 v104, v104, v105\n"
    "v_cvt_pk_bf16_f32 v105, v108, v109\n"
    "s_nop 11\n"
    "ds_read_b64_tr_b16 v[112:113], v8 offset:0\n"
    "ds_read_b64_tr_b16 v[114:115], v8 offset:272\n"
    "s_lshr_b32 s53, s46, 8\n"
    "s_xor_b32 s53, s53, s56\n"
    "s_and_b32 s53, s53, 0x2000\n"
    "s_or_b32 s53, s53, s46\n"
    "s_add_u32 s47, s53, 0x800000\n"
    "s_add_u32 s46, s46, 0x20000\n"
    "s_waitcnt lgkmcnt(0)\n"
    "buffer_store_dwordx4 v[112:115], v6, s[36:39], s53 offen sc1\n"
    "buffer_store_dword v104, v20, s[36:39], s47 offen sc1\n"
    "buffer_store_dword v105, v20, s[36:39], s47 offen offset:256 sc1\n"
    "v_mov_b32 v21, 64\n"
    "s_waitcnt vmcnt(0) lgkmcnt(0)\n"
    "buffer_store_dword v21, v19, s[36:39], s49 offen sc1\n"
    "s_waitcnt vmcnt(0)\n"
    "L_R_FIN:\ns_endpgm\n"
    :
    : "{s[40:47]}"(sa), "{s[52:55]}"(sc), "{s[28:29]}"(sx), "{v[24:27]}"(ve), "{s[36:37]}"(p.ws), "{s38}"(NREC), "{s[48:51]}"(sb), "{s[56:57]}"(sd), "{s[12:15]}"(se), "{s4}"(p.ex2), "{s[20:27]}"(sw), "{v[0:7]}"(va), "{v[8:15]}"(vb), "{v[16:19]}"(vc), "{v20}"(vd), "{s[74:75]}"(sg)
    : ROLE_CLOB);
}

template <int KIND> DI void role_gemm(const Params& p, const Ctx& c, unsigned id) {
  const unsigned tid = threadIdx.x, lane = tid & 63, w = UNI(tid >> 6), lb = c.lb, tok = lane & 31, hh = lane >> 5;
  const unsigned gm = lane >> 4, n = lane & 15, qq = (lane >> 2) & 3, pp = lane & 3;
  const u16 *wa, *wb;
  unsigned ld, fin, src, sstr, out, ostr, vout, qsrc = c.q, seen = 0, fo, x57 = 0, x58 = 0;
  if (KIND == 0) {
    wa = (w < 2 ? p.wk : p.wv) + id * DH + 64 * (w & 1); wb = wa + 32; ld = HD;
    fin = flag(F_XN + (lane & 3), 0); src = 0; qsrc = 0; sstr = 65536; seen = (p.stag >> 16) & 255u;
    id &= 7;
    out = (w < 2 ? O_KF : O_VF) + id * 16384 + (w & 1) * 4096; ostr = 524288; vout = w < 2 ? (gm & 1) * 1024 + (gm >> 1) * 512 + n * 16 : gm * 512 + n * 16; fo = F_KV; x57 = w >> 1;
  } else if (KIND == 1) {
    wa = p.wg + 128 * id + 32 * w; wb = p.wu + 128 * id + 32 * w; ld = DF;
    fin = flag(F_R1 + (lane & 3), c.q); src = O_XNM; sstr = 65536;
    out = O_HF + id * 16384 + w * 2048; ostr = 262144; vout = gm * 512 + n * 16; fo = F_GU;
  } else if (KIND == 2) {
    wa = p.wo + (size_t)(1024 * (id >> 2)) * DM + 128 * (id & 3) + 32 * w; wb = wa + 512 * DM; ld = DM;
    fin = flag(F_QA + 4 * (id >> 2) + (lane & 3), c.q); src = O_OF + (id >> 2) * 131072; sstr = 524288;
    out = O_PW + id * 16384 + w * 2048; ostr = 262144; vout = gm * 512 + n * 16; fo = F_WO;
  } else if (KIND == 3) {
    wa = p.wd + (size_t)(896 * (id >> 2)) * DM + 128 * (id & 3) + 32 * w; wb = wa + 512 * DM; ld = DM;
    fin = flag(F_GU + 7 * (id >> 2) + min(lane & 7, 6u), c.q); src = O_HF + (id >> 2) * 114688; sstr = 262144;
    out = O_PD + id * 16384 + w * 2048; ostr = 131072; vout = gm * 512 + n * 16; fo = F_DN;
  } else {
    const unsigned eh = w & 1, k = lane & 7;
    wa = p.wq + (2 * id + (w >> 1)) * DH + 64 * eh; wb = wa + 32; ld = HD;
    id &= 3;
    const unsigned h = 2 * id + (w >> 1);
    fin = flag(k == 2 ? F_KV + h : (k == 3 ? F_G2 + id : fr(h, 2 * eh + (k & 1))), 0);
    src = 0; qsrc = 0; sstr = 65536;
    out = O_OF + h * 16384 + eh * 4096; ostr = 524288; vout = hh * 1024 + tok * 16; fo = F_QA;
    x57 = O_KF + h * 16384 + eh * 4096; x58 = O_KM + mp(h * 2048) + eh * 1024;
  }
  const v2u pa = ptr2(wa), pb = ptr2(wb);
  constexpr bool WIDE = KIND == 0 || KIND == 4;
  v8u sa = {pa.x, pa.y, KIND == 0 ? UNI(flag(F_GO + id, 0)) : pb.x, KIND == 0 ? (w < 2 ? 8u : 16u) : pb.y, (WIDE ? 16 : 32) * ld, UNI(lb + w * 32768), UNI(src), sstr};
  v8u sb = {UNI(out), ostr, O_ABORT, UNI(qsrc), UNI(c.q), UNI(lb + w * 2048), UNI(seen), UNI(O_STAMP + c.slot * 8192 + w * 2048)};
  v4u sc = {UNI(O_INB + 524288 + (4 * id + w) * 256), UNI(x57), UNI(x58), KIND == 0 ? UNI(id == 0 && w == 0 ? flag(F_R1 + 16, 0) : OOB) : 0u};
  const unsigned cb = (16 * (pp & 1) + 4 * (2 * (gm & 1) + (pp >> 1))) * 2;
  const v8u va = {WIDE ? (lane >> 3) * ld * 2 + ((lane & 7) ^ (gm & 1) * 4) * 16 : (lane >> 2) * ld * 2 + pp * 16,
                  lb + w * 32768 + (KIND == 4 ? (8 * (gm >> 1) + qq) * 128 + (qq >> 1) * 64 + cb
                                              : (KIND == 0 ? (8 * gm + qq) * 128 + (qq >> 1) * 64 + (w < 2 ? pp * 16 : (pp & 1) * 32 + (pp >> 1) * 8) : (8 * gm + qq) * 64 + pp * 16)),
                  w * 2048 + lane * 16, lb + (KIND == 4 ? lane * 16 : gm * 512 + (KIND == 0 && w >= 2 ? 16 * (n >> 3) + (n & 7) : n) * 16), fin, tid == 0 ? flag(fo + id, c.q) : OOB, vout, lane * 16};
#define GEMM_OPS : : "{s[40:47]}"(sa), "{s[48:55]}"(sb), "{s[56:59]}"(sc), "{s[36:37]}"(p.ws), "{s38}"(NREC), "{v[0:7]}"(va)
  if constexpr (KIND == 0) {
    asm volatile("s_mov_b64 exec, -1\ns_and_b32 s37, s37, 0xffff\ns_mov_b32 s39, 0x20000\ns_nop 4\n"
    "s_mov_b32 s64, 0\n"
    "s_mov_b32 s65, 0\n"
    "s_mov_b32 s70, 0x224000\n"
    "s_load_dwordx2 s[72:73], s[36:37], s70\n"
    "s_mov_b32 s78, 0\n"
    "s_mov_b32 s79, 0x220000\n"
    "L_KV_PRO%=:\n"
    "v_xor_b32 v250, 64, v1\n"
    "v_add_u32 v251, s43, v1\n"
    "v_add_u32 v252, s43, v250\n"
    "s_add_u32 m0, s45, 0\n"
    "s_nop 0\n"
    "global_load_lds_dwordx4 v0, s[40:41] \n"
    "s_add_u32 s40, s40, s44\n"
    "s_addc_u32 s41, s41, 0\n"
    "s_add_u32 m0, s45, 1024\n"
    "s_nop 0\n"
    "global_load_lds_dwordx4 v0, s[40:41] \n"
    "s_add_u32 s40, s40, s44\n"
    "s_addc_u32 s41, s41, 0\n"
    "s_add_u32 m0, s45, 2048\n"
    "s_nop 0\n"
    "global_load_lds_dwordx4 v0, s[40:41] \n"
    "s_add_u32 s40, s40, s44\n"
    "s_addc_u32 s41, s41, 0\n"
    "s_add_u32 m0, s45, 3072\n"
    "s_nop 0\n"
    "global_load_lds_dwordx4 v0, s[40:41] \n"
    "s_add_u32 s40, s40, s44\n"
    "s_addc_u32 s41, s41, 0\n"
    "s_add_u32 m0, s45, 4096\n"
    "s_nop 0\n"
    "global_load_lds_dwordx4 v0, s[40:41] \n"
    "s_add_u32 s40, s40, s44\n"
    "s_addc_u32 s41, s41, 0\n"
    "s_add_u32 m0, s45, 5120\n"
    "s_nop 0\n"
    "global_load_lds_dwordx4 v0, s[40:41] \n"
    "s_add_u32 s40, s40, s44\n"
    "s_addc_u32 s41, s41, 0\n"
    "s_add_u32 m0, s45, 6144\n"
    "s_nop 0\n"
    "global_load_lds_dwordx4 v0, s[40:41] \n"
    "s_add_u32 s40, s40, s44\n"
    "s_addc_u32 s41, s41, 0\n"
    "s_add_u32 m0, s45, 7168\n"
    "s_nop 0\n"
    "global_load_lds_dwordx4 v0, s[40:41] \n"
    "s_add_u32 s40, s40, 0x90000\n"
    "s_addc_u32 s41, s41, 0\n"
    "s_add_u32 m0, s45, 8192\n"
    "s_nop 0\n"
    "global_load_lds_dwordx4 v0, s[40:41] \n"
    "s_add_u32 s40, s40, s44\n"
    "s_addc_u32 s41, s41, 0\n"
    "s_add_u32 m0, s45, 9216\n"
    "s_nop 0\n"
    "global_load_lds_dwordx4 v0, s[40:41] \n"
    "s_add_u32 s40, s40, s44\n"
    "s_addc_u32 s41, s41, 0\n"
    "s_add_u32 m0, s45, 10240\n"
    "s_nop 0\n"
    "global_load_lds_dwordx4 v0, s[40:41] \n"
    "s_add_u32 s40, s40, s44\n"
    "s_addc_u32 s41, s41, 0\n"
    "s_add_u32 m0, s45, 11264\n"
    "s_nop 0\n"
    "global_load_lds_dwordx4 v0, s[40:41] \n"
    "s_add_u32 s40, s40, s44\n"
    "s_addc_u32 s41, s41, 0\n"
    "s_add_u32 m0, s45, 12288\n"
    "s_nop 0\n"
    "global_load_lds_dwordx4 v0, s[40:41] \n"
    "s_add_u32 s40, s40, s44\n"
    "s_addc_u32 s41, s41, 0\n"
    "s_add_u32 m0, s45, 13312\n"
    "s_nop 0\n"
    "global_load_lds_dwordx4 v0, s[40:41] \n"
    "s_add_u32 s40, s40, s44\n"
    "s_addc_u32 s41, s41, 0\n"
    "s_add_u32 m0, s45, 14336\n"
    "s_nop 0\n"
    "global_load_lds_dwordx4 v0, s[40:41] \n"
    "s_add_u32 s40, s40, s44\n"
    "s_addc_u32 s41, s41, 0\n"
    "s_add_u32 m0, s45, 15360\n"
    "s_nop 0\n"
    "global_load_lds_dwordx4 v0, s[40:41] \n"
    "s_add_u32 s40, s40, 0x90000\n"
    "s_addc_u32 s41, s41, 0\n"
    "s_add_u32 m0, s45, 16384\n"
    "s_nop 0\n"
    "global_load_lds_dwordx4 v0, s[40:41] \n"
    "s_add_u32 s40, s40, s44\n"
    "s_addc_u32 s41, s41, 0\n"
    "s_add_u32 m0, s45, 17408\n"
    "s_nop 0\n"
    "global_load_lds_dwordx4 v0, s[40:41] \n"
    "s_add_u32 s40, s40, s44\n"
    "s_addc_u32 s41, s41, 0\n"
    "s_add_u32 m0, s45, 18432\n"
    "s_nop 0\n"
    "global_load_lds_dwordx4 v0, s[40:41] \n"
    "s_add_u32 s40, s40, s44\n"
    "s_addc_u32 s41, s41, 0\n"
    "s_add_u32 m0, s45, 19456\n"
    "s_nop 0\n"
    "global_load_lds_dwordx4 v0, s[40:41] \n"
    "s_add_u32 s40, s40, s44\n"
    "s_addc_u32 s41, s41, 0\n"
    "s_add_u32 m0, s45, 20480\n"
    "s_nop 0\n"
    "global_load_lds_dwordx4 v0, s[40:41] \n"
    "s_add_u32 s40, s40, s44\n"
    "s_addc_u32 s41, s41, 0\n"
    "s_add_u32 m0, s45, 21504\n"
    "s_nop 0\n"
    "global_load_lds_dwordx4 v0, s[40:41] \n"
    "s_add_u32 s40, s40, s44\n"
    "s_addc_u32 s41, s41, 0\n"
    "s_add_u32 m0, s45, 22528\n"
    "s_nop 0\n"
    "global_load_lds_dwordx4 v0, s[40:41] \n"
    "s_add_u32 s40, s40, s44\n"
    "s_addc_u32 s41, s41, 0\n"
    "s_add_u32 m0, s45, 23552\n"
    "s_nop 0\n"
    "global_load_lds_dwordx4 v0, s[40:41] \n"
    "s_add_u32 s40, s40, 0x90000\n"
    "s_addc_u32 s41, s41, 0\n"
    "s_add_u32 m0, s45, 24576\n"
    "s_nop 0\n"
    "global_load_lds_dwordx4 v0, s[40:41] \n"
    "s_add_u32 s40, s40, s44\n"
    "s_addc_u32 s41, s41, 0\n"
    "s_add_u32 m0, s45, 25600\n"
    "s_nop 0\n"
    "global_load_lds_dwordx4 v0, s[40:41] \n"
    "s_add_u32 s40, s40, s44\n"
    "s_addc_u32 s41, s41, 0\n"
    "s_add_u32 m0, s45, 26624\n"
    "s_nop 0\n"
    "global_load_lds_dwordx4 v0, s[40:41] \n"
    "s_add_u32 s40, s40, s44\n"
    "s_addc_u32 s41, s41, 0\n"
    "s_add_u32 m0, s45, 27648\n"
    "s_nop 0\n"
    "global_load_lds_dwordx4 v0, s[40:41] \n"
    "s_add_u32 s40, s40, s44\n"
    "s_addc_u32 s41, s41, 0\n"
    "s_add_u32 m0, s45, 28672\n"
    "s_nop 0\n"
    "global_load_lds_dwordx4 v0, s[40:41] \n"
    "s_add_u32 s40, s40, s44\n"
    "s_addc_u32 s41, s41, 0\n"
    "s_add_u32 m0, s45, 29696\n"
    "s_nop 0\n"
    "global_load_lds_dwordx4 v0, s[40:41] \n"
    "s_add_u32 s40, s40, s44\n"
    "s_addc_u32 s41, s41, 0\n"
    "s_add_u32 m0, s45, 30720\n"
    "s_nop 0\n"
    "global_load_lds_dwordx4 v0, s[40:41] \n"
    "s_add_u32 s40, s40, s44\n"
    "s_addc_u32 s41, s41, 0\n"
    "s_add_u32 m0, s45, 31744\n"
    "s_nop 0\n"
    "global_load_lds_dwordx4 v0, s[40:41] \n"
    "s_sub_u32 s40, s40, 0x2f0000\n"
    "s_subb_u32 s41, s41, 0\n"
    "s_waitcnt vmcnt(28)\n"
    "ds_read_b64_tr_b16 a[0:1], v1 offset:0\n"
    "ds_read_b64_tr_b16 a[2:3], v1 offset:512\n"
    "ds_read_b64_tr_b16 a[4:5], v251 offset:0\n"
    "ds_read_b64_tr_b16 a[6:7], v251 offset:512\n"
    "ds_read_b64_tr_b16 a[128:129], v250 offset:0\n"
    "ds_read_b64_tr_b16 a[130:131], v250 offset:512\n"
    "ds_read_b64_tr_b16 a[132:133], v252 offset:0\n"
    "ds_read_b64_tr_b16 a[134:135], v252 offset:512\n"
    "s_waitcnt lgkmcnt(0)\n"
    "s_add_u32 m0, s45, 0\n"
    "s_nop 0\n"
    "global_load_lds_dwordx4 v0, s[40:41] \n"
    "s_add_u32 s40, s40, s44\n"
    "s_addc_u32 s41, s41, 0\n"
    "s_add_u32 m0, s45, 1024\n"
    "s_nop 0\n"
    "global_load_lds_dwordx4 v0, s[40:41] \n"
    "s_add_u32 s40, s40, s44\n"
    "s_addc_u32 s41, s41, 0\n"
    "s_add_u32 m0, s45, 2048\n"
    "s_nop 0\n"
    "global_load_lds_dwordx4 v0, s[40:41] \n"
    "s_add_u32 s40, s40, s44\n"
    "s_addc_u32 s41, s41, 0\n"
    "s_add_u32 m0, s45, 3072\n"
    "s_nop 0\n"
    "global_load_lds_dwordx4 v0, s[40:41] \n"
    "s_add_u32 s40, s40, s44\n"
    "s_addc_u32 s41, s41, 0\n"
    "s_waitcnt vmcnt(28)\n"
    "ds_read_b64_tr_b16 a[8:9], v1 offset:4096\n"
    "ds_read_b64_tr_b16 a[10:11], v1 offset:4608\n"
    "ds_read_b64_tr_b16 a[12:13], v251 offset:4096\n"
    "ds_read_b64_tr_b16 a[14:15], v251 offset:4608\n"
    "ds_read_b64_tr_b16 a[136:137], v250 offset:4096\n"
    "ds_read_b64_tr_b16 a[138:139], v250 offset:4608\n"
    "ds_read_b64_tr_b16 a[140:141], v252 offset:4096\n"
    "ds_read_b64_tr_b16 a[142:143], v252 offset:4608\n"
    "s_waitcnt lgkmcnt(0)\n"
    "s_add_u32 m0, s45, 4096\n"
    "s_nop 0\n"
    "global_load_lds_dwordx4 v0, s[40:41] \n"
    "s_add_u32 s40, s40, s44\n"
    "s_addc_u32 s41, s41, 0\n"
    "s_add_u32 m0, s45, 5120\n"
    "s_nop 0\n"
    "global_load_lds_dwordx4 v0, s[40:41] \n"
    "s_add_u32 s40, s40, s44\n"
    "s_addc_u32 s41, s41, 0\n"
    "s_add_u32 m0, s45, 6144\n"
    "s_nop 0\n"
    "global_load_lds_dwordx4 v0, s[40:41] \n"
    "s_add_u32 s40, s40, s44\n"
    "s_addc_u32 s41, s41, 0\n"
    "s_add_u32 m0, s45, 7168\n"
    "s_nop 0\n"
    "global_load_lds_dwordx4 v0, s[40:41] \n"
    "s_add_u32 s40, s40, 0x90000\n"
    "s_addc_u32 s41, s41, 0\n"
    "s_waitcnt vmcnt(28)\n"
    "ds_read_b64_tr_b16 a[32:33], v1 offset:8192\n"
    "ds_read_b64_tr_b16 a[34:35], v1 offset:8704\n"
    "ds_read_b64_tr_b16 a[36:37], v251 offset:8192\n"
    "ds_read_b64_tr_b16 a[38:39], v251 offset:8704\n"
    "ds_read_b64_tr_b16 a[160:161], v250 offset:8192\n"
    "ds_read_b64_tr_b16 a[162:163], v250 offset:8704\n"
    "ds_read_b64_tr_b16 a[164:165], v252 offset:8192\n"
    "ds_read_b64_tr_b16 a[166:167], v252 offset:8704\n"
    "s_waitcnt lgkmcnt(0)\n"
    "s_add_u32 m0, s45, 8192\n"
    "s_nop 0\n"
    "global_load_lds_dwordx4 v0, s[40:41] \n"
    "s_add_u32 s40, s40, s44\n"
    "s_addc_u32 s41, s41, 0\n"
    "s_add_u32 m0, s45, 9216\n"
    "s_nop 0\n"
    "global_load_lds_dwordx4 v0, s[40:41] \n"
    "s_add_u32 s40, s40, s44\n"
    "s_addc_u32 s41, s41, 0\n"
    "s_add_u32 m0, s45, 10240\n"
    "s_nop 0\n"
    "global_load_lds_dwordx4 v0, s[40:41] \n"
    "s_add_u32 s40, s40, s44\n"
    "s_addc_u32 s41, s41, 0\n"
    "s_add_u32 m0, s45, 11264\n"
    "s_nop 0\n"
    "global_load_lds_dwordx4 v0, s[40:41] \n"
    "s_add_u32 s40, s40, s44\n"
    "s_addc_u32 s41, s41, 0\n"
    "s_waitcnt vmcnt(28)\n"
    "ds_read_b64_tr_b16 a[40:41], v1 offset:12288\n"
    "ds_read_b64_tr_b16 a[42:43], v1 offset:12800\n"
    "ds_read_b64_tr_b16 a[44:45], v251 offset:12288\n"
    "ds_read_b64_tr_b16 a[46:47], v251 offset:12800\n"
    "ds_read_b64_tr_b16 a[168:169], v250 offset:12288\n"
    "ds_read_b64_tr_b16 a[170:171], v250 offset:12800\n"
    "ds_read_b64_tr_b16 a[172:173], v252 offset:12288\n"
    "ds_read_b64_tr_b16 a[174:175], v252 offset:12800\n"
    "s_waitcnt lgkmcnt(0)\n"
    "s_add_u32 m0, s45, 12288\n"
    "s_nop 0\n"
    "global_load_lds_dwordx4 v0, s[40:41] \n"
    "s_add_u32 s40, s40, s44\n"
    "s_addc_u32 s41, s41, 0\n"
    "s_add_u32 m0, s45, 13312\n"
    "s_nop 0\n"
    "global_load_lds_dwordx4 v0, s[40:41] \n"
    "s_add_u32 s40, s40, s44\n"
    "s_addc_u32 s41, s41, 0\n"
    "s_add_u32 m0, s45, 14336\n"
    "s_nop 0\n"
    "global_load_lds_dwordx4 v0, s[40:41] \n"
    "s_add_u32 s40, s40, s44\n"
    "s_addc_u32 s41, s41, 0\n"
    "s_add_u32 m0, s45, 15360\n"
    "s_nop 0\n"
    "global_load_lds_dwordx4 v0, s[40:41] \n"
    "s_add_u32 s40, s40, 0x90000\n"
    "s_addc_u32 s41, s41, 0\n"
    "s_waitcnt vmcnt(28)\n"
    "ds_read_b64_tr_b16 a[64:65], v1 offset:16384\n"
    "ds_read_b64_tr_b16 a[66:67], v1 offset:16896\n"
    "ds_read_b64_tr_b16 a[68:69], v251 offset:16384\n"
    "ds_read_b64_tr_b16 a[70:71], v251 offset:16896\n"
    "ds_read_b64_tr_b16 a[192:193], v250 offset:16384\n"
    "ds_read_b64_tr_b16 a[194:195], v250 offset:16896\n"
    "ds_read_b64_tr_b16 a[196:197], v252 offset:16384\n"
    "ds_read_b64_tr_b16 a[198:199], v252 offset:16896\n"
    "s_waitcnt lgkmcnt(0)\n"
    "s_add_u32 m0, s45, 16384\n"
    "s_nop 0\n"
    "global_load_lds_dwordx4 v0, s[40:41] \n"
    "s_add_u32 s40, s40, s44\n"
    "s_addc_u32 s41, s41, 0\n"
    "s_add_u32 m0, s45, 17408\n"
    "s_nop 0\n"
    "global_load_lds_dwordx4 v0, s[40:41] \n"
    "s_add_u32 s40, s40, s44\n"
    "s_addc_u32 s41, s41, 0\n"
    "s_add_u32 m0, s45, 18432\n"
    "s_nop 0\n"
    "global_load_lds_dwordx4 v0, s[40:41] \n"
    "s_add_u32 s40, s40, s44\n"
    "s_addc_u32 s41, s41, 0\n"
    "s_add_u32 m0, s45, 19456\n"
    "s_nop 0\n"
    "global_load_lds_dwordx4 v0, s[40:41] \n"
    "s_add_u32 s40, s40, s44\n"
    "s_addc_u32 s41, s41, 0\n"
    "s_waitcnt vmcnt(28)\n"
    "ds_read_b64_tr_b16 a[72:73], v1 offset:20480\n"
    "ds_read_b64_tr_b16 a[74:75], v1 offset:20992\n"
    "ds_read_b64_tr_b16 a[76:77], v251 offset:20480\n"
    "ds_read_b64_tr_b16 a[78:79], v251 offset:20992\n"
    "ds_read_b64_tr_b16 a[200:201], v250 offset:20480\n"
    "ds_read_b64_tr_b16 a[202:203], v250 offset:20992\n"
    "ds_read_b64_tr_b16 a[204:205], v252 offset:20480\n"
    "ds_read_b64_tr_b16 a[206:207], v252 offset:20992\n"
    "s_waitcnt lgkmcnt(0)\n"
    "s_add_u32 m0, s45, 20480\n"
    "s_nop 0\n"
    "global_load_lds_dwordx4 v0, s[40:41] \n"
    "s_add_u32 s40, s40, s44\n"
    "s_addc_u32 s41, s41, 0\n"
    "s_add_u32 m0, s45, 21504\n"
    "s_nop 0\n"
    "global_load_lds_dwordx4 v0, s[40:41] \n"
    "s_add_u32 s40, s40, s44\n"
    "s_addc_u32 s41, s41, 0\n"
    "s_add_u32 m0, s45, 22528\n"
    "s_nop 0\n"
    "global_load_lds_dwordx4 v0, s[40:41] \n"
    "s_add_u32 s40, s40, s44\n"
    "s_addc_u32 s41, s41, 0\n"
    "s_add_u32 m0, s45, 23552\n"
    "s_nop 0\n"
    "global_load_lds_dwordx4 v0, s[40:41] \n"
    "s_add_u32 s40, s40, 0x90000\n"
    "s_addc_u32 s41, s41, 0\n"
    "s_waitcnt vmcnt(28)\n"
    "ds_read_b64_tr_b16 a[96:97], v1 offset:24576\n"
    "ds_read_b64_tr_b16 a[98:99], v1 offset:25088\n"
    "ds_read_b64_tr_b16 a[100:101], v251 offset:24576\n"
    "ds_read_b64_tr_b16 a[102:103], v251 offset:25088\n"
    "ds_read_b64_tr_b16 a[224:225], v250 offset:24576\n"
    "ds_read_b64_tr_b16 a[226:227], v250 offset:25088\n"
    "ds_read_b64_tr_b16 a[228:229], v252 offset:24576\n"
    "ds_read_b64_tr_b16 a[230:231], v252 offset:25088\n"
    "s_waitcnt lgkmcnt(0)\n"
    "s_add_u32 m0, s45, 24576\n"
    "s_nop 0\n"
    "global_load_lds_dwordx4 v0, s[40:41] \n"
    "s_add_u32 s40, s40, s44\n"
    "s_addc_u32 s41, s41, 0\n"
    "s_add_u32 m0, s45, 25600\n"
    "s_nop 0\n"
    "global_load_lds_dwordx4 v0, s[40:41] \n"
    "s_add_u32 s40, s40, s44\n"
    "s_addc_u32 s41, s41, 0\n"
    "s_add_u32 m0, s45, 26624\n"
    "s_nop 0\n"
    "global_load_lds_dwordx4 v0, s[40:41] \n"
    "s_add_u32 s40, s40, s44\n"
    "s_addc_u32 s41, s41, 0\n"
    "s_add_u32 m0, s45, 27648\n"
    "s_nop 0\n"
    "global_load_lds_dwordx4 v0, s[40:41] \n"
    "s_add_u32 s40, s40, s44\n"
    "s_addc_u32 s41, s41, 0\n"
    "s_waitcnt vmcnt(28)\n"
    "ds_read_b64_tr_b16 a[104:105], v1 offset:28672\n"
    "ds_read_b64_tr_b16 a[106:107], v1 offset:29184\n"
    "ds_read_b64_tr_b16 a[108:109], v251 offset:28672\n"
    "ds_read_b64_tr_b16 a[110:111], v251 offset:29184\n"
    "ds_read_b64_tr_b16 a[232:233], v250 offset:28672\n"
    "ds_read_b64_tr_b16 a[234:235], v250 offset:29184\n"
    "ds_read_b64_tr_b16 a[236:237], v252 offset:28672\n"
    "ds_read_b64_tr_b16 a[238:239], v252 offset:29184\n"
    "s_waitcnt lgkmcnt(0)\n"
    "s_add_u32 m0, s45, 28672\n"
    "s_nop 0\n"
    "global_load_lds_dwordx4 v0, s[40:41] \n"
    "s_add_u32 s40, s40, s44\n"
    "s_addc_u32 s41, s41, 0\n"
    "s_add_u32 m0, s45, 29696\n"
    "s_nop 0\n"
    "global_load_lds_dwordx4 v0, s[40:41] \n"
    "s_add_u32 s40, s40, s44\n"
    "s_addc_u32 s41, s41, 0\n"
    "s_add_u32 m0, s45, 30720\n"
    "s_nop 0\n"
    "global_load_lds_dwordx4 v0, s[40:41] \n"
    "s_add_u32 s40, s40, s44\n"
    "s_addc_u32 s41, s41, 0\n"
    "s_add_u32 m0, s45, 31744\n"
    "s_nop 0\n"
    "global_load_lds_dwordx4 v0, s[40:41] \n"
    "s_add_u32 s40, s40, s44\n"
    "s_addc_u32 s41, s41, 0\n"
    "s_waitcnt vmcnt(28)\n"
    "ds_read_b64_tr_b16 a[16:17], v1 offset:0\n"
    "ds_read_b64_tr_b16 a[18:19], v1 offset:512\n"
    "ds_read_b64_tr_b16 a[20:21], v251 offset:0\n"
    "ds_read_b64_tr_b16 a[22:23], v251 offset:512\n"
    "ds_read_b64_tr_b16 a[144:145], v250 offset:0\n"
    "ds_read_b64_tr_b16 a[146:147], v250 offset:512\n"
    "ds_read_b64_tr_b16 a[148:149], v252 offset:0\n"
    "ds_read_b64_tr_b16 a[150:151], v252 offset:512\n"
    "s_waitcnt vmcnt(24)\n"
    "ds_read_b64_tr_b16 a[24:25], v1 offset:4096\n"
    "ds_read_b64_tr_b16 a[26:27], v1 offset:4608\n"
    "ds_read_b64_tr_b16 a[28:29], v251 offset:4096\n"
    "ds_read_b64_tr_b16 a[30:31], v251 offset:4608\n"
    "ds_read_b64_tr_b16 a[152:153], v250 offset:4096\n"
    "ds_read_b64_tr_b16 a[154:155], v250 offset:4608\n"
    "ds_read_b64_tr_b16 a[156:157], v252 offset:4096\n"
    "ds_read_b64_tr_b16 a[158:159], v252 offset:4608\n"
    "s_waitcnt vmcnt(20)\n"
    "ds_read_b64_tr_b16 a[48:49], v1 offset:8192\n"
    "ds_read_b64_tr_b16 a[50:51], v1 offset:8704\n"
    "ds_read_b64_tr_b16 a[52:53], v251 offset:8192\n"
    "ds_read_b64_tr_b16 a[54:55], v251 offset:8704\n"
    "ds_read_b64_tr_b16 a[176:177], v250 offset:8192\n"
    "ds_read_b64_tr_b16 a[178:179], v250 offset:8704\n"
    "ds_read_b64_tr_b16 a[180:181], v252 offset:8192\n"
    "ds_read_b64_tr_b16 a[182:183], v252 offset:8704\n"
    "s_waitcnt vmcnt(16)\n"
    "ds_read_b64_tr_b16 a[56:57], v1 offset:12288\n"
    "ds_read_b64_tr_b16 a[58:59], v1 offset:12800\n"
    "ds_read_b64_tr_b16 a[60:61], v251 offset:12288\n"
    "ds_read_b64_tr_b16 a[62:63], v251 offset:12800\n"
    "ds_read_b64_tr_b16 a[184:185], v250 offset:12288\n"
    "ds_read_b64_tr_b16 a[186:187], v250 offset:12800\n"
    "ds_read_b64_tr_b16 a[188:189], v252 offset:12288\n"
    "ds_read_b64_tr_b16 a[190:191], v252 offset:12800\n"
    "s_waitcnt vmcnt(12)\n"
    "ds_read_b64_tr_b16 a[80:81], v1 offset:16384\n"
    "ds_read_b64_tr_b16 a[82:83], v1 offset:16896\n"
    "ds_read_b64_tr_b16 a[84:85], v251 offset:16384\n"
    "ds_read_b64_tr_b16 a[86:87], v251 offset:16896\n"
    "ds_read_b64_tr_b16 a[208:209], v250 offset:16384\n"
    "ds_read_b64_tr_b16 a[210:211], v250 offset:16896\n"
    "ds_read_b64_tr_b16 a[212:213], v252 offset:16384\n"
    "ds_read_b64_tr_b16 a[214:215], v252 offset:16896\n"
    "s_waitcnt vmcnt(8)\n"
    "ds_read_b64_tr_b16 a[88:89], v1 offset:20480\n"
    "ds_read_b64_tr_b16 a[90:91], v1 offset:20992\n"
    "ds_read_b64_tr_b16 a[92:93], v251 offset:20480\n"
    "ds_read_b64_tr_b16 a[94:95], v251 offset:20992\n"
    "ds_read_b64_tr_b16 a[216:217], v250 offset:20480\n"
    "ds_read_b64_tr_b16 a[218:219], v250 offset:20992\n"
    "ds_read_b64_tr_b16 a[220:221], v252 offset:20480\n"
    "ds_read_b64_tr_b16 a[222:223], v252 offset:20992\n"
    "s_waitcnt vmcnt(4)\n"
    "ds_read_b64_tr_b16 a[112:113], v1 offset:24576\n"
    "ds_read_b64_tr_b16 a[114:115], v1 offset:25088\n"
    "ds_read_b64_tr_b16 a[116:117], v251 offset:24576\n"
    "ds_read_b64_tr_b16 a[118:119], v251 offset:25088\n"
    "ds_read_b64_tr_b16 a[240:241], v250 offset:24576\n"
    "ds_read_b64_tr_b16 a[242:243], v250 offset:25088\n"
    "ds_read_b64_tr_b16 a[244:245], v252 offset:24576\n"
    "ds_read_b64_tr_b16 a[246:247], v252 offset:25088\n"
    "s_waitcnt vmcnt(0)\n"
    "ds_read_b64_tr_b16 a[120:121], v1 offset:28672\n"
    "ds_read_b64_tr_b16 a[122:123], v1 offset:29184\n"
    "ds_read_b64_tr_b16 a[124:125], v251 offset:28672\n"
    "ds_read_b64_tr_b16 a[126:127], v251 offset:29184\n"
    "ds_read_b64_tr_b16 a[248:249], v250 offset:28672\n"
    "ds_read_b64_tr_b16 a[250:251], v250 offset:29184\n"
    "ds_read_b64_tr_b16 a[252:253], v252 offset:28672\n"
    "ds_read_b64_tr_b16 a[254:255], v252 offset:29184\n"
    "s_waitcnt lgkmcnt(0)\n"
    "s_barrier\n"
    "s_branch L_KV_GLD%=\n"
    "L_KV_GATE%=:\n"
    "s_sleep 10\n"
    "s_add_u32 s78, s78, 1\n"
    "s_cmp_lt_u32 s78, 0x10000\n"
    "s_cbranch_scc1 L_KV_GLD%=\n"
    "s_endpgm\n"
    "L_KV_GLD%=:\n"
    "v_lshlrev_b32 v16, 3, v7\n"
    "v_add_u32 v18, 0x2000, v7\n"
    "buffer_load_dwordx2 v[16:17], v16, s[36:39], s79 offen sc1\n"
    "buffer_load_dwordx2 v[18:19], v18, s[36:39], s79 offen sc1\n"
    "s_waitcnt vmcnt(0)\n"
    "L_KV_GCHK%=:\n"
    "s_waitcnt lgkmcnt(0)\n"
    "s_add_u32 s76, s72, 0x7F4A7C15\n"
    "s_addc_u32 s77, s73, 0x9E3779B9\n"
    "s_andn2_b32 s77, s77, 255\n"
    "v_xor_b32 v16, s76, v16\n"
    "v_xor_b32 v18, s76, v18\n"
    "v_xor_b32 v17, s77, v17\n"
    "v_xor_b32 v19, s77, v19\n"
    "v_or_b32 v16, v16, v18\n"
    "v_or_b32 v18, v17, v19\n"
    "v_lshrrev_b32 v18, 8, v18\n"
    "v_or_b32 v16, v16, v18\n"
    "v_cmp_ne_u32 vcc, 0, v16\n"
    "s_cbranch_vccnz L_KV_GATE%=\n"
    "v_readfirstlane_b32 s70, v17\n"
    "v_and_b32 v16, 8, v17\n"
    "v_cmp_ne_u32 vcc, 0, v16\n"
    "s_bcnt1_i32_b64 s76, vcc\n"
    "s_cmp_gt_u32 s76, 4\n"
    "s_cselect_b32 s77, 0, 4\n"
    "s_add_u32 s71, s70, s74\n"
    "s_xor_b32 s77, s77, s71\n"
    "s_and_b32 s77, s77, 4\n"
    "s_lshl_b32 s77, s77, 11\n"
    "s_and_b32 s76, s71, 3\n"
    "s_and_b32 s70, s75, 3\n"
    "s_and_b32 s75, s71, 7\n"
    "s_sub_i32 s70, s76, s70\n"
    "s_cmp_lg_u32 s70, 0\n"
    "s_cbranch_scc1 L_KV_REDO%=\n"
    "s_lshl_b32 s70, s76, 17\n"
    "s_add_u32 s48, s48, s70\n"
    "s_lshl_b32 s70, s76, 10\n"
    "v_add_u32 v5, s70, v5\n"
    "s_lshl_b32 s70, s76, 7\n"
    "s_add_u32 s59, s59, s70\n"
    "s_xor_b32 s59, s59, s77\n"
    "s_lshl_b32 s70, s76, 14\n"
    "s_add_u32 s42, s42, s70\n"
    "s_add_u32 s56, s56, s70\n"
    "s_mov_b32 s52, s77\n"
    "v_lshrrev_b32 v15, 8, v7\n"
    "v_and_b32 v16, 255, v7\n"
    "v_lshl_or_b32 v15, v15, 16, v16\n"
    "v_mov_b64 v[20:21], -1\n"
    "v_mov_b64 v[22:23], -1\n"
    "s_cmp_gt_u32 s75, 3\n"
    "s_cselect_b32 s51, s77, s51\n"
    "s_cselect_b32 s54, 0, s54\n"
    "s_cselect_b32 s58, 0, 3\n"
    "s_mov_b32 s46, 0x400000\n"
    "s_cselect_b32 s46, 0x800000, s46\n"
    "s_cselect_b64 vcc, 15, 0\n"
    "v_mov_b32 v16, s42\n"
    "v_cndmask_b32 v4, v16, v4, vcc\n"
    "v_xor_b32 v4, s77, v4\n"
    "v_xor_b32 v5, s77, v5\n"
    "v_mov_b32 v8, s58\n"
    "v_mov_b32 v13, 0x80000000\n"
    "s_sub_u32 s71, 1, s54\n"
    "s_mov_b32 s60, 0\n"
    "s_mov_b32 s61, 1\n"
    "s_mov_b32 s62, 0\n"
    "s_mov_b32 s63, 0\n"
    "s_bfe_u32 s17, s45, 0x2000f\n"
    "s_cmp_eq_u32 s17, 1\n"
    "s_cselect_b32 s17, 1, 0\n"
    "L_KV_GO%=:\n"
    "s_lshr_b32 s68, s46, 8\n"
    "s_xor_b32 s68, s68, s51\n"
    "s_and_b32 s68, s68, 0x2000\n"
    "s_or_b32 s68, s68, s46\n"
    "s_add_u32 s66, s53, s62\n"
    "s_mov_b32 m0, s66\n"
    "v_add_u32 v11, s62, v3\n"
    "s_add_u32 s46, s46, s47\n"
    "s_cmp_lg_u32 s57, 0\n"
    "s_cbranch_scc1 L_KV_LOOPB%=\n"
    "L_KV_LOOPA%=:\n"
    "s_lshr_b32 s69, s48, 8\n"
    "s_xor_b32 s69, s69, s52\n"
    "s_and_b32 s69, s69, 0x2000\n"
    "s_or_b32 s69, s69, s48\n"
    "s_add_u32 s48, s48, s49\n"
    "v_mov_b32 v12, s61\n"
    "s_memtime s[96:97]\n"
    "s_memrealtime s[98:99]\n"
    "s_waitcnt lgkmcnt(0)\n"
    "v_mov_b32 v24, s96\n"
    "v_mov_b32 v25, s98\n"
    "s_mov_b64 exec, 1\n"
    "buffer_store_dwordx2 v[24:25], off, s[36:39], s59 sc1\n"
    "s_mov_b64 exec, -1\n"
    "s_cmp_eq_u32 s17, 0\n"
    "s_cbranch_scc1 L_KV_WAok%=\n"
    "v_cmp_gt_i32 vcc, s71, v8\n"
    "s_cbranch_vccz L_KV_WAok%=\n"
    "L_KV_WA%=:\n"
    "buffer_load_dword v8, v4, s[36:39], 0 offen sc1\n"
    "s_waitcnt vmcnt(0)\n"
    "v_cmp_gt_i32 vcc, s71, v8\n"
    "s_cbranch_vccnz L_KV_WAf%=\n"
    "L_KV_WAok%=:\n"
    "s_cmp_lg_u32 s63, 0\n"
    "s_barrier\n"
    "s_cbranch_scc1 L_KV_HAVEA%=\n"
    "buffer_load_dwordx4 v2, s[36:39], s68 offen sc1 lds\n"
    "buffer_load_dwordx4 v2, s[36:39], s68 offen offset:1024 sc1 lds\n"
    "s_add_u32 s68, s68, 0x4000\n"
    "s_add_u32 m0, s66, 8192\n"
    "s_nop 0\n"
    "buffer_load_dwordx4 v2, s[36:39], s68 offen sc1 lds\n"
    "buffer_load_dwordx4 v2, s[36:39], s68 offen offset:1024 sc1 lds\n"
    "s_add_u32 s68, s68, 0x4000\n"
    "s_add_u32 m0, s66, 16384\n"
    "s_nop 0\n"
    "buffer_load_dwordx4 v2, s[36:39], s68 offen sc1 lds\n"
    "buffer_load_dwordx4 v2, s[36:39], s68 offen offset:1024 sc1 lds\n"
    "s_add_u32 s68, s68, 0x4000\n"
    "s_add_u32 m0, s66, 24576\n"
    "s_nop 0\n"
    "buffer_load_dwordx4 v2, s[36:39], s68 offen sc1 lds\n"
    "buffer_load_dwordx4 v2, s[36:39], s68 offen offset:1024 sc1 lds\n"
    "buffer_load_dword v10, v13, s[36:39], 0 offen\n"
    "L_KV_HAVEA%=:\n"
    "s_waitcnt vmcnt(7)\n"
    "s_barrier\n"
    "ds_read_b128 v[128:131], v11 offset:0\n"
    "ds_read_b128 v[132:135], v11 offset:256\n"
    "ds_read_b128 v[136:139], v11 offset:2048\n"
    "ds_read_b128 v[140:143], v11 offset:2304\n"
    "s_waitcnt lgkmcnt(3)\n"
    "v_mfma_f32_16x16x32_bf16 v[32:35], a[0:3], v[128:131], 0\n"
    "s_waitcnt lgkmcnt(2)\n"
    "v_mfma_f32_16x16x32_bf16 v[40:43], a[0:3], v[132:135], 0\n"
    "v_mfma_f32_16x16x32_bf16 v[44:47], a[4:7], v[132:135], 0\n"
    "v_mfma_f32_16x16x32_bf16 v[36:39], a[4:7], v[128:131], 0\n"
    "v_mfma_f32_16x16x32_bf16 v[48:51], a[128:131], v[128:131], 0\n"
    "v_mfma_f32_16x16x32_bf16 v[56:59], a[128:131], v[132:135], 0\n"
    "v_mfma_f32_16x16x32_bf16 v[60:63], a[132:135], v[132:135], 0\n"
    "ds_read_b128 v[144:147], v11 offset:4096\n"
    "v_mfma_f32_16x16x32_bf16 v[52:55], a[132:135], v[128:131], 0\n"
    "ds_read_b128 v[148:151], v11 offset:4352\n"
    "s_waitcnt lgkmcnt(3)\n"
    "v_mfma_f32_16x16x32_bf16 v[32:35], a[8:11], v[136:139], v[32:35]\n"
    "s_waitcnt lgkmcnt(2)\n"
    "v_mfma_f32_16x16x32_bf16 v[40:43], a[8:11], v[140:143], v[40:43]\n"
    "v_mfma_f32_16x16x32_bf16 v[44:47], a[12:15], v[140:143], v[44:47]\n"
    "v_mfma_f32_16x16x32_bf16 v[36:39], a[12:15], v[136:139], v[36:39]\n"
    "v_mfma_f32_16x16x32_bf16 v[48:51], a[136:139], v[136:139], v[48:51]\n"
    "v_mfma_f32_16x16x32_bf16 v[56:59], a[136:139], v[140:143], v[56:59]\n"
    "v_mfma_f32_16x16x32_bf16 v[60:63], a[140:143], v[140:143], v[60:63]\n"
    "ds_read_b128 v[152:155], v11 offset:6144\n"
    "v_mfma_f32_16x16x32_bf16 v[52:55], a[140:143], v[136:139], v[52:55]\n"
    "ds_read_b128 v[156:159], v11 offset:6400\n"
    "s_waitcnt lgkmcnt(3)\n"
    "v_mfma_f32_16x16x32_bf16 v[32:35], a[16:19], v[144:147], v[32:35]\n"
    "s_waitcnt lgkmcnt(2)\n"
    "v_mfma_f32_16x16x32_bf16 v[40:43], a[16:19], v[148:151], v[40:43]\n"
    "v_mfma_f32_16x16x32_bf16 v[44:47], a[20:23], v[148:151], v[44:47]\n"
    "v_mfma_f32_16x16x32_bf16 v[36:39], a[20:23], v[144:147], v[36:39]\n"
    "v_mfma_f32_16x16x32_bf16 v[48:51], a[144:147], v[144:147], v[48:51]\n"
    "v_mfma_f32_16x16x32_bf16 v[56:59], a[144:147], v[148:151], v[56:59]\n"
    "v_mfma_f32_16x16x32_bf16 v[60:63], a[148:151], v[148:151], v[60:63]\n"
    "s_waitcnt vmcnt(5)\n"
    "s_barrier\n"
    "ds_read_b128 v[160:163], v11 offset:8192\n"
    "v_mfma_f32_16x16x32_bf16 v[52:55], a[148:151], v[144:147], v[52:55]\n"
    "ds_read_b128 v[164:167], v11 offset:8448\n"
    "s_waitcnt lgkmcnt(3)\n"
    "v_mfma_f32_16x16x32_bf16 v[32:35], a[24:27], v[152:155], v[32:35]\n"
    "s_waitcnt lgkmcnt(2)\n"
    "v_mfma_f32_16x16x32_bf16 v[40:43], a[24:27], v[156:159], v[40:43]\n"
    "v_mfma_f32_16x16x32_bf16 v[44:47], a[28:31], v[156:159], v[44:47]\n"
    "v_mfma_f32_16x16x32_bf16 v[36:39], a[28:31], v[152:155], v[36:39]\n"
    "v_mfma_f32_16x16x32_bf16 v[48:51], a[152:155], v[152:155], v[48:51]\n"
    "v_mfma_f32_16x16x32_bf16 v[56:59], a[152:155], v[156:159], v[56:59]\n"
    "v_mfma_f32_16x16x32_bf16 v[60:63], a[156:159], v[156:159], v[60:63]\n"
    "ds_read_b128 v[168:171], v11 offset:10240\n"
    "v_mfma_f32_16x16x32_bf16 v[52:55], a[156:159], v[152:155], v[52:55]\n"
    "ds_read_b128 v[172:175], v11 offset:10496\n"
    "s_waitcnt lgkmcnt(3)\n"
    "v_mfma_f32_16x16x32_bf16 v[32:35], a[32:35], v[160:163], v[32:35]\n"
    "s_waitcnt lgkmcnt(2)\n"
    "v_mfma_f32_16x16x32_bf16 v[40:43], a[32:35], v[164:167], v[40:43]\n"
    "v_mfma_f32_16x16x32_bf16 v[44:47], a[36:39], v[164:167], v[44:47]\n"
    "v_mfma_f32_16x16x32_bf16 v[36:39], a[36:39], v[160:163], v[36:39]\n"
    "v_mfma_f32_16x16x32_bf16 v[48:51], a[160:163], v[160:163], v[48:51]\n"
    "v_mfma_f32_16x16x32_bf16 v[56:59], a[160:163], v[164:167], v[56:59]\n"
    "v_mfma_f32_16x16x32_bf16 v[60:63], a[164:167], v[164:167], v[60:63]\n"
    "ds_read_b128 v[176:179], v11 offset:12288\n"
    "v_mfma_f32_16x16x32_bf16 v[52:55], a[164:167], v[160:163], v[52:55]\n"
    "ds_read_b128 v[180:183], v11 offset:12544\n"
    "s_waitcnt lgkmcnt(3)\n"
    "v_mfma_f32_16x16x32_bf16 v[32:35], a[40:43], v[168:171], v[32:35]\n"
    "s_waitcnt lgkmcnt(2)\n"
    "v_mfma_f32_16x16x32_bf16 v[40:43], a[40:43], v[172:175], v[40:43]\n"
    "v_mfma_f32_16x16x32_bf16 v[44:47], a[44:47], v[172:175], v[44:47]\n"
    "v_mfma_f32_16x16x32_bf16 v[36:39], a[44:47], v[168:171], v[36:39]\n"
    "v_mfma_f32_16x16x32_bf16 v[48:51], a[168:171], v[168:171], v[48:51]\n"
    "v_mfma_f32_16x16x32_bf16 v[56:59], a[168:171], v[172:175], v[56:59]\n"
    "v_mfma_f32_16x16x32_bf16 v[60:63], a[172:175], v[172:175], v[60:63]\n"
    "ds_read_b128 v[184:187], v11 offset:14336\n"
    "v_mfma_f32_16x16x32_bf16 v[52:55], a[172:175], v[168:171], v[52:55]\n"
    "ds_read_b128 v[188:191], v11 offset:14592\n"
    "s_waitcnt lgkmcnt(3)\n"
    "v_mfma_f32_16x16x32_bf16 v[32:35], a[48:51], v[176:179], v[32:35]\n"
    "s_waitcnt lgkmcnt(2)\n"
    "v_mfma_f32_16x16x32_bf16 v[40:43], a[48:51], v[180:183], v[40:43]\n"
    "v_mfma_f32_16x16x32_bf16 v[44:47], a[52:55], v[180:183], v[44:47]\n"
    "v_mfma_f32_16x16x32_bf16 v[36:39], a[52:55], v[176:179], v[36:39]\n"
    "v_mfma_f32_16x16x32_bf16 v[48:51], a[176:179], v[176:179], v[48:51]\n"
    "v_mfma_f32_16x16x32_bf16 v[56:59], a[176:179], v[180:183], v[56:59]\n"
    "v_mfma_f32_16x16x32_bf16 v[60:63], a[180:183], v[180:183], v[60:63]\n"
    "s_waitcnt vmcnt(3)\n"
    "s_barrier\n"
    "ds_read_b128 v[192:195], v11 offset:16384\n"
    "v_mfma_f32_16x16x32_bf16 v[52:55], a[180:183], v[176:179], v[52:55]\n"
    "ds_read_b128 v[196:199], v11 offset:16640\n"
    "s_waitcnt lgkmcnt(3)\n"
    "v_mfma_f32_16x16x32_bf16 v[32:35], a[56:59], v[184:187], v[32:35]\n"
    "s_waitcnt lgkmcnt(2)\n"
    "v_mfma_f32_16x16x32_bf16 v[40:43], a[56:59], v[188:191], v[40:43]\n"
    "v_mfma_f32_16x16x32_bf16 v[44:47], a[60:63], v[188:191], v[44:47]\n"
    "v_mfma_f32_16x16x32_bf16 v[36:39], a[60:63], v[184:187], v[36:39]\n"
    "v_mfma_f32_16x16x32_bf16 v[48:51], a[184:187], v[184:187], v[48:51]\n"
    "v_mfma_f32_16x16x32_bf16 v[56:59], a[184:187], v[188:191], v[56:59]\n"
    "v_mfma_f32_16x16x32_bf16 v[60:63], a[188:191], v[188:191], v[60:63]\n"
    "ds_read_b128 v[200:203], v11 offset:18432\n"
    "v_mfma_f32_16x16x32_bf16 v[52:55], a[188:191], v[184:187], v[52:55]\n"
    "ds_read_b128 v[204:207], v11 offset:18688\n"
    "s_waitcnt lgkmcnt(3)\n"
    "v_mfma_f32_16x16x32_bf16 v[32:35], a[64:67], v[192:195], v[32:35]\n"
    "s_waitcnt lgkmcnt(2)\n"
    "v_mfma_f32_16x16x32_bf16 v[40:43], a[64:67], v[196:199], v[40:43]\n"
    "v_mfma_f32_16x16x32_bf16 v[44:47], a[68:71], v[196:199], v[44:47]\n"
    "v_mfma_f32_16x16x32_bf16 v[36:39], a[68:71], v[192:195], v[36:39]\n"
    "v_mfma_f32_16x16x32_bf16 v[48:51], a[192:195], v[192:195], v[48:51]\n"
    "v_mfma_f32_16x16x32_bf16 v[56:59], a[192:195], v[196:199], v[56:59]\n"
    "v_mfma_f32_16x16x32_bf16 v[60:63], a[196:199], v[196:199], v[60:63]\n"
    "ds_read_b128 v[208:211], v11 offset:20480\n"
    "v_mfma_f32_16x16x32_bf16 v[52:55], a[196:199], v[192:195], v[52:55]\n"
    "ds_read_b128 v[212:215], v11 offset:20736\n"
    "s_waitcnt lgkmcnt(3)\n"
    "v_mfma_f32_16x16x32_bf16 v[32:35], a[72:75], v[200:203], v[32:35]\n"
    "s_waitcnt lgkmcnt(2)\n"
    "v_mfma_f32_16x16x32_bf16 v[40:43], a[72:75], v[204:207], v[40:43]\n"
    "v_mfma_f32_16x16x32_bf16 v[44:47], a[76:79], v[204:207], v[44:47]\n"
    "v_mfma_f32_16x16x32_bf16 v[36:39], a[76:79], v[200:203], v[36:39]\n"
    "v_mfma_f32_16x16x32_bf16 v[48:51], a[200:203], v[200:203], v[48:51]\n"
    "v_mfma_f32_16x16x32_bf16 v[56:59], a[200:203], v[204:207], v[56:59]\n"
    "v_mfma_f32_16x16x32_bf16 v[60:63], a[204:207], v[204:207], v[60:63]\n"
    "ds_read_b128 v[216:219], v11 offset:22528\n"
    "v_mfma_f32_16x16x32_bf16 v[52:55], a[204:207], v[200:203], v[52:55]\n"
    "ds_read_b128 v[220:223], v11 offset:22784\n"
    "s_waitcnt lgkmcnt(3)\n"
    "v_mfma_f32_16x16x32_bf16 v[32:35], a[80:83], v[208:211], v[32:35]\n"
    "s_lshr_b32 s67, s56, 8\n"
    "s_xor_b32 s67, s67, s52\n"
    "s_waitcnt lgkmcnt(2)\n"
    "v_mfma_f32_16x16x32_bf16 v[40:43], a[80:83], v[212:215], v[40:43]\n"
    "s_and_b32 s67, s67, 0x2000\n"
    "s_or_b32 s67, s67, s56\n"
    "v_mfma_f32_16x16x32_bf16 v[44:47], a[84:87], v[212:215], v[44:47]\n"
    "v_mfma_f32_16x16x32_bf16 v[36:39], a[84:87], v[208:211], v[36:39]\n"
    "v_mfma_f32_16x16x32_bf16 v[48:51], a[208:211], v[208:211], v[48:51]\n"
    "buffer_store_dwordx4 v[20:23], v15, s[36:39], s67 offen sc1\n"
    "v_mfma_f32_16x16x32_bf16 v[56:59], a[208:211], v[212:215], v[56:59]\n"
    "v_mfma_f32_16x16x32_bf16 v[60:63], a[212:215], v[212:215], v[60:63]\n"
    "s_waitcnt vmcnt(2)\n"
    "s_barrier\n"
    "ds_read_b128 v[224:227], v11 offset:24576\n"
    "v_mfma_f32_16x16x32_bf16 v[52:55], a[212:215], v[208:211], v[52:55]\n"
    "ds_read_b128 v[228:231], v11 offset:24832\n"
    "s_waitcnt lgkmcnt(3)\n"
    "v_mfma_f32_16x16x32_bf16 v[32:35], a[88:91], v[216:219], v[32:35]\n"
    "s_xor_b32 s67, s67, 0x40000\n"
    "s_add_u32 s56, s56, 0x80000\n"
    "s_waitcnt lgkmcnt(2)\n"
    "v_mfma_f32_16x16x32_bf16 v[40:43], a[88:91], v[220:223], v[40:43]\n"
    "v_mfma_f32_16x16x32_bf16 v[44:47], a[92:95], v[220:223], v[44:47]\n"
    "v_mfma_f32_16x16x32_bf16 v[36:39], a[92:95], v[216:219], v[36:39]\n"
    "v_mfma_f32_16x16x32_bf16 v[48:51], a[216:219], v[216:219], v[48:51]\n"
    "buffer_store_dwordx4 v[20:23], v15, s[36:39], s67 offen sc1\n"
    "v_mfma_f32_16x16x32_bf16 v[56:59], a[216:219], v[220:223], v[56:59]\n"
    "v_mfma_f32_16x16x32_bf16 v[60:63], a[220:223], v[220:223], v[60:63]\n"
    "ds_read_b128 v[232:235], v11 offset:26624\n"
    "v_mfma_f32_16x16x32_bf16 v[52:55], a[220:223], v[216:219], v[52:55]\n"
    "s_waitcnt lgkmcnt(2)\n"
    "v_mfma_f32_16x16x32_bf16 v[32:35], a[96:99], v[224:227], v[32:35]\n"
    "ds_read_b128 v[236:239], v11 offset:28672\n"
    "s_waitcnt lgkmcnt(2)\n"
    "v_mfma_f32_16x16x32_bf16 v[40:43], a[96:99], v[228:231], v[40:43]\n"
    "v_mfma_f32_16x16x32_bf16 v[44:47], a[100:103], v[228:231], v[44:47]\n"
    "ds_read_b128 v[240:243], v11 offset:30720\n"
    "v_mfma_f32_16x16x32_bf16 v[36:39], a[100:103], v[224:227], v[36:39]\n"
    "v_mfma_f32_16x16x32_bf16 v[48:51], a[224:227], v[224:227], v[48:51]\n"
    "buffer_load_dword v9, v4, s[36:39], 0 offen sc1\n"
    "v_mfma_f32_16x16x32_bf16 v[56:59], a[224:227], v[228:231], v[56:59]\n"
    "v_mfma_f32_16x16x32_bf16 v[60:63], a[228:231], v[228:231], v[60:63]\n"
    "v_mfma_f32_16x16x32_bf16 v[52:55], a[228:231], v[224:227], v[52:55]\n"
    "s_waitcnt lgkmcnt(2)\n"
    "v_mfma_f32_16x16x32_bf16 v[32:35], a[104:107], v[232:235], v[32:35]\n"
    "v_mfma_f32_16x16x32_bf16 v[36:39], a[108:111], v[232:235], v[36:39]\n"
    "s_waitcnt lgkmcnt(1)\n"
    "v_mfma_f32_16x16x32_bf16 v[32:35], a[112:115], v[236:239], v[32:35]\n"
    "ds_read_b128 v[244:247], v11 offset:26880\n"
    "v_mfma_f32_16x16x32_bf16 v[36:39], a[116:119], v[236:239], v[36:39]\n"
    "s_waitcnt lgkmcnt(1)\n"
    "v_mfma_f32_16x16x32_bf16 v[32:35], a[120:123], v[240:243], v[32:35]\n"
    "ds_read_b128 v[132:135], v11 offset:28928\n"
    "v_mfma_f32_16x16x32_bf16 v[36:39], a[124:127], v[240:243], v[36:39]\n"
    "v_mfma_f32_16x16x32_bf16 v[48:51], a[232:235], v[232:235], v[48:51]\n"
    "ds_read_b128 v[128:131], v11 offset:30976\n"
    "v_mfma_f32_16x16x32_bf16 v[52:55], a[236:239], v[232:235], v[52:55]\n"
    "v_cvt_pk_bf16_f32 v96, v32, v33\n"
    "v_cvt_pk_bf16_f32 v97, v34, v35\n"
    "v_mfma_f32_16x16x32_bf16 v[48:51], a[240:243], v[236:239], v[48:51]\n"
    "v_cvt_pk_bf16_f32 v98, v36, v37\n"
    "v_cvt_pk_bf16_f32 v99, v38, v39\n"
    "v_mfma_f32_16x16x32_bf16 v[52:55], a[244:247], v[236:239], v[52:55]\n"
    "buffer_store_dwordx4 v[96:99], v6, s[36:39], s69 offen offset:0 sc1\n"
    "v_mfma_f32_16x16x32_bf16 v[48:51], a[248:251], v[240:243], v[48:51]\n"
    "v_mfma_f32_16x16x32_bf16 v[52:55], a[252:255], v[240:243], v[52:55]\n"
    "s_waitcnt lgkmcnt(2)\n"
    "v_mfma_f32_16x16x32_bf16 v[40:43], a[104:107], v[244:247], v[40:43]\n"
    "v_mfma_f32_16x16x32_bf16 v[44:47], a[108:111], v[244:247], v[44:47]\n"
    "v_cvt_pk_bf16_f32 v100, v48, v49\n"
    "v_cvt_pk_bf16_f32 v101, v50, v51\n"
    "s_waitcnt lgkmcnt(1)\n"
    "v_mfma_f32_16x16x32_bf16 v[40:43], a[112:115], v[132:135], v[40:43]\n"
    "v_cvt_pk_bf16_f32 v102, v52, v53\n"
    "v_cvt_pk_bf16_f32 v103, v54, v55\n"
    "v_mfma_f32_16x16x32_bf16 v[44:47], a[116:119], v[132:135], v[44:47]\n"
    "buffer_store_dwordx4 v[100:103], v6, s[36:39], s69 offen offset:2048 sc1\n"
    "s_waitcnt lgkmcnt(0)\n"
    "v_mfma_f32_16x16x32_bf16 v[40:43], a[120:123], v[128:131], v[40:43]\n"
    "v_mfma_f32_16x16x32_bf16 v[44:47], a[124:127], v[128:131], v[44:47]\n"
    "v_mfma_f32_16x16x32_bf16 v[56:59], a[232:235], v[244:247], v[56:59]\n"
    "v_mfma_f32_16x16x32_bf16 v[60:63], a[236:239], v[244:247], v[60:63]\n"
    "v_cvt_pk_bf16_f32 v104, v40, v41\n"
    "v_cvt_pk_bf16_f32 v105, v42, v43\n"
    "v_mfma_f32_16x16x32_bf16 v[56:59], a[240:243], v[132:135], v[56:59]\n"
    "v_cvt_pk_bf16_f32 v106, v44, v45\n"
    "v_cvt_pk_bf16_f32 v107, v46, v47\n"
    "v_mfma_f32_16x16x32_bf16 v[60:63], a[244:247], v[132:135], v[60:63]\n"
    "buffer_store_dwordx4 v[104:107], v6, s[36:39], s69 offen offset:256 sc1\n"
    "v_mfma_f32_16x16x32_bf16 v[56:59], a[248:251], v[128:131], v[56:59]\n"
    "v_mfma_f32_16x16x32_bf16 v[60:63], a[252:255], v[128:131], v[60:63]\n"
    "s_nop 3\n"
    "v_cvt_pk_bf16_f32 v108, v56, v57\n"
    "v_cvt_pk_bf16_f32 v109, v58, v59\n"
    "s_nop 1\n"
    "v_cvt_pk_bf16_f32 v110, v60, v61\n"
    "v_cvt_pk_bf16_f32 v111, v62, v63\n"
    "buffer_store_dwordx4 v[108:111], v6, s[36:39], s69 offen offset:2304 sc1\n"
    "s_add_u32 s60, s60, 1\n"
    "s_add_u32 s61, s61, 1\n"
    "s_xor_b32 s62, s62, 0x10000\n"
    "s_mov_b32 s63, 0\n"
    "s_add_u32 s71, s71, 1\n"
    "s_cmp_eq_u32 s60, 64\n"
    "s_cselect_b32 s71, 1000, s71\n"
    "s_lshr_b32 s68, s46, 8\n"
    "s_xor_b32 s68, s68, s51\n"
    "s_and_b32 s68, s68, 0x2000\n"
    "s_or_b32 s68, s68, s46\n"
    "s_add_u32 s66, s53, s62\n"
    "s_mov_b32 m0, s66\n"
    "v_add_u32 v11, s62, v3\n"
    "s_add_u32 s46, s46, s47\n"
    "s_waitcnt vmcnt(4)\n"
    "v_max_u32 v8, v8, v9\n"
    "v_cmp_gt_i32 vcc, s71, v8\n"
    "s_cbranch_vccnz L_KV_NOIA%=\n"
    "s_mov_b32 s63, 1\n"
    "buffer_load_dwordx4 v2, s[36:39], s68 offen sc1 lds\n"
    "buffer_load_dwordx4 v2, s[36:39], s68 offen offset:1024 sc1 lds\n"
    "s_add_u32 s68, s68, 0x4000\n"
    "s_add_u32 m0, s66, 8192\n"
    "s_nop 0\n"
    "buffer_load_dwordx4 v2, s[36:39], s68 offen sc1 lds\n"
    "buffer_load_dwordx4 v2, s[36:39], s68 offen offset:1024 sc1 lds\n"
    "s_add_u32 s68, s68, 0x4000\n"
    "s_add_u32 m0, s66, 16384\n"
    "s_nop 0\n"
    "buffer_load_dwordx4 v2, s[36:39], s68 offen sc1 lds\n"
    "buffer_load_dwordx4 v2, s[36:39], s68 offen offset:1024 sc1 lds\n"
    "s_add_u32 s68, s68, 0x4000\n"
    "s_add_u32 m0, s66, 24576\n"
    "s_nop 0\n"
    "buffer_load_dwordx4 v2, s[36:39], s68 offen sc1 lds\n"
    "buffer_load_dwordx4 v2, s[36:39], s68 offen offset:1024 sc1 lds\n"
    "s_waitcnt vmcnt(8)\n"
    "s_branch L_KV_PUBA%=\n"
    "L_KV_NOIA%=:\n"
    "s_waitcnt vmcnt(0)\n"
    "L_KV_PUBA%=:\n"
    "s_barrier\n"
    "buffer_store_dword v12, v5, s[36:39], 0 offen sc1\n"
    "s_cmp_lt_u32 s60, 64\n"
    "s_cbranch_scc1 L_KV_LOOPA%=\n"
    "s_branch L_KV_END%=\n"
    "L_KV_WAf%=:\n"
    "s_nop 0\n"
    "s_cmp_lg_u32 s65, 0\n"
    "s_cbranch_scc1 L_KV_WAok%=\n"
    "s_add_u32 s64, s64, 1\n"
    "s_and_b32 s70, s64, 255\n"
    "s_cbranch_scc1 L_KV_WA%=\n"
    "buffer_load_dword v10, off, s[36:39], s50 sc1\n"
    "s_waitcnt vmcnt(0)\n"
    "v_readfirstlane_b32 s65, v10\n"
    "s_cmp_lt_u32 s64, 0x40000\n"
    "s_cbranch_scc1 L_KV_WA%=\n"
    "s_mov_b32 s65, 1\n"
    "v_mov_b32 v10, 1\n"
    "buffer_store_dword v10, off, s[36:39], s50 sc1\n"
    "s_branch L_KV_WA%=\n"
    "L_KV_LOOPB%=:\n"
    "s_lshr_b32 s69, s48, 8\n"
    "s_xor_b32 s69, s69, s52\n"
    "s_and_b32 s69, s69, 0x2000\n"
    "s_or_b32 s69, s69, s48\n"
    "s_add_u32 s48, s48, s49\n"
    "v_mov_b32 v12, s61\n"
    "s_cmp_eq_u32 s17, 0\n"
    "s_cbranch_scc1 L_KV_WBok%=\n"
    "v_cmp_gt_i32 vcc, s71, v8\n"
    "s_cbranch_vccz L_KV_WBok%=\n"
    "L_KV_WB%=:\n"
    "buffer_load_dword v8, v4, s[36:39], 0 offen sc1\n"
    "s_waitcnt vmcnt(0)\n"
    "v_cmp_gt_i32 vcc, s71, v8\n"
    "s_cbranch_vccnz L_KV_WBf%=\n"
    "L_KV_WBok%=:\n"
    "s_cmp_lg_u32 s63, 0\n"
    "s_barrier\n"
    "s_cbranch_scc1 L_KV_HAVEB%=\n"
    "buffer_load_dwordx4 v2, s[36:39], s68 offen sc1 lds\n"
    "buffer_load_dwordx4 v2, s[36:39], s68 offen offset:1024 sc1 lds\n"
    "s_add_u32 s68, s68, 0x4000\n"
    "s_add_u32 m0, s66, 8192\n"
    "s_nop 0\n"
    "buffer_load_dwordx4 v2, s[36:39], s68 offen sc1 lds\n"
    "buffer_load_dwordx4 v2, s[36:39], s68 offen offset:1024 sc1 lds\n"
    "s_add_u32 s68, s68, 0x4000\n"
    "s_add_u32 m0, s66, 16384\n"
    "s_nop 0\n"
    "buffer_load_dwordx4 v2, s[36:39], s68 offen sc1 lds\n"
    "buffer_load_dwordx4 v2, s[36:39], s68 offen offset:1024 sc1 lds\n"
    "s_add_u32 s68, s68, 0x4000\n"
    "s_add_u32 m0, s66, 24576\n"
    "s_nop 0\n"
    "buffer_load_dwordx4 v2, s[36:39], s68 offen sc1 lds\n"
    "buffer_load_dwordx4 v2, s[36:39], s68 offen offset:1024 sc1 lds\n"
    "buffer_load_dword v10, v13, s[36:39], 0 offen\n"
    "L_KV_HAVEB%=:\n"
    "s_waitcnt vmcnt(7)\n"
    "s_barrier\n"
    "ds_read_b128 v[128:131], v11 offset:0\n"
    "ds_read_b128 v[132:135], v11 offset:128\n"
    "ds_read_b128 v[136:139], v11 offset:2048\n"
    "ds_read_b128 v[140:143], v11 offset:2176\n"
    "s_waitcnt lgkmcnt(3)\n"
    "v_mfma_f32_16x16x32_bf16 v[32:35], v[128:131], a[0:3], 0\n"
    "s_waitcnt lgkmcnt(2)\n"
    "v_mfma_f32_16x16x32_bf16 v[36:39], v[132:135], a[0:3], 0\n"
    "v_mfma_f32_16x16x32_bf16 v[44:47], v[132:135], a[4:7], 0\n"
    "v_mfma_f32_16x16x32_bf16 v[40:43], v[128:131], a[4:7], 0\n"
    "v_mfma_f32_16x16x32_bf16 v[48:51], v[128:131], a[128:131], 0\n"
    "v_mfma_f32_16x16x32_bf16 v[52:55], v[132:135], a[128:131], 0\n"
    "v_mfma_f32_16x16x32_bf16 v[60:63], v[132:135], a[132:135], 0\n"
    "ds_read_b128 v[144:147], v11 offset:4096\n"
    "v_mfma_f32_16x16x32_bf16 v[56:59], v[128:131], a[132:135], 0\n"
    "ds_read_b128 v[148:151], v11 offset:4224\n"
    "s_waitcnt lgkmcnt(3)\n"
    "v_mfma_f32_16x16x32_bf16 v[32:35], v[136:139], a[8:11], v[32:35]\n"
    "s_waitcnt lgkmcnt(2)\n"
    "v_mfma_f32_16x16x32_bf16 v[36:39], v[140:143], a[8:11], v[36:39]\n"
    "v_mfma_f32_16x16x32_bf16 v[44:47], v[140:143], a[12:15], v[44:47]\n"
    "v_mfma_f32_16x16x32_bf16 v[40:43], v[136:139], a[12:15], v[40:43]\n"
    "v_mfma_f32_16x16x32_bf16 v[48:51], v[136:139], a[136:139], v[48:51]\n"
    "v_mfma_f32_16x16x32_bf16 v[52:55], v[140:143], a[136:139], v[52:55]\n"
    "v_mfma_f32_16x16x32_bf16 v[60:63], v[140:143], a[140:143], v[60:63]\n"
    "ds_read_b128 v[152:155], v11 offset:6144\n"
    "v_mfma_f32_16x16x32_bf16 v[56:59], v[136:139], a[140:143], v[56:59]\n"
    "ds_read_b128 v[156:159], v11 offset:6272\n"
    "s_waitcnt lgkmcnt(3)\n"
    "v_mfma_f32_16x16x32_bf16 v[32:35], v[144:147], a[16:19], v[32:35]\n"
    "s_waitcnt lgkmcnt(2)\n"
    "v_mfma_f32_16x16x32_bf16 v[36:39], v[148:151], a[16:19], v[36:39]\n"
    "v_mfma_f32_16x16x32_bf16 v[44:47], v[148:151], a[20:23], v[44:47]\n"
    "v_mfma_f32_16x16x32_bf16 v[40:43], v[144:147], a[20:23], v[40:43]\n"
    "v_mfma_f32_16x16x32_bf16 v[48:51], v[144:147], a[144:147], v[48:51]\n"
    "v_mfma_f32_16x16x32_bf16 v[52:55], v[148:151], a[144:147], v[52:55]\n"
    "v_mfma_f32_16x16x32_bf16 v[60:63], v[148:151], a[148:151], v[60:63]\n"
    "s_waitcnt vmcnt(5)\n"
    "s_barrier\n"
    "ds_read_b128 v[160:163], v11 offset:8192\n"
    "v_mfma_f32_16x16x32_bf16 v[56:59], v[144:147], a[148:151], v[56:59]\n"
    "ds_read_b128 v[164:167], v11 offset:8320\n"
    "s_waitcnt lgkmcnt(3)\n"
    "v_mfma_f32_16x16x32_bf16 v[32:35], v[152:155], a[24:27], v[32:35]\n"
    "s_waitcnt lgkmcnt(2)\n"
    "v_mfma_f32_16x16x32_bf16 v[36:39], v[156:159], a[24:27], v[36:39]\n"
    "v_mfma_f32_16x16x32_bf16 v[44:47], v[156:159], a[28:31], v[44:47]\n"
    "v_mfma_f32_16x16x32_bf16 v[40:43], v[152:155], a[28:31], v[40:43]\n"
    "v_mfma_f32_16x16x32_bf16 v[48:51], v[152:155], a[152:155], v[48:51]\n"
    "v_mfma_f32_16x16x32_bf16 v[52:55], v[156:159], a[152:155], v[52:55]\n"
    "v_mfma_f32_16x16x32_bf16 v[60:63], v[156:159], a[156:159], v[60:63]\n"
    "ds_read_b128 v[168:171], v11 offset:10240\n"
    "v_mfma_f32_16x16x32_bf16 v[56:59], v[152:155], a[156:159], v[56:59]\n"
    "ds_read_b128 v[172:175], v11 offset:10368\n"
    "s_waitcnt lgkmcnt(3)\n"
    "v_mfma_f32_16x16x32_bf16 v[32:35], v[160:163], a[32:35], v[32:35]\n"
    "s_waitcnt lgkmcnt(2)\n"
    "v_mfma_f32_16x16x32_bf16 v[36:39], v[164:167], a[32:35], v[36:39]\n"
    "v_mfma_f32_16x16x32_bf16 v[44:47], v[164:167], a[36:39], v[44:47]\n"
    "v_mfma_f32_16x16x32_bf16 v[40:43], v[160:163], a[36:39], v[40:43]\n"
    "v_mfma_f32_16x16x32_bf16 v[48:51], v[160:163], a[160:163], v[48:51]\n"
    "v_mfma_f32_16x16x32_bf16 v[52:55], v[164:167], a[160:163], v[52:55]\n"
    "v_mfma_f32_16x16x32_bf16 v[60:63], v[164:167], a[164:167], v[60:63]\n"
    "ds_read_b128 v[176:179], v11 offset:12288\n"
    "v_mfma_f32_16x16x32_bf16 v[56:59], v[160:163], a[164:167], v[56:59]\n"
    "ds_read_b128 v[180:183], v11 offset:12416\n"
    "s_waitcnt lgkmcnt(3)\n"
    "v_mfma_f32_16x16x32_bf16 v[32:35], v[168:171], a[40:43], v[32:35]\n"
    "s_waitcnt lgkmcnt(2)\n"
    "v_mfma_f32_16x16x32_bf16 v[36:39], v[172:175], a[40:43], v[36:39]\n"
    "v_mfma_f32_16x16x32_bf16 v[44:47], v[172:175], a[44:47], v[44:47]\n"
    "v_mfma_f32_16x16x32_bf16 v[40:43], v[168:171], a[44:47], v[40:43]\n"
    "v_mfma_f32_16x16x32_bf16 v[48:51], v[168:171], a[168:171], v[48:51]\n"
    "v_mfma_f32_16x16x32_bf16 v[52:55], v[172:175], a[168:171], v[52:55]\n"
    "v_mfma_f32_16x16x32_bf16 v[60:63], v[172:175], a[172:175], v[60:63]\n"
    "ds_read_b128 v[184:187], v11 offset:14336\n"
    "v_mfma_f32_16x16x32_bf16 v[56:59], v[168:171], a[172:175], v[56:59]\n"
    "ds_read_b128 v[188:191], v11 offset:14464\n"
    "s_waitcnt lgkmcnt(3)\n"
    "v_mfma_f32_16x16x32_bf16 v[32:35], v[176:179], a[48:51], v[32:35]\n"
    "s_waitcnt lgkmcnt(2)\n"
    "v_mfma_f32_16x16x32_bf16 v[36:39], v[180:183], a[48:51], v[36:39]\n"
    "v_mfma_f32_16x16x32_bf16 v[44:47], v[180:183], a[52:55], v[44:47]\n"
    "v_mfma_f32_16x16x32_bf16 v[40:43], v[176:179], a[52:55], v[40:43]\n"
    "v_mfma_f32_16x16x32_bf16 v[48:51], v[176:179], a[176:179], v[48:51]\n"
    "v_mfma_f32_16x16x32_bf16 v[52:55], v[180:183], a[176:179], v[52:55]\n"
    "v_mfma_f32_16x16x32_bf16 v[60:63], v[180:183], a[180:183], v[60:63]\n"
    "s_waitcnt vmcnt(3)\n"
    "s_barrier\n"
    "ds_read_b128 v[192:195], v11 offset:16384\n"
    "v_mfma_f32_16x16x32_bf16 v[56:59], v[176:179], a[180:183], v[56:59]\n"
    "ds_read_b128 v[196:199], v11 offset:16512\n"
    "s_waitcnt lgkmcnt(3)\n"
    "v_mfma_f32_16x16x32_bf16 v[32:35], v[184:187], a[56:59], v[32:35]\n"
    "s_waitcnt lgkmcnt(2)\n"
    "v_mfma_f32_16x16x32_bf16 v[36:39], v[188:191], a[56:59], v[36:39]\n"
    "v_mfma_f32_16x16x32_bf16 v[44:47], v[188:191], a[60:63], v[44:47]\n"
    "v_mfma_f32_16x16x32_bf16 v[40:43], v[184:187], a[60:63], v[40:43]\n"
    "v_mfma_f32_16x16x32_bf16 v[48:51], v[184:187], a[184:187], v[48:51]\n"
    "v_mfma_f32_16x16x32_bf16 v[52:55], v[188:191], a[184:187], v[52:55]\n"
    "v_mfma_f32_16x16x32_bf16 v[60:63], v[188:191], a[188:191], v[60:63]\n"
    "ds_read_b128 v[200:203], v11 offset:18432\n"
    "v_mfma_f32_16x16x32_bf16 v[56:59], v[184:187], a[188:191], v[56:59]\n"
    "ds_read_b128 v[204:207], v11 offset:18560\n"
    "s_waitcnt lgkmcnt(3)\n"
    "v_mfma_f32_16x16x32_bf16 v[32:35], v[192:195], a[64:67], v[32:35]\n"
    "s_waitcnt lgkmcnt(2)\n"
    "v_mfma_f32_16x16x32_bf16 v[36:39], v[196:199], a[64:67], v[36:39]\n"
    "v_mfma_f32_16x16x32_bf16 v[44:47], v[196:199], a[68:71], v[44:47]\n"
    "v_mfma_f32_16x16x32_bf16 v[40:43], v[192:195], a[68:71], v[40:43]\n"
    "v_mfma_f32_16x16x32_bf16 v[48:51], v[192:195], a[192:195], v[48:51]\n"
    "v_mfma_f32_16x16x32_bf16 v[52:55], v[196:199], a[192:195], v[52:55]\n"
    "v_mfma_f32_16x16x32_bf16 v[60:63], v[196:199], a[196:199], v[60:63]\n"
    "ds_read_b128 v[208:211], v11 offset:20480\n"
    "v_mfma_f32_16x16x32_bf16 v[56:59], v[192:195], a[196:199], v[56:59]\n"
    "ds_read_b128 v[212:215], v11 offset:20608\n"
    "s_waitcnt lgkmcnt(3)\n"
    "v_mfma_f32_16x16x32_bf16 v[32:35], v[200:203], a[72:75], v[32:35]\n"
    "s_waitcnt lgkmcnt(2)\n"
    "v_mfma_f32_16x16x32_bf16 v[36:39], v[204:207], a[72:75], v[36:39]\n"
    "v_mfma_f32_16x16x32_bf16 v[44:47], v[204:207], a[76:79], v[44:47]\n"
    "v_mfma_f32_16x16x32_bf16 v[40:43], v[200:203], a[76:79], v[40:43]\n"
    "v_mfma_f32_16x16x32_bf16 v[48:51], v[200:203], a[200:203], v[48:51]\n"
    "v_mfma_f32_16x16x32_bf16 v[52:55], v[204:207], a[200:203], v[52:55]\n"
    "v_mfma_f32_16x16x32_bf16 v[60:63], v[204:207], a[204:207], v[60:63]\n"
    "ds_read_b128 v[216:219], v11 offset:22528\n"
    "v_mfma_f32_16x16x32_bf16 v[56:59], v[200:203], a[204:207], v[56:59]\n"
    "ds_read_b128 v[220:223], v11 offset:22656\n"
    "s_waitcnt lgkmcnt(3)\n"
    "v_mfma_f32_16x16x32_bf16 v[32:35], v[208:211], a[80:83], v[32:35]\n"
    "s_lshr_b32 s67, s56, 8\n"
    "s_xor_b32 s67, s67, s52\n"
    "s_waitcnt lgkmcnt(2)\n"
    "v_mfma_f32_16x16x32_bf16 v[36:39], v[212:215], a[80:83], v[36:39]\n"
    "s_and_b32 s67, s67, 0x2000\n"
    "s_or_b32 s67, s67, s56\n"
    "v_mfma_f32_16x16x32_bf16 v[44:47], v[212:215], a[84:87], v[44:47]\n"
    "v_mfma_f32_16x16x32_bf16 v[40:43], v[208:211], a[84:87], v[40:43]\n"
    "v_mfma_f32_16x16x32_bf16 v[48:51], v[208:211], a[208:211], v[48:51]\n"
    "buffer_store_dwordx4 v[20:23], v15, s[36:39], s67 offen sc1\n"
    "v_mfma_f32_16x16x32_bf16 v[52:55], v[212:215], a[208:211], v[52:55]\n"
    "v_mfma_f32_16x16x32_bf16 v[60:63], v[212:215], a[212:215], v[60:63]\n"
    "s_waitcnt vmcnt(2)\n"
    "s_barrier\n"
    "ds_read_b128 v[224:227], v11 offset:24576\n"
    "v_mfma_f32_16x16x32_bf16 v[56:59], v[208:211], a[212:215], v[56:59]\n"
    "ds_read_b128 v[228:231], v11 offset:24704\n"
    "s_waitcnt lgkmcnt(3)\n"
    "v_mfma_f32_16x16x32_bf16 v[32:35], v[216:219], a[88:91], v[32:35]\n"
    "s_xor_b32 s67, s67, 0x40000\n"
    "s_add_u32 s56, s56, 0x80000\n"
    "s_waitcnt lgkmcnt(2)\n"
    "v_mfma_f32_16x16x32_bf16 v[36:39], v[220:223], a[88:91], v[36:39]\n"
    "v_mfma_f32_16x16x32_bf16 v[44:47], v[220:223], a[92:95], v[44:47]\n"
    "v_mfma_f32_16x16x32_bf16 v[40:43], v[216:219], a[92:95], v[40:43]\n"
    "v_mfma_f32_16x16x32_bf16 v[48:51], v[216:219], a[216:219], v[48:51]\n"
    "buffer_store_dwordx4 v[20:23], v15, s[36:39], s67 offen sc1\n"
    "v_mfma_f32_16x16x32_bf16 v[52:55], v[220:223], a[216:219], v[52:55]\n"
    "v_mfma_f32_16x16x32_bf16 v[60:63], v[220:223], a[220:223], v[60:63]\n"
    "ds_read_b128 v[232:235], v11 offset:26624\n"
    "v_mfma_f32_16x16x32_bf16 v[56:59], v[216:219], a[220:223], v[56:59]\n"
    "ds_read_b128 v[236:239], v11 offset:26752\n"
    "s_waitcnt lgkmcnt(3)\n"
    "v_mfma_f32_16x16x32_bf16 v[32:35], v[224:227], a[96:99], v[32:35]\n"
    "ds_read_b128 v[240:243], v11 offset:28672\n"
    "s_waitcnt lgkmcnt(3)\n"
    "v_mfma_f32_16x16x32_bf16 v[36:39], v[228:231], a[96:99], v[36:39]\n"
    "ds_read_b128 v[244:247], v11 offset:28800\n"
    "v_mfma_f32_16x16x32_bf16 v[44:47], v[228:231], a[100:103], v[44:47]\n"
    "ds_read_b128 v[132:135], v11 offset:30720\n"
    "v_mfma_f32_16x16x32_bf16 v[40:43], v[224:227], a[100:103], v[40:43]\n"
    "ds_read_b128 v[128:131], v11 offset:30848\n"
    "v_mfma_f32_16x16x32_bf16 v[48:51], v[224:227], a[224:227], v[48:51]\n"
    "buffer_load_dword v9, v4, s[36:39], 0 offen sc1\n"
    "v_mfma_f32_16x16x32_bf16 v[52:55], v[228:231], a[224:227], v[52:55]\n"
    "v_mfma_f32_16x16x32_bf16 v[60:63], v[228:231], a[228:231], v[60:63]\n"
    "v_mfma_f32_16x16x32_bf16 v[56:59], v[224:227], a[228:231], v[56:59]\n"
    "s_waitcnt lgkmcnt(5)\n"
    "v_mfma_f32_16x16x32_bf16 v[32:35], v[232:235], a[104:107], v[32:35]\n"
    "s_waitcnt lgkmcnt(4)\n"
    "v_mfma_f32_16x16x32_bf16 v[36:39], v[236:239], a[104:107], v[36:39]\n"
    "s_waitcnt lgkmcnt(3)\n"
    "v_mfma_f32_16x16x32_bf16 v[32:35], v[240:243], a[112:115], v[32:35]\n"
    "s_waitcnt lgkmcnt(2)\n"
    "v_mfma_f32_16x16x32_bf16 v[36:39], v[244:247], a[112:115], v[36:39]\n"
    "s_waitcnt lgkmcnt(1)\n"
    "v_mfma_f32_16x16x32_bf16 v[32:35], v[132:135], a[120:123], v[32:35]\n"
    "s_waitcnt lgkmcnt(0)\n"
    "v_mfma_f32_16x16x32_bf16 v[36:39], v[128:131], a[120:123], v[36:39]\n"
    "v_mfma_f32_16x16x32_bf16 v[48:51], v[232:235], a[232:235], v[48:51]\n"
    "v_mfma_f32_16x16x32_bf16 v[52:55], v[236:239], a[232:235], v[52:55]\n"
    "v_cvt_pk_bf16_f32 v96, v32, v33\n"
    "v_cvt_pk_bf16_f32 v97, v34, v35\n"
    "v_mfma_f32_16x16x32_bf16 v[48:51], v[240:243], a[240:243], v[48:51]\n"
    "v_cvt_pk_bf16_f32 v98, v36, v37\n"
    "v_cvt_pk_bf16_f32 v99, v38, v39\n"
    "v_mfma_f32_16x16x32_bf16 v[52:55], v[244:247], a[240:243], v[52:55]\n"
    "buffer_store_dwordx4 v[96:99], v6, s[36:39], s69 offen offset:0 sc1\n"
    "v_mfma_f32_16x16x32_bf16 v[48:51], v[132:135], a[248:251], v[48:51]\n"
    "v_mfma_f32_16x16x32_bf16 v[52:55], v[128:131], a[248:251], v[52:55]\n"
    "v_mfma_f32_16x16x32_bf16 v[40:43], v[232:235], a[108:111], v[40:43]\n"
    "v_mfma_f32_16x16x32_bf16 v[44:47], v[236:239], a[108:111], v[44:47]\n"
    "v_cvt_pk_bf16_f32 v100, v48, v49\n"
    "v_cvt_pk_bf16_f32 v101, v50, v51\n"
    "v_mfma_f32_16x16x32_bf16 v[40:43], v[240:243], a[116:119], v[40:43]\n"
    "v_cvt_pk_bf16_f32 v102, v52, v53\n"
    "v_cvt_pk_bf16_f32 v103, v54, v55\n"
    "v_mfma_f32_16x16x32_bf16 v[44:47], v[244:247], a[116:119], v[44:47]\n"
    "buffer_store_dwordx4 v[100:103], v6, s[36:39], s69 offen offset:2048 sc1\n"
    "v_mfma_f32_16x16x32_bf16 v[40:43], v[132:135], a[124:127], v[40:43]\n"
    "v_mfma_f32_16x16x32_bf16 v[44:47], v[128:131], a[124:127], v[44:47]\n"
    "v_mfma_f32_16x16x32_bf16 v[56:59], v[232:235], a[236:239], v[56:59]\n"
    "v_mfma_f32_16x16x32_bf16 v[60:63], v[236:239], a[236:239], v[60:63]\n"
    "v_cvt_pk_bf16_f32 v104, v40, v41\n"
    "v_cvt_pk_bf16_f32 v105, v42, v43\n"
    "v_mfma_f32_16x16x32_bf16 v[56:59], v[240:243], a[244:247], v[56:59]\n"
    "v_cvt_pk_bf16_f32 v106, v44, v45\n"
    "v_cvt_pk_bf16_f32 v107, v46, v47\n"
    "v_mfma_f32_16x16x32_bf16 v[60:63], v[244:247], a[244:247], v[60:63]\n"
    "buffer_store_dwordx4 v[104:107], v6, s[36:39], s69 offen offset:256 sc1\n"
    "v_mfma_f32_16x16x32_bf16 v[56:59], v[132:135], a[252:255], v[56:59]\n"
    "v_mfma_f32_16x16x32_bf16 v[60:63], v[128:131], a[252:255], v[60:63]\n"
    "s_nop 3\n"
    "v_cvt_pk_bf16_f32 v108, v56, v57\n"
    "v_cvt_pk_bf16_f32 v109, v58, v59\n"
    "s_nop 1\n"
    "v_cvt_pk_bf16_f32 v110, v60, v61\n"
    "v_cvt_pk_bf16_f32 v111, v62, v63\n"
    "buffer_store_dwordx4 v[108:111], v6, s[36:39], s69 offen offset:2304 sc1\n"
    "s_add_u32 s60, s60, 1\n"
    "s_add_u32 s61, s61, 1\n"
    "s_xor_b32 s62, s62, 0x10000\n"
    "s_mov_b32 s63, 0\n"
    "s_add_u32 s71, s71, 1\n"
    "s_cmp_eq_u32 s60, 64\n"
    "s_cselect_b32 s71, 1000, s71\n"
    "s_lshr_b32 s68, s46, 8\n"
    "s_xor_b32 s68, s68, s51\n"
    "s_and_b32 s68, s68, 0x2000\n"
    "s_or_b32 s68, s68, s46\n"
    "s_add_u32 s66, s53, s62\n"
    "s_mov_b32 m0, s66\n"
    "v_add_u32 v11, s62, v3\n"
    "s_add_u32 s46, s46, s47\n"
    "s_waitcnt vmcnt(4)\n"
    "v_max_u32 v8, v8, v9\n"
    "v_cmp_gt_i32 vcc, s71, v8\n"
    "s_cbranch_vccnz L_KV_NOIB%=\n"
    "s_mov_b32 s63, 1\n"
    "buffer_load_dwordx4 v2, s[36:39], s68 offen sc1 lds\n"
    "buffer_load_dwordx4 v2, s[36:39], s68 offen offset:1024 sc1 lds\n"
    "s_add_u32 s68, s68, 0x4000\n"
    "s_add_u32 m0, s66, 8192\n"
    "s_nop 0\n"
    "buffer_load_dwordx4 v2, s[36:39], s68 offen sc1 lds\n"
    "buffer_load_dwordx4 v2, s[36:39], s68 offen offset:1024 sc1 lds\n"
    "s_add_u32 s68, s68, 0x4000\n"
    "s_add_u32 m0, s66, 16384\n"
    "s_nop 0\n"
    "buffer_load_dwordx4 v2, s[36:39], s68 offen sc1 lds\n"
    "buffer_load_dwordx4 v2, s[36:39], s68 offen offset:1024 sc1 lds\n"
    "s_add_u32 s68, s68, 0x4000\n"
    "s_add_u32 m0, s66, 24576\n"
    "s_nop 0\n"
    "buffer_load_dwordx4 v2, s[36:39], s68 offen sc1 lds\n"
    "buffer_load_dwordx4 v2, s[36:39], s68 offen offset:1024 sc1 lds\n"
    "s_waitcnt vmcnt(8)\n"
    "s_branch L_KV_PUBB%=\n"
    "L_KV_NOIB%=:\n"
    "s_waitcnt vmcnt(0)\n"
    "L_KV_PUBB%=:\n"
    "s_barrier\n"
    "buffer_store_dword v12, v5, s[36:39], 0 offen sc1\n"
    "s_cmp_lt_u32 s60, 64\n"
    "s_cbranch_scc1 L_KV_LOOPB%=\n"
    "s_branch L_KV_END%=\n"
    "L_KV_WBf%=:\n"
    "s_nop 0\n"
    "s_cmp_lg_u32 s65, 0\n"
    "s_cbranch_scc1 L_KV_WBok%=\n"
    "s_add_u32 s64, s64, 1\n"
    "s_and_b32 s70, s64, 255\n"
    "s_cbranch_scc1 L_KV_WB%=\n"
    "buffer_load_dword v10, off, s[36:39], s50 sc1\n"
    "s_waitcnt vmcnt(0)\n"
    "v_readfirstlane_b32 s65, v10\n"
    "s_cmp_lt_u32 s64, 0x40000\n"
    "s_cbranch_scc1 L_KV_WB%=\n"
    "s_mov_b32 s65, 1\n"
    "v_mov_b32 v10, 1\n"
    "buffer_store_dword v10, off, s[36:39], s50 sc1\n"
    "s_branch L_KV_WB%=\n"
    "L_KV_REDO%=:\n"
    "s_lshl_b32 s70, s70, 11\n"
    "s_lshl_b32 s71, s44, 6\n"
    "s_sub_i32 s70, s70, s71\n"
    "s_ashr_i32 s71, s70, 31\n"
    "s_add_u32 s40, s40, s70\n"
    "s_addc_u32 s41, s41, s71\n"
    "s_branch L_KV_PRO%=\n"
    "L_KV_END%=:\n"
    "s_waitcnt vmcnt(0)\n"
    "L_KV_FIN:\ns_endpgm\n"
      GEMM_OPS, "{s[74:75]}"(v2u{blockIdx.x, c.slot >> 5}) : ROLE_CLOB);
  } else if constexpr (KIND == 1) {
    asm volatile("s_mov_b64 exec, -1\ns_and_b32 s37, s37, 0xffff\ns_mov_b32 s39, 0x20000\ns_nop 4\n"
    "s_mov_b32 s64, 0\n"
    "s_mov_b32 s65, 0\n"
    "s_add_u32 m0, s45, 0\n"
    "s_nop 0\n"
    "global_load_lds_dwordx4 v0, s[40:41] \n"
    "s_add_u32 s40, s40, s44\n"
    "s_addc_u32 s41, s41, 0\n"
    "s_add_u32 m0, s45, 1024\n"
    "s_nop 0\n"
    "global_load_lds_dwordx4 v0, s[40:41] \n"
    "s_add_u32 s40, s40, s44\n"
    "s_addc_u32 s41, s41, 0\n"
    "s_add_u32 m0, s45, 2048\n"
    "s_nop 0\n"
    "global_load_lds_dwordx4 v0, s[40:41] \n"
    "s_add_u32 s40, s40, s44\n"
    "s_addc_u32 s41, s41, 0\n"
    "s_add_u32 m0, s45, 3072\n"
    "s_nop 0\n"
    "global_load_lds_dwordx4 v0, s[40:41] \n"
    "s_add_u32 s40, s40, s44\n"
    "s_addc_u32 s41, s41, 0\n"
    "s_add_u32 m0, s45, 4096\n"
    "s_nop 0\n"
    "global_load_lds_dwordx4 v0, s[40:41] \n"
    "s_add_u32 s40, s40, s44\n"
    "s_addc_u32 s41, s41, 0\n"
    "s_add_u32 m0, s45, 5120\n"
    "s_nop 0\n"
    "global_load_lds_dwordx4 v0, s[40:41] \n"
    "s_add_u32 s40, s40, s44\n"
    "s_addc_u32 s41, s41, 0\n"
    "s_add_u32 m0, s45, 6144\n"
    "s_nop 0\n"
    "global_load_lds_dwordx4 v0, s[40:41] \n"
    "s_add_u32 s40, s40, s44\n"
    "s_addc_u32 s41, s41, 0\n"
    "s_add_u32 m0, s45, 7168\n"
    "s_nop 0\n"
    "global_load_lds_dwordx4 v0, s[40:41] \n"
    "s_add_u32 s40, s40, s44\n"
    "s_addc_u32 s41, s41, 0\n"
    "s_add_u32 m0, s45, 8192\n"
    "s_nop 0\n"
    "global_load_lds_dwordx4 v0, s[40:41] \n"
    "s_add_u32 s40, s40, s44\n"
    "s_addc_u32 s41, s41, 0\n"
    "s_add_u32 m0, s45, 9216\n"
    "s_nop 0\n"
    "global_load_lds_dwordx4 v0, s[40:41] \n"
    "s_add_u32 s40, s40, s44\n"
    "s_addc_u32 s41, s41, 0\n"
    "s_add_u32 m0, s45, 10240\n"
    "s_nop 0\n"
    "global_load_lds_dwordx4 v0, s[40:41] \n"
    "s_add_u32 s40, s40, s44\n"
    "s_addc_u32 s41, s41, 0\n"
    "s_add_u32 m0, s45, 11264\n"
    "s_nop 0\n"
    "global_load_lds_dwordx4 v0, s[40:41] \n"
    "s_add_u32 s40, s40, s44\n"
    "s_addc_u32 s41, s41, 0\n"
    "s_add_u32 m0, s45, 12288\n"
    "s_nop 0\n"
    "global_load_lds_dwordx4 v0, s[40:41] \n"
    "s_add_u32 s40, s40, s44\n"
    "s_addc_u32 s41, s41, 0\n"
    "s_add_u32 m0, s45, 13312\n"
    "s_nop 0\n"
    "global_load_lds_dwordx4 v0, s[40:41] \n"
    "s_add_u32 s40, s40, s44\n"
    "s_addc_u32 s41, s41, 0\n"
    "s_add_u32 m0, s45, 14336\n"
    "s_nop 0\n"
    "global_load_lds_dwordx4 v0, s[40:41] \n"
    "s_add_u32 s40, s40, s44\n"
    "s_addc_u32 s41, s41, 0\n"
    "s_add_u32 m0, s45, 15360\n"
    "s_nop 0\n"
    "global_load_lds_dwordx4 v0, s[40:41] \n"
    "s_add_u32 s40, s40, s44\n"
    "s_addc_u32 s41, s41, 0\n"
    "s_add_u32 m0, s45, 16384\n"
    "s_nop 0\n"
    "global_load_lds_dwordx4 v0, s[40:41] \n"
    "s_add_u32 s40, s40, s44\n"
    "s_addc_u32 s41, s41, 0\n"
    "s_add_u32 m0, s45, 17408\n"
    "s_nop 0\n"
    "global_load_lds_dwordx4 v0, s[40:41] \n"
    "s_add_u32 s40, s40, s44\n"
    "s_addc_u32 s41, s41, 0\n"
    "s_add_u32 m0, s45, 18432\n"
    "s_nop 0\n"
    "global_load_lds_dwordx4 v0, s[40:41] \n"
    "s_add_u32 s40, s40, s44\n"
    "s_addc_u32 s41, s41, 0\n"
    "s_add_u32 m0, s45, 19456\n"
    "s_nop 0\n"
    "global_load_lds_dwordx4 v0, s[40:41] \n"
    "s_add_u32 s40, s40, s44\n"
    "s_addc_u32 s41, s41, 0\n"
    "s_add_u32 m0, s45, 20480\n"
    "s_nop 0\n"
    "global_load_lds_dwordx4 v0, s[40:41] \n"
    "s_add_u32 s40, s40, s44\n"
    "s_addc_u32 s41, s41, 0\n"
    "s_add_u32 m0, s45, 21504\n"
    "s_nop 0\n"
    "global_load_lds_dwordx4 v0, s[40:41] \n"
    "s_add_u32 s40, s40, s44\n"
    "s_addc_u32 s41, s41, 0\n"
    "s_add_u32 m0, s45, 22528\n"
    "s_nop 0\n"
    "global_load_lds_dwordx4 v0, s[40:41] \n"
    "s_add_u32 s40, s40, s44\n"
    "s_addc_u32 s41, s41, 0\n"
    "s_add_u32 m0, s45, 23552\n"
    "s_nop 0\n"
    "global_load_lds_dwordx4 v0, s[40:41] \n"
    "s_add_u32 s40, s40, s44\n"
    "s_addc_u32 s41, s41, 0\n"
    "s_add_u32 m0, s45, 24576\n"
    "s_nop 0\n"
    "global_load_lds_dwordx4 v0, s[40:41] \n"
    "s_add_u32 s40, s40, s44\n"
    "s_addc_u32 s41, s41, 0\n"
    "s_add_u32 m0, s45, 25600\n"
    "s_nop 0\n"
    "global_load_lds_dwordx4 v0, s[40:41] \n"
    "s_add_u32 s40, s40, s44\n"
    "s_addc_u32 s41, s41, 0\n"
    "s_add_u32 m0, s45, 26624\n"
    "s_nop 0\n"
    "global_load_lds_dwordx4 v0, s[40:41] \n"
    "s_add_u32 s40, s40, s44\n"
    "s_addc_u32 s41, s41, 0\n"
    "s_add_u32 m0, s45, 27648\n"
    "s_nop 0\n"
    "global_load_lds_dwordx4 v0, s[40:41] \n"
    "s_add_u32 s40, s40, s44\n"
    "s_addc_u32 s41, s41, 0\n"
    "s_add_u32 m0, s45, 28672\n"
    "s_nop 0\n"
    "global_load_lds_dwordx4 v0, s[40:41] \n"
    "s_add_u32 s40, s40, s44\n"
    "s_addc_u32 s41, s41, 0\n"
    "s_add_u32 m0, s45, 29696\n"
    "s_nop 0\n"
    "global_load_lds_dwordx4 v0, s[40:41] \n"
    "s_add_u32 s40, s40, s44\n"
    "s_addc_u32 s41, s41, 0\n"
    "s_add_u32 m0, s45, 30720\n"
    "s_nop 0\n"
    "global_load_lds_dwordx4 v0, s[40:41] \n"
    "s_add_u32 s40, s40, s44\n"
    "s_addc_u32 s41, s41, 0\n"
    "s_add_u32 m0, s45, 31744\n"
    "s_nop 0\n"
    "global_load_lds_dwordx4 v0, s[40:41] \n"
    "s_add_u32 s40, s40, s44\n"
    "s_addc_u32 s41, s41, 0\n"
    "s_waitcnt vmcnt(28)\n"
    "ds_read_b64_tr_b16 a[0:1], v1 offset:0\n"
    "ds_read_b64_tr_b16 a[2:3], v1 offset:256\n"
    "ds_read_b64_tr_b16 a[4:5], v1 offset:8\n"
    "ds_read_b64_tr_b16 a[6:7], v1 offset:264\n"
    "ds_read_b64_tr_b16 a[8:9], v1 offset:2048\n"
    "ds_read_b64_tr_b16 a[10:11], v1 offset:2304\n"
    "ds_read_b64_tr_b16 a[12:13], v1 offset:2056\n"
    "ds_read_b64_tr_b16 a[14:15], v1 offset:2312\n"
    "s_waitcnt lgkmcnt(0)\n"
    "s_add_u32 m0, s45, 0\n"
    "s_nop 0\n"
    "global_load_lds_dwordx4 v0, s[42:43] \n"
    "s_add_u32 s42, s42, s44\n"
    "s_addc_u32 s43, s43, 0\n"
    "s_add_u32 m0, s45, 1024\n"
    "s_nop 0\n"
    "global_load_lds_dwordx4 v0, s[42:43] \n"
    "s_add_u32 s42, s42, s44\n"
    "s_addc_u32 s43, s43, 0\n"
    "s_add_u32 m0, s45, 2048\n"
    "s_nop 0\n"
    "global_load_lds_dwordx4 v0, s[42:43] \n"
    "s_add_u32 s42, s42, s44\n"
    "s_addc_u32 s43, s43, 0\n"
    "s_add_u32 m0, s45, 3072\n"
    "s_nop 0\n"
    "global_load_lds_dwordx4 v0, s[42:43] \n"
    "s_add_u32 s42, s42, s44\n"
    "s_addc_u32 s43, s43, 0\n"
    "s_waitcnt vmcnt(28)\n"
    "ds_read_b64_tr_b16 a[16:17], v1 offset:4096\n"
    "ds_read_b64_tr_b16 a[18:19], v1 offset:4352\n"
    "ds_read_b64_tr_b16 a[20:21], v1 offset:4104\n"
    "ds_read_b64_tr_b16 a[22:23], v1 offset:4360\n"
    "ds_read_b64_tr_b16 a[24:25], v1 offset:6144\n"
    "ds_read_b64_tr_b16 a[26:27], v1 offset:6400\n"
    "ds_read_b64_tr_b16 a[28:29], v1 offset:6152\n"
    "ds_read_b64_tr_b16 a[30:31], v1 offset:6408\n"
    "s_waitcnt lgkmcnt(0)\n"
    "s_add_u32 m0, s45, 4096\n"
    "s_nop 0\n"
    "global_load_lds_dwordx4 v0, s[42:43] \n"
    "s_add_u32 s42, s42, s44\n"
    "s_addc_u32 s43, s43, 0\n"
    "s_add_u32 m0, s45, 5120\n"
    "s_nop 0\n"
    "global_load_lds_dwordx4 v0, s[42:43] \n"
    "s_add_u32 s42, s42, s44\n"
    "s_addc_u32 s43, s43, 0\n"
    "s_add_u32 m0, s45, 6144\n"
    "s_nop 0\n"
    "global_load_lds_dwordx4 v0, s[42:43] \n"
    "s_add_u32 s42, s42, s44\n"
    "s_addc_u32 s43, s43, 0\n"
    "s_add_u32 m0, s45, 7168\n"
    "s_nop 0\n"
    "global_load_lds_dwordx4 v0, s[42:43] \n"
    "s_add_u32 s42, s42, s44\n"
    "s_addc_u32 s43, s43, 0\n"
    "s_waitcnt vmcnt(28)\n"
    "ds_read_b64_tr_b16 a[32:33], v1 offset:8192\n"
    "ds_read_b64_tr_b16 a[34:35], v1 offset:8448\n"
    "ds_read_b64_tr_b16 a[36:37], v1 offset:8200\n"
    "ds_read_b64_tr_b16 a[38:39], v1 offset:8456\n"
    "ds_read_b64_tr_b16 a[40:41], v1 offset:10240\n"
    "ds_read_b64_tr_b16 a[42:43], v1 offset:10496\n"
    "ds_read_b64_tr_b16 a[44:45], v1 offset:10248\n"
    "ds_read_b64_tr_b16 a[46:47], v1 offset:10504\n"
    "s_waitcnt lgkmcnt(0)\n"
    "s_add_u32 m0, s45, 8192\n"
    "s_nop 0\n"
    "global_load_lds_dwordx4 v0, s[42:43] \n"
    "s_add_u32 s42, s42, s44\n"
    "s_addc_u32 s43, s43, 0\n"
    "s_add_u32 m0, s45, 9216\n"
    "s_nop 0\n"
    "global_load_lds_dwordx4 v0, s[42:43] \n"
    "s_add_u32 s42, s42, s44\n"
    "s_addc_u32 s43, s43, 0\n"
    "s_add_u32 m0, s45, 10240\n"
    "s_nop 0\n"
    "global_load_lds_dwordx4 v0, s[42:43] \n"
    "s_add_u32 s42, s42, s44\n"
    "s_addc_u32 s43, s43, 0\n"
    "s_add_u32 m0, s45, 11264\n"
    "s_nop 0\n"
    "global_load_lds_dwordx4 v0, s[42:43] \n"
    "s_add_u32 s42, s42, s44\n"
    "s_addc_u32 s43, s43, 0\n"
    "s_waitcnt vmcnt(28)\n"
    "ds_read_b64_tr_b16 a[48:49], v1 offset:12288\n"
    "ds_read_b64_tr_b16 a[50:51], v1 offset:12544\n"
    "ds_read_b64_tr_b16 a[52:53], v1 offset:12296\n"
    "ds_read_b64_tr_b16 a[54:55], v1 offset:12552\n"
    "ds_read_b64_tr_b16 a[56:57], v1 offset:14336\n"
    "ds_read_b64_tr_b16 a[58:59], v1 offset:14592\n"
    "ds_read_b64_tr_b16 a[60:61], v1 offset:14344\n"
    "ds_read_b64_tr_b16 a[62:63], v1 offset:14600\n"
    "s_waitcnt lgkmcnt(0)\n"
    "s_add_u32 m0, s45, 12288\n"
    "s_nop 0\n"
    "global_load_lds_dwordx4 v0, s[42:43] \n"
    "s_add_u32 s42, s42, s44\n"
    "s_addc_u32 s43, s43, 0\n"
    "s_add_u32 m0, s45, 13312\n"
    "s_nop 0\n"
    "global_load_lds_dwordx4 v0, s[42:43] \n"
    "s_add_u32 s42, s42, s44\n"
    "s_addc_u32 s43, s43, 0\n"
    "s_add_u32 m0, s45, 14336\n"
    "s_nop 0\n"
    "global_load_lds_dwordx4 v0, s[42:43] \n"
    "s_add_u32 s42, s42, s44\n"
    "s_addc_u32 s43, s43, 0\n"
    "s_add_u32 m0, s45, 15360\n"
    "s_nop 0\n"
    "global_load_lds_dwordx4 v0, s[42:43] \n"
    "s_add_u32 s42, s42, s44\n"
    "s_addc_u32 s43, s43, 0\n"
    "s_waitcnt vmcnt(28)\n"
    "ds_read_b64_tr_b16 a[64:65], v1 offset:16384\n"
    "ds_read_b64_tr_b16 a[66:67], v1 offset:16640\n"
    "ds_read_b64_tr_b16 a[68:69], v1 offset:16392\n"
    "ds_read_b64_tr_b16 a[70:71], v1 offset:16648\n"
    "ds_read_b64_tr_b16 a[72:73], v1 offset:18432\n"
    "ds_read_b64_tr_b16 a[74:75], v1 offset:18688\n"
    "ds_read_b64_tr_b16 a[76:77], v1 offset:18440\n"
    "ds_read_b64_tr_b16 a[78:79], v1 offset:18696\n"
    "s_waitcnt lgkmcnt(0)\n"
    "s_add_u32 m0, s45, 16384\n"
    "s_nop 0\n"
    "global_load_lds_dwordx4 v0, s[42:43] \n"
    "s_add_u32 s42, s42, s44\n"
    "s_addc_u32 s43, s43, 0\n"
    "s_add_u32 m0, s45, 17408\n"
    "s_nop 0\n"
    "global_load_lds_dwordx4 v0, s[42:43] \n"
    "s_add_u32 s42, s42, s44\n"
    "s_addc_u32 s43, s43, 0\n"
    "s_add_u32 m0, s45, 18432\n"
    "s_nop 0\n"
    "global_load_lds_dwordx4 v0, s[42:43] \n"
    "s_add_u32 s42, s42, s44\n"
    "s_addc_u32 s43, s43, 0\n"
    "s_add_u32 m0, s45, 19456\n"
    "s_nop 0\n"
    "global_load_lds_dwordx4 v0, s[42:43] \n"
    "s_add_u32 s42, s42, s44\n"
    "s_addc_u32 s43, s43, 0\n"
    "s_waitcnt vmcnt(28)\n"
    "ds_read_b64_tr_b16 a[80:81], v1 offset:20480\n"
    "ds_read_b64_tr_b16 a[82:83], v1 offset:20736\n"
    "ds_read_b64_tr_b16 a[84:85], v1 offset:20488\n"
    "ds_read_b64_tr_b16 a[86:87], v1 offset:20744\n"
    "ds_read_b64_tr_b16 a[88:89], v1 offset:22528\n"
    "ds_read_b64_tr_b16 a[90:91], v1 offset:22784\n"
    "ds_read_b64_tr_b16 a[92:93], v1 offset:22536\n"
    "ds_read_b64_tr_b16 a[94:95], v1 offset:22792\n"
    "s_waitcnt lgkmcnt(0)\n"
    "s_add_u32 m0, s45, 20480\n"
    "s_nop 0\n"
    "global_load_lds_dwordx4 v0, s[42:43] \n"
    "s_add_u32 s42, s42, s44\n"
    "s_addc_u32 s43, s43, 0\n"
    "s_add_u32 m0, s45, 21504\n"
    "s_nop 0\n"
    "global_load_lds_dwordx4 v0, s[42:43] \n"
    "s_add_u32 s42, s42, s44\n"
    "s_addc_u32 s43, s43, 0\n"
    "s_add_u32 m0, s45, 22528\n"
    "s_nop 0\n"
    "global_load_lds_dwordx4 v0, s[42:43] \n"
    "s_add_u32 s42, s42, s44\n"
    "s_addc_u32 s43, s43, 0\n"
    "s_add_u32 m0, s45, 23552\n"
    "s_nop 0\n"
    "global_load_lds_dwordx4 v0, s[42:43] \n"
    "s_add_u32 s42, s42, s44\n"
    "s_addc_u32 s43, s43, 0\n"
    "s_waitcnt vmcnt(28)\n"
    "ds_read_b64_tr_b16 a[96:97], v1 offset:24576\n"
    "ds_read_b64_tr_b16 a[98:99], v1 offset:24832\n"
    "ds_read_b64_tr_b16 a[100:101], v1 offset:24584\n"
    "ds_read_b64_tr_b16 a[102:103], v1 offset:24840\n"
    "ds_read_b64_tr_b16 a[104:105], v1 offset:26624\n"
    "ds_read_b64_tr_b16 a[106:107], v1 offset:26880\n"
    "ds_read_b64_tr_b16 a[108:109], v1 offset:26632\n"
    "ds_read_b64_tr_b16 a[110:111], v1 offset:26888\n"
    "s_waitcnt lgkmcnt(0)\n"
    "s_add_u32 m0, s45, 24576\n"
    "s_nop 0\n"
    "global_load_lds_dwordx4 v0, s[42:43] \n"
    "s_add_u32 s42, s42, s44\n"
    "s_addc_u32 s43, s43, 0\n"
    "s_add_u32 m0, s45, 25600\n"
    "s_nop 0\n"
    "global_load_lds_dwordx4 v0, s[42:43] \n"
    "s_add_u32 s42, s42, s44\n"
    "s_addc_u32 s43, s43, 0\n"
    "s_add_u32 m0, s45, 26624\n"
    "s_nop 0\n"
    "global_load_lds_dwordx4 v0, s[42:43] \n"
    "s_add_u32 s42, s42, s44\n"
    "s_addc_u32 s43, s43, 0\n"
    "s_add_u32 m0, s45, 27648\n"
    "s_nop 0\n"
    "global_load_lds_dwordx4 v0, s[42:43] \n"
    "s_add_u32 s42, s42, s44\n"
    "s_addc_u32 s43, s43, 0\n"
    "s_waitcnt vmcnt(28)\n"
    "ds_read_b64_tr_b16 a[112:113], v1 offset:28672\n"
    "ds_read_b64_tr_b16 a[114:115], v1 offset:28928\n"
    "ds_read_b64_tr_b16 a[116:117], v1 offset:28680\n"
    "ds_read_b64_tr_b16 a[118:119], v1 offset:28936\n"
    "ds_read_b64_tr_b16 a[120:121], v1 offset:30720\n"
    "ds_read_b64_tr_b16 a[122:123], v1 offset:30976\n"
    "ds_read_b64_tr_b16 a[124:125], v1 offset:30728\n"
    "ds_read_b64_tr_b16 a[126:127], v1 offset:30984\n"
    "s_waitcnt lgkmcnt(0)\n"
    "s_add_u32 m0, s45, 28672\n"
    "s_nop 0\n"
    "global_load_lds_dwordx4 v0, s[42:43] \n"
    "s_add_u32 s42, s42, s44\n"
    "s_addc_u32 s43, s43, 0\n"
    "s_add_u32 m0, s45, 29696\n"
    "s_nop 0\n"
    "global_load_lds_dwordx4 v0, s[42:43] \n"
    "s_add_u32 s42, s42, s44\n"
    "s_addc_u32 s43, s43, 0\n"
    "s_add_u32 m0, s45, 30720\n"
    "s_nop 0\n"
    "global_load_lds_dwordx4 v0, s[42:43] \n"
    "s_add_u32 s42, s42, s44\n"
    "s_addc_u32 s43, s43, 0\n"
    "s_add_u32 m0, s45, 31744\n"
    "s_nop 0\n"
    "global_load_lds_dwordx4 v0, s[42:43] \n"
    "s_add_u32 s42, s42, s44\n"
    "s_addc_u32 s43, s43, 0\n"
    "s_waitcnt vmcnt(28)\n"
    "ds_read_b64_tr_b16 a[128:129], v1 offset:0\n"
    "ds_read_b64_tr_b16 a[130:131], v1 offset:256\n"
    "ds_read_b64_tr_b16 a[132:133], v1 offset:8\n"
    "ds_read_b64_tr_b16 a[134:135], v1 offset:264\n"
    "ds_read_b64_tr_b16 a[136:137], v1 offset:2048\n"
    "ds_read_b64_tr_b16 a[138:139], v1 offset:2304\n"
    "ds_read_b64_tr_b16 a[140:141], v1 offset:2056\n"
    "ds_read_b64_tr_b16 a[142:143], v1 offset:2312\n"
    "s_waitcnt vmcnt(24)\n"
    "ds_read_b64_tr_b16 a[144:145], v1 offset:4096\n"
    "ds_read_b64_tr_b16 a[146:147], v1 offset:4352\n"
    "ds_read_b64_tr_b16 a[148:149], v1 offset:4104\n"
    "ds_read_b64_tr_b16 a[150:151], v1 offset:4360\n"
    "ds_read_b64_tr_b16 a[152:153], v1 offset:6144\n"
    "ds_read_b64_tr_b16 a[154:155], v1 offset:6400\n"
    "ds_read_b64_tr_b16 a[156:157], v1 offset:6152\n"
    "ds_read_b64_tr_b16 a[158:159], v1 offset:6408\n"
    "s_waitcnt vmcnt(20)\n"
    "ds_read_b64_tr_b16 a[160:161], v1 offset:8192\n"
    "ds_read_b64_tr_b16 a[162:163], v1 offset:8448\n"
    "ds_read_b64_tr_b16 a[164:165], v1 offset:8200\n"
    "ds_read_b64_tr_b16 a[166:167], v1 offset:8456\n"
    "ds_read_b64_tr_b16 a[168:169], v1 offset:10240\n"
    "ds_read_b64_tr_b16 a[170:171], v1 offset:10496\n"
    "ds_read_b64_tr_b16 a[172:173], v1 offset:10248\n"
    "ds_read_b64_tr_b16 a[174:175], v1 offset:10504\n"
    "s_waitcnt vmcnt(16)\n"
    "ds_read_b64_tr_b16 a[176:177], v1 offset:12288\n"
    "ds_read_b64_tr_b16 a[178:179], v1 offset:12544\n"
    "ds_read_b64_tr_b16 a[180:181], v1 offset:12296\n"
    "ds_read_b64_tr_b16 a[182:183], v1 offset:12552\n"
    "ds_read_b64_tr_b16 a[184:185], v1 offset:14336\n"
    "ds_read_b64_tr_b16 a[186:187], v1 offset:14592\n"
    "ds_read_b64_tr_b16 a[188:189], v1 offset:14344\n"
    "ds_read_b64_tr_b16 a[190:191], v1 offset:14600\n"
    "s_waitcnt vmcnt(12)\n"
    "ds_read_b64_tr_b16 a[192:193], v1 offset:16384\n"
    "ds_read_b64_tr_b16 a[194:195], v1 offset:16640\n"
    "ds_read_b64_tr_b16 a[196:197], v1 offset:16392\n"
    "ds_read_b64_tr_b16 a[198:199], v1 offset:16648\n"
    "ds_read_b64_tr_b16 a[200:201], v1 offset:18432\n"
    "ds_read_b64_tr_b16 a[202:203], v1 offset:18688\n"
    "ds_read_b64_tr_b16 a[204:205], v1 offset:18440\n"
    "ds_read_b64_tr_b16 a[206:207], v1 offset:18696\n"
    "s_waitcnt vmcnt(8)\n"
    "ds_read_b64_tr_b16 a[208:209], v1 offset:20480\n"
    "ds_read_b64_tr_b16 a[210:211], v1 offset:20736\n"
    "ds_read_b64_tr_b16 a[212:213], v1 offset:20488\n"
    "ds_read_b64_tr_b16 a[214:215], v1 offset:20744\n"
    "ds_read_b64_tr_b16 a[216:217], v1 offset:22528\n"
    "ds_read_b64_tr_b16 a[218:219], v1 offset:22784\n"
    "ds_read_b64_tr_b16 a[220:221], v1 offset:22536\n"
    "ds_read_b64_tr_b16 a[222:223], v1 offset:22792\n"
    "s_waitcnt vmcnt(4)\n"
    "ds_read_b64_tr_b16 a[224:225], v1 offset:24576\n"
    "ds_read_b64_tr_b16 a[226:227], v1 offset:24832\n"
    "ds_read_b64_tr_b16 a[228:229], v1 offset:24584\n"
    "ds_read_b64_tr_b16 a[230:231], v1 offset:24840\n"
    "ds_read_b64_tr_b16 a[232:233], v1 offset:26624\n"
    "ds_read_b64_tr_b16 a[234:235], v1 offset:26880\n"
    "ds_read_b64_tr_b16 a[236:237], v1 offset:26632\n"
    "ds_read_b64_tr_b16 a[238:239], v1 offset:26888\n"
    "s_waitcnt vmcnt(0)\n"
    "ds_read_b64_tr_b16 a[240:241], v1 offset:28672\n"
    "ds_read_b64_tr_b16 a[242:243], v1 offset:28928\n"
    "ds_read_b64_tr_b16 a[244:245], v1 offset:28680\n"
    "ds_read_b64_tr_b16 a[246:247], v1 offset:28936\n"
    "ds_read_b64_tr_b16 a[248:249], v1 offset:30720\n"
    "ds_read_b64_tr_b16 a[250:251], v1 offset:30976\n"
    "ds_read_b64_tr_b16 a[252:253], v1 offset:30728\n"
    "ds_read_b64_tr_b16 a[254:255], v1 offset:30984\n"
    "s_waitcnt lgkmcnt(0)\n"
    "s_barrier\n"
    "v_mov_b32 v8, 0\n"
    "v_mov_b32 v13, 0x80000000\n"
    "s_sub_u32 s71, 1, s54\n"
    "s_mov_b32 s60, 0\n"
    "s_mov_b32 s61, 1\n"
    "s_mov_b32 s62, 0\n"
    "s_mov_b32 s63, 0\n"
    "s_mov_b32 s58, 0xbdd2d3e8\n"
    "v_mov_b32 v14, 0xc0135761\n"
    "s_bfe_u32 s17, s45, 0x2000f\n"
    "s_cmp_eq_u32 s17, 1\n"
    "s_cselect_b32 s17, 1, 0\n"
    "L_GU_GO%=:\n"
    "s_lshr_b32 s68, s46, 8\n"
    "s_xor_b32 s68, s68, s51\n"
    "s_and_b32 s68, s68, 0x2000\n"
    "s_or_b32 s68, s68, s46\n"
    "s_add_u32 s66, s53, s62\n"
    "s_mov_b32 m0, s66\n"
    "v_add_u32 v11, s62, v3\n"
    "s_add_u32 s46, s46, s47\n"
    "L_GU_LOOPA%=:\n"
    "s_lshr_b32 s69, s48, 8\n"
    "s_xor_b32 s69, s69, s52\n"
    "s_and_b32 s69, s69, 0x2000\n"
    "s_or_b32 s69, s69, s48\n"
    "s_add_u32 s48, s48, s49\n"
    "v_mov_b32 v12, s61\n"
    "s_cmp_eq_u32 s17, 0\n"
    "s_cbranch_scc1 L_GU_WAok%=\n"
    "v_cmp_gt_i32 vcc, s71, v8\n"
    "s_cbranch_vccz L_GU_WAok%=\n"
    "L_GU_WA%=:\n"
    "buffer_load_dword v8, v4, s[36:39], 0 offen sc1\n"
    "s_waitcnt vmcnt(0)\n"
    "v_cmp_gt_i32 vcc, s71, v8\n"
    "s_cbranch_vccnz L_GU_WAf%=\n"
    "L_GU_WAok%=:\n"
    "s_cmp_lg_u32 s63, 0\n"
    "s_barrier\n"
    "s_cbranch_scc1 L_GU_HAVEA%=\n"
    "buffer_load_dwordx4 v2, s[36:39], s68 offen sc1 lds\n"
    "buffer_load_dwordx4 v2, s[36:39], s68 offen offset:1024 sc1 lds\n"
    "s_add_u32 s68, s68, 0x4000\n"
    "s_add_u32 m0, s66, 8192\n"
    "s_nop 0\n"
    "buffer_load_dwordx4 v2, s[36:39], s68 offen sc1 lds\n"
    "buffer_load_dwordx4 v2, s[36:39], s68 offen offset:1024 sc1 lds\n"
    "s_add_u32 s68, s68, 0x4000\n"
    "s_add_u32 m0, s66, 16384\n"
    "s_nop 0\n"
    "buffer_load_dwordx4 v2, s[36:39], s68 offen sc1 lds\n"
    "buffer_load_dwordx4 v2, s[36:39], s68 offen offset:1024 sc1 lds\n"
    "s_add_u32 s68, s68, 0x4000\n"
    "s_add_u32 m0, s66, 24576\n"
    "s_nop 0\n"
    "buffer_load_dwordx4 v2, s[36:39], s68 offen sc1 lds\n"
    "buffer_load_dwordx4 v2, s[36:39], s68 offen offset:1024 sc1 lds\n"
    "buffer_load_dword v10, v13, s[36:39], 0 offen\n"
    "L_GU_HAVEA%=:\n"
    "s_waitcnt vmcnt(7)\n"
    "s_barrier\n"
    "ds_read_b128 v[128:131], v11 offset:0\n"
    "ds_read_b128 v[132:135], v11 offset:2048\n"
    "ds_read_b128 v[136:139], v11 offset:4096\n"
    "ds_read_b128 v[140:143], v11 offset:6144\n"
    "s_waitcnt lgkmcnt(3)\n"
    "v_mfma_f32_16x16x32_bf16 v[32:35], a[0:3], v[128:131], 0\n"
    "ds_read_b128 v[144:147], v11 offset:256\n"
    "v_mfma_f32_16x16x32_bf16 v[36:39], a[4:7], v[128:131], 0\n"
    "s_waitcnt lgkmcnt(3)\n"
    "v_mfma_f32_16x16x32_bf16 v[32:35], a[8:11], v[132:135], v[32:35]\n"
    "ds_read_b128 v[148:151], v11 offset:2304\n"
    "v_mfma_f32_16x16x32_bf16 v[36:39], a[12:15], v[132:135], v[36:39]\n"
    "s_waitcnt lgkmcnt(3)\n"
    "v_mfma_f32_16x16x32_bf16 v[32:35], a[16:19], v[136:139], v[32:35]\n"
    "ds_read_b128 v[152:155], v11 offset:4352\n"
    "v_mfma_f32_16x16x32_bf16 v[36:39], a[20:23], v[136:139], v[36:39]\n"
    "s_waitcnt lgkmcnt(3)\n"
    "v_mfma_f32_16x16x32_bf16 v[32:35], a[24:27], v[140:143], v[32:35]\n"
    "ds_read_b128 v[156:159], v11 offset:6400\n"
    "v_mfma_f32_16x16x32_bf16 v[36:39], a[28:31], v[140:143], v[36:39]\n"
    "s_waitcnt lgkmcnt(3)\n"
    "v_mfma_f32_16x16x32_bf16 v[40:43], a[0:3], v[144:147], 0\n"
    "s_waitcnt vmcnt(5)\n"
    "s_barrier\n"
    "ds_read_b128 v[160:163], v11 offset:8448\n"
    "v_mfma_f32_16x16x32_bf16 v[44:47], a[4:7], v[144:147], 0\n"
    "s_waitcnt lgkmcnt(3)\n"
    "v_mfma_f32_16x16x32_bf16 v[40:43], a[8:11], v[148:151], v[40:43]\n"
    "ds_read_b128 v[164:167], v11 offset:10496\n"
    "v_mfma_f32_16x16x32_bf16 v[44:47], a[12:15], v[148:151], v[44:47]\n"
    "s_waitcnt lgkmcnt(3)\n"
    "v_mfma_f32_16x16x32_bf16 v[40:43], a[16:19], v[152:155], v[40:43]\n"
    "ds_read_b128 v[168:171], v11 offset:8192\n"
    "v_mfma_f32_16x16x32_bf16 v[44:47], a[20:23], v[152:155], v[44:47]\n"
    "s_waitcnt lgkmcnt(3)\n"
    "v_mfma_f32_16x16x32_bf16 v[40:43], a[24:27], v[156:159], v[40:43]\n"
    "ds_read_b128 v[172:175], v11 offset:10240\n"
    "v_mfma_f32_16x16x32_bf16 v[44:47], a[28:31], v[156:159], v[44:47]\n"
    "s_waitcnt lgkmcnt(3)\n"
    "v_mfma_f32_16x16x32_bf16 v[40:43], a[32:35], v[160:163], v[40:43]\n"
    "ds_read_b128 v[176:179], v11 offset:12288\n"
    "v_mfma_f32_16x16x32_bf16 v[44:47], a[36:39], v[160:163], v[44:47]\n"
    "s_waitcnt lgkmcnt(3)\n"
    "v_mfma_f32_16x16x32_bf16 v[40:43], a[40:43], v[164:167], v[40:43]\n"
    "ds_read_b128 v[180:183], v11 offset:14336\n"
    "v_mfma_f32_16x16x32_bf16 v[44:47], a[44:47], v[164:167], v[44:47]\n"
    "s_waitcnt lgkmcnt(3)\n"
    "v_mfma_f32_16x16x32_bf16 v[32:35], a[32:35], v[168:171], v[32:35]\n"
    "s_waitcnt vmcnt(3)\n"
    "s_barrier\n"
    "ds_read_b128 v[184:187], v11 offset:16384\n"
    "v_mfma_f32_16x16x32_bf16 v[36:39], a[36:39], v[168:171], v[36:39]\n"
    "s_waitcnt lgkmcnt(3)\n"
    "v_mfma_f32_16x16x32_bf16 v[32:35], a[40:43], v[172:175], v[32:35]\n"
    "ds_read_b128 v[188:191], v11 offset:18432\n"
    "v_mfma_f32_16x16x32_bf16 v[36:39], a[44:47], v[172:175], v[36:39]\n"
    "s_waitcnt lgkmcnt(3)\n"
    "v_mfma_f32_16x16x32_bf16 v[32:35], a[48:51], v[176:179], v[32:35]\n"
    "ds_read_b128 v[192:195], v11 offset:20480\n"
    "v_mfma_f32_16x16x32_bf16 v[36:39], a[52:55], v[176:179], v[36:39]\n"
    "s_waitcnt lgkmcnt(3)\n"
    "v_mfma_f32_16x16x32_bf16 v[32:35], a[56:59], v[180:183], v[32:35]\n"
    "ds_read_b128 v[196:199], v11 offset:22528\n"
    "v_mfma_f32_16x16x32_bf16 v[36:39], a[60:63], v[180:183], v[36:39]\n"
    "s_waitcnt lgkmcnt(3)\n"
    "v_mfma_f32_16x16x32_bf16 v[32:35], a[64:67], v[184:187], v[32:35]\n"
    "s_waitcnt vmcnt(1)\n"
    "s_barrier\n"
    "ds_read_b128 v[200:203], v11 offset:24576\n"
    "v_mfma_f32_16x16x32_bf16 v[36:39], a[68:71], v[184:187], v[36:39]\n"
    "s_waitcnt lgkmcnt(3)\n"
    "v_mfma_f32_16x16x32_bf16 v[32:35], a[72:75], v[188:191], v[32:35]\n"
    "ds_read_b128 v[204:207], v11 offset:26624\n"
    "v_mfma_f32_16x16x32_bf16 v[36:39], a[76:79], v[188:191], v[36:39]\n"
    "s_waitcnt lgkmcnt(3)\n"
    "v_mfma_f32_16x16x32_bf16 v[32:35], a[80:83], v[192:195], v[32:35]\n"
    "ds_read_b128 v[208:211], v11 offset:28672\n"
    "v_mfma_f32_16x16x32_bf16 v[36:39], a[84:87], v[192:195], v[36:39]\n"
    "s_waitcnt lgkmcnt(3)\n"
    "v_mfma_f32_16x16x32_bf16 v[32:35], a[88:91], v[196:199], v[32:35]\n"
    "ds_read_b128 v[212:215], v11 offset:30720\n"
    "v_mfma_f32_16x16x32_bf16 v[36:39], a[92:95], v[196:199], v[36:39]\n"
    "s_waitcnt lgkmcnt(3)\n"
    "v_mfma_f32_16x16x32_bf16 v[32:35], a[96:99], v[200:203], v[32:35]\n"
    "ds_read_b128 v[216:219], v11 offset:12544\n"
    "v_mfma_f32_16x16x32_bf16 v[36:39], a[100:103], v[200:203], v[36:39]\n"
    "s_waitcnt lgkmcnt(3)\n"
    "v_mfma_f32_16x16x32_bf16 v[32:35], a[104:107], v[204:207], v[32:35]\n"
    "ds_read_b128 v[220:223], v11 offset:14592\n"
    "v_mfma_f32_16x16x32_bf16 v[36:39], a[108:111], v[204:207], v[36:39]\n"
    "s_waitcnt lgkmcnt(3)\n"
    "v_mfma_f32_16x16x32_bf16 v[32:35], a[112:115], v[208:211], v[32:35]\n"
    "ds_read_b128 v[224:227], v11 offset:16640\n"
    "v_mfma_f32_16x16x32_bf16 v[36:39], a[116:119], v[208:211], v[36:39]\n"
    "s_waitcnt lgkmcnt(3)\n"
    "v_mfma_f32_16x16x32_bf16 v[32:35], a[120:123], v[212:215], v[32:35]\n"
    "ds_read_b128 v[228:231], v11 offset:18688\n"
    "v_mfma_f32_16x16x32_bf16 v[36:39], a[124:127], v[212:215], v[36:39]\n"
    "s_waitcnt lgkmcnt(3)\n"
    "v_mfma_f32_16x16x32_bf16 v[40:43], a[48:51], v[216:219], v[40:43]\n"
    "ds_read_b128 v[232:235], v11 offset:20736\n"
    "v_mfma_f32_16x16x32_bf16 v[44:47], a[52:55], v[216:219], v[44:47]\n"
    "s_waitcnt lgkmcnt(3)\n"
    "v_mfma_f32_16x16x32_bf16 v[40:43], a[56:59], v[220:223], v[40:43]\n"
    "ds_read_b128 v[236:239], v11 offset:22784\n"
    "v_mul_f32 v112, v32, v32\n"
    "v_mfma_f32_16x16x32_bf16 v[44:47], a[60:63], v[220:223], v[44:47]\n"
    "v_mul_f32 v113, v33, v33\n"
    "v_fma_f32 v112, v112, s58, v14\n"
    "s_waitcnt lgkmcnt(3)\n"
    "v_mfma_f32_16x16x32_bf16 v[40:43], a[64:67], v[224:227], v[40:43]\n"
    "ds_read_b128 v[240:243], v11 offset:24832\n"
    "v_fma_f32 v113, v113, s58, v14\n"
    "v_mfma_f32_16x16x32_bf16 v[44:47], a[68:71], v[224:227], v[44:47]\n"
    "v_mul_f32 v112, v112, v32\n"
    "v_mul_f32 v113, v113, v33\n"
    "s_waitcnt lgkmcnt(3)\n"
    "v_mfma_f32_16x16x32_bf16 v[40:43], a[72:75], v[228:231], v[40:43]\n"
    "ds_read_b128 v[244:247], v11 offset:26880\n"
    "v_mfma_f32_16x16x32_bf16 v[44:47], a[76:79], v[228:231], v[44:47]\n"
    "v_exp_f32 v112, v112\n"
    "s_waitcnt lgkmcnt(3)\n"
    "v_mfma_f32_16x16x32_bf16 v[40:43], a[80:83], v[232:235], v[40:43]\n"
    "ds_read_b128 v[248:251], v11 offset:28928\n"
    "v_mfma_f32_16x16x32_bf16 v[44:47], a[84:87], v[232:235], v[44:47]\n"
    "v_exp_f32 v113, v113\n"
    "s_waitcnt lgkmcnt(3)\n"
    "v_mfma_f32_16x16x32_bf16 v[40:43], a[88:91], v[236:239], v[40:43]\n"
    "ds_read_b128 v[252:255], v11 offset:30976\n"
    "v_add_f32 v112, 1.0, v112\n"
    "v_mfma_f32_16x16x32_bf16 v[44:47], a[92:95], v[236:239], v[44:47]\n"
    "v_add_f32 v113, 1.0, v113\n"
    "s_waitcnt lgkmcnt(3)\n"
    "v_mfma_f32_16x16x32_bf16 v[40:43], a[96:99], v[240:243], v[40:43]\n"
    "v_rcp_f32 v112, v112\n"
    "v_mfma_f32_16x16x32_bf16 v[44:47], a[100:103], v[240:243], v[44:47]\n"
    "v_rcp_f32 v113, v113\n"
    "s_waitcnt lgkmcnt(2)\n"
    "v_mfma_f32_16x16x32_bf16 v[40:43], a[104:107], v[244:247], v[40:43]\n"
    "v_mul_f32 v112, v112, v32\n"
    "v_mul_f32 v113, v113, v33\n"
    "v_mfma_f32_16x16x32_bf16 v[44:47], a[108:111], v[244:247], v[44:47]\n"
    "v_mul_f32 v114, v34, v34\n"
    "v_mul_f32 v115, v35, v35\n"
    "s_waitcnt lgkmcnt(1)\n"
    "v_mfma_f32_16x16x32_bf16 v[40:43], a[112:115], v[248:251], v[40:43]\n"
    "v_fma_f32 v114, v114, s58, v14\n"
    "v_fma_f32 v115, v115, s58, v14\n"
    "v_mfma_f32_16x16x32_bf16 v[44:47], a[116:119], v[248:251], v[44:47]\n"
    "v_mul_f32 v114, v114, v34\n"
    "v_mul_f32 v115, v115, v35\n"
    "s_waitcnt lgkmcnt(0)\n"
    "v_mfma_f32_16x16x32_bf16 v[40:43], a[120:123], v[252:255], v[40:43]\n"
    "v_exp_f32 v114, v114\n"
    "v_mfma_f32_16x16x32_bf16 v[44:47], a[124:127], v[252:255], v[44:47]\n"
    "v_exp_f32 v115, v115\n"
    "v_mfma_f32_16x16x32_bf16 v[48:51], a[128:131], v[128:131], 0\n"
    "v_add_f32 v114, 1.0, v114\n"
    "v_add_f32 v115, 1.0, v115\n"
    "v_mfma_f32_16x16x32_bf16 v[52:55], a[132:135], v[128:131], 0\n"
    "v_rcp_f32 v114, v114\n"
    "v_mfma_f32_16x16x32_bf16 v[48:51], a[136:139], v[132:135], v[48:51]\n"
    "v_rcp_f32 v115, v115\n"
    "v_mfma_f32_16x16x32_bf16 v[52:55], a[140:143], v[132:135], v[52:55]\n"
    "v_mul_f32 v114, v114, v34\n"
    "v_mul_f32 v115, v115, v35\n"
    "v_mfma_f32_16x16x32_bf16 v[48:51], a[144:147], v[136:139], v[48:51]\n"
    "v_mul_f32 v116, v36, v36\n"
    "v_mul_f32 v117, v37, v37\n"
    "v_mfma_f32_16x16x32_bf16 v[52:55], a[148:151], v[136:139], v[52:55]\n"
    "v_fma_f32 v116, v116, s58, v14\n"
    "v_fma_f32 v117, v117, s58, v14\n"
    "v_mfma_f32_16x16x32_bf16 v[48:51], a[152:155], v[140:143], v[48:51]\n"
    "v_mul_f32 v116, v116, v36\n"
    "v_mul_f32 v117, v117, v37\n"
    "v_mfma_f32_16x16x32_bf16 v[52:55], a[156:159], v[140:143], v[52:55]\n"
    "v_exp_f32 v116, v116\n"
    "v_mfma_f32_16x16x32_bf16 v[48:51], a[160:163], v[168:171], v[48:51]\n"
    "v_exp_f32 v117, v117\n"
    "v_mfma_f32_16x16x32_bf16 v[52:55], a[164:167], v[168:171], v[52:55]\n"
    "v_add_f32 v116, 1.0, v116\n"
    "v_add_f32 v117, 1.0, v117\n"
    "v_mfma_f32_16x16x32_bf16 v[48:51], a[168:171], v[172:175], v[48:51]\n"
    "v_rcp_f32 v116, v116\n"
    "v_mfma_f32_16x16x32_bf16 v[52:55], a[172:175], v[172:175], v[52:55]\n"
    "v_rcp_f32 v117, v117\n"
    "v_mfma_f32_16x16x32_bf16 v[48:51], a[176:179], v[176:179], v[48:51]\n"
    "v_mul_f32 v116, v116, v36\n"
    "v_mul_f32 v117, v117, v37\n"
    "v_mfma_f32_16x16x32_bf16 v[52:55], a[180:183], v[176:179], v[52:55]\n"
    "v_mul_f32 v118, v38, v38\n"
    "v_mul_f32 v119, v39, v39\n"
    "v_mfma_f32_16x16x32_bf16 v[48:51], a[184:187], v[180:183], v[48:51]\n"
    "v_fma_f32 v118, v118, s58, v14\n"
    "v_fma_f32 v119, v119, s58, v14\n"
    "v_mfma_f32_16x16x32_bf16 v[52:55], a[188:191], v[180:183], v[52:55]\n"
    "v_mul_f32 v118, v118, v38\n"
    "v_mul_f32 v119, v119, v39\n"
    "v_mfma_f32_16x16x32_bf16 v[48:51], a[192:195], v[184:187], v[48:51]\n"
    "v_exp_f32 v118, v118\n"
    "v_mfma_f32_16x16x32_bf16 v[52:55], a[196:199], v[184:187], v[52:55]\n"
    "v_exp_f32 v119, v119\n"
    "v_mfma_f32_16x16x32_bf16 v[48:51], a[200:203], v[188:191], v[48:51]\n"
    "v_add_f32 v118, 1.0, v118\n"
    "v_add_f32 v119, 1.0, v119\n"
    "v_mfma_f32_16x16x32_bf16 v[52:55], a[204:207], v[188:191], v[52:55]\n"
    "v_rcp_f32 v118, v118\n"
    "v_mfma_f32_16x16x32_bf16 v[48:51], a[208:211], v[192:195], v[48:51]\n"
    "v_rcp_f32 v119, v119\n"
    "v_mfma_f32_16x16x32_bf16 v[52:55], a[212:215], v[192:195], v[52:55]\n"
    "v_mul_f32 v118, v118, v38\n"
    "v_mul_f32 v119, v119, v39\n"
    "v_mfma_f32_16x16x32_bf16 v[48:51], a[216:219], v[196:199], v[48:51]\n"
    "v_mul_f32 v120, v40, v40\n"
    "v_mul_f32 v121, v41, v41\n"
    "v_mfma_f32_16x16x32_bf16 v[52:55], a[220:223], v[196:199], v[52:55]\n"
    "v_fma_f32 v120, v120, s58, v14\n"
    "v_fma_f32 v121, v121, s58, v14\n"
    "v_mfma_f32_16x16x32_bf16 v[48:51], a[224:227], v[200:203], v[48:51]\n"
    "v_mul_f32 v120, v120, v40\n"
    "v_mul_f32 v121, v121, v41\n"
    "v_mfma_f32_16x16x32_bf16 v[52:55], a[228:231], v[200:203], v[52:55]\n"
    "v_exp_f32 v120, v120\n"
    "v_mfma_f32_16x16x32_bf16 v[48:51], a[232:235], v[204:207], v[48:51]\n"
    "v_exp_f32 v121, v121\n"
    "v_mfma_f32_16x16x32_bf16 v[52:55], a[236:239], v[204:207], v[52:55]\n"
    "v_add_f32 v120, 1.0, v120\n"
    "v_add_f32 v121, 1.0, v121\n"
    "v_mfma_f32_16x16x32_bf16 v[48:51], a[240:243], v[208:211], v[48:51]\n"
    "v_rcp_f32 v120, v120\n"
    "v_mfma_f32_16x16x32_bf16 v[52:55], a[244:247], v[208:211], v[52:55]\n"
    "v_rcp_f32 v121, v121\n"
    "v_mfma_f32_16x16x32_bf16 v[48:51], a[248:251], v[212:215], v[48:51]\n"
    "v_mul_f32 v120, v120, v40\n"
    "v_mul_f32 v121, v121, v41\n"
    "v_mfma_f32_16x16x32_bf16 v[52:55], a[252:255], v[212:215], v[52:55]\n"
    "v_mul_f32 v122, v42, v42\n"
    "v_mul_f32 v123, v43, v43\n"
    "v_mfma_f32_16x16x32_bf16 v[56:59], a[128:131], v[144:147], 0\n"
    "v_fma_f32 v122, v122, s58, v14\n"
    "v_fma_f32 v123, v123, s58, v14\n"
    "v_mfma_f32_16x16x32_bf16 v[60:63], a[132:135], v[144:147], 0\n"
    "v_mul_f32 v122, v122, v42\n"
    "v_mul_f32 v123, v123, v43\n"
    "v_mfma_f32_16x16x32_bf16 v[56:59], a[136:139], v[148:151], v[56:59]\n"
    "v_exp_f32 v122, v122\n"
    "v_mfma_f32_16x16x32_bf16 v[60:63], a[140:143], v[148:151], v[60:63]\n"
    "v_exp_f32 v123, v123\n"
    "v_mfma_f32_16x16x32_bf16 v[56:59], a[144:147], v[152:155], v[56:59]\n"
    "v_add_f32 v122, 1.0, v122\n"
    "v_add_f32 v123, 1.0, v123\n"
    "v_mfma_f32_16x16x32_bf16 v[60:63], a[148:151], v[152:155], v[60:63]\n"
    "v_rcp_f32 v122, v122\n"
    "v_mfma_f32_16x16x32_bf16 v[56:59], a[152:155], v[156:159], v[56:59]\n"
    "v_rcp_f32 v123, v123\n"
    "v_mfma_f32_16x16x32_bf16 v[60:63], a[156:159], v[156:159], v[60:63]\n"
    "v_mul_f32 v122, v122, v42\n"
    "v_mul_f32 v123, v123, v43\n"
    "v_mfma_f32_16x16x32_bf16 v[56:59], a[160:163], v[160:163], v[56:59]\n"
    "v_mul_f32 v124, v44, v44\n"
    "v_mul_f32 v125, v45, v45\n"
    "v_mfma_f32_16x16x32_bf16 v[60:63], a[164:167], v[160:163], v[60:63]\n"
    "v_fma_f32 v124, v124, s58, v14\n"
    "v_fma_f32 v125, v125, s58, v14\n"
    "v_mfma_f32_16x16x32_bf16 v[56:59], a[168:171], v[164:167], v[56:59]\n"
    "v_mul_f32 v124, v124, v44\n"
    "v_mul_f32 v125, v125, v45\n"
    "v_mfma_f32_16x16x32_bf16 v[60:63], a[172:175], v[164:167], v[60:63]\n"
    "v_exp_f32 v124, v124\n"
    "v_mfma_f32_16x16x32_bf16 v[56:59], a[176:179], v[216:219], v[56:59]\n"
    "v_exp_f32 v125, v125\n"
    "v_mfma_f32_16x16x32_bf16 v[60:63], a[180:183], v[216:219], v[60:63]\n"
    "v_add_f32 v124, 1.0, v124\n"
    "v_add_f32 v125, 1.0, v125\n"
    "v_mfma_f32_16x16x32_bf16 v[56:59], a[184:187], v[220:223], v[56:59]\n"
    "buffer_load_dword v9, v4, s[36:39], 0 offen sc1\n"
    "v_rcp_f32 v124, v124\n"
    "v_mfma_f32_16x16x32_bf16 v[60:63], a[188:191], v[220:223], v[60:63]\n"
    "v_rcp_f32 v125, v125\n"
    "v_mfma_f32_16x16x32_bf16 v[56:59], a[192:195], v[224:227], v[56:59]\n"
    "v_mul_f32 v124, v124, v44\n"
    "v_mul_f32 v125, v125, v45\n"
    "v_mfma_f32_16x16x32_bf16 v[60:63], a[196:199], v[224:227], v[60:63]\n"
    "v_mul_f32 v126, v46, v46\n"
    "v_mul_f32 v127, v47, v47\n"
    "v_mfma_f32_16x16x32_bf16 v[56:59], a[200:203], v[228:231], v[56:59]\n"
    "v_fma_f32 v126, v126, s58, v14\n"
    "v_fma_f32 v127, v127, s58, v14\n"
    "v_mfma_f32_16x16x32_bf16 v[60:63], a[204:207], v[228:231], v[60:63]\n"
    "v_mul_f32 v126, v126, v46\n"
    "v_mul_f32 v127, v127, v47\n"
    "v_mfma_f32_16x16x32_bf16 v[56:59], a[208:211], v[232:235], v[56:59]\n"
    "v_exp_f32 v126, v126\n"
    "v_mfma_f32_16x16x32_bf16 v[60:63], a[212:215], v[232:235], v[60:63]\n"
    "v_exp_f32 v127, v127\n"
    "v_mfma_f32_16x16x32_bf16 v[56:59], a[216:219], v[236:239], v[56:59]\n"
    "v_add_f32 v126, 1.0, v126\n"
    "v_add_f32 v127, 1.0, v127\n"
    "v_mfma_f32_16x16x32_bf16 v[60:63], a[220:223], v[236:239], v[60:63]\n"
    "v_rcp_f32 v126, v126\n"
    "v_mfma_f32_16x16x32_bf16 v[56:59], a[224:227], v[240:243], v[56:59]\n"
    "v_rcp_f32 v127, v127\n"
    "v_mfma_f32_16x16x32_bf16 v[60:63], a[228:231], v[240:243], v[60:63]\n"
    "v_mul_f32 v126, v126, v46\n"
    "v_mul_f32 v127, v127, v47\n"
    "v_mfma_f32_16x16x32_bf16 v[56:59], a[232:235], v[244:247], v[56:59]\n"
    "v_mul_f32 v112, v112, v48\n"
    "v_mul_f32 v113, v113, v49\n"
    "v_mfma_f32_16x16x32_bf16 v[60:63], a[236:239], v[244:247], v[60:63]\n"
    "v_mul_f32 v114, v114, v50\n"
    "v_mul_f32 v115, v115, v51\n"
    "v_mfma_f32_16x16x32_bf16 v[56:59], a[240:243], v[248:251], v[56:59]\n"
    "v_mul_f32 v116, v116, v52\n"
    "v_mul_f32 v117, v117, v53\n"
    "v_mfma_f32_16x16x32_bf16 v[60:63], a[244:247], v[248:251], v[60:63]\n"
    "v_mul_f32 v118, v118, v54\n"
    "v_mul_f32 v119, v119, v55\n"
    "v_mfma_f32_16x16x32_bf16 v[56:59], a[248:251], v[252:255], v[56:59]\n"
    "v_cvt_pk_bf16_f32 v96, v112, v113\n"
    "v_cvt_pk_bf16_f32 v97, v114, v115\n"
    "v_mfma_f32_16x16x32_bf16 v[60:63], a[252:255], v[252:255], v[60:63]\n"
    "v_cvt_pk_bf16_f32 v98, v116, v117\n"
    "v_cvt_pk_bf16_f32 v99, v118, v119\n"
    "buffer_store_dwordx4 v[96:99], v6, s[36:39], s69 offen offset:0 sc1\n"
    "v_pk_mul_f32 v[120:121], v[120:121], v[56:57]\n"
    "v_pk_mul_f32 v[122:123], v[122:123], v[58:59]\n"
    "s_nop 1\n"
    "v_pk_mul_f32 v[124:125], v[124:125], v[60:61]\n"
    "v_pk_mul_f32 v[126:127], v[126:127], v[62:63]\n"
    "v_cvt_pk_bf16_f32 v100, v120, v121\n"
    "v_cvt_pk_bf16_f32 v101, v122, v123\n"
    "v_cvt_pk_bf16_f32 v102, v124, v125\n"
    "v_cvt_pk_bf16_f32 v103, v126, v127\n"
    "buffer_store_dwordx4 v[100:103], v6, s[36:39], s69 offen offset:256 sc1\n"
    "s_add_u32 s60, s60, 1\n"
    "s_add_u32 s61, s61, 1\n"
    "s_xor_b32 s62, s62, 0x10000\n"
    "s_mov_b32 s63, 0\n"
    "s_add_u32 s71, s71, 1\n"
    "s_cmp_eq_u32 s60, 64\n"
    "s_cselect_b32 s71, 1000, s71\n"
    "s_lshr_b32 s68, s46, 8\n"
    "s_xor_b32 s68, s68, s51\n"
    "s_and_b32 s68, s68, 0x2000\n"
    "s_or_b32 s68, s68, s46\n"
    "s_add_u32 s66, s53, s62\n"
    "s_mov_b32 m0, s66\n"
    "v_add_u32 v11, s62, v3\n"
    "s_add_u32 s46, s46, s47\n"
    "s_waitcnt vmcnt(2)\n"
    "v_max_u32 v8, v8, v9\n"
    "v_cmp_gt_i32 vcc, s71, v8\n"
    "s_cbranch_vccnz L_GU_NOIA%=\n"
    "s_mov_b32 s63, 1\n"
    "buffer_load_dwordx4 v2, s[36:39], s68 offen sc1 lds\n"
    "buffer_load_dwordx4 v2, s[36:39], s68 offen offset:1024 sc1 lds\n"
    "s_add_u32 s68, s68, 0x4000\n"
    "s_add_u32 m0, s66, 8192\n"
    "s_nop 0\n"
    "buffer_load_dwordx4 v2, s[36:39], s68 offen sc1 lds\n"
    "buffer_load_dwordx4 v2, s[36:39], s68 offen offset:1024 sc1 lds\n"
    "s_add_u32 s68, s68, 0x4000\n"
    "s_add_u32 m0, s66, 16384\n"
    "s_nop 0\n"
    "buffer_load_dwordx4 v2, s[36:39], s68 offen sc1 lds\n"
    "buffer_load_dwordx4 v2, s[36:39], s68 offen offset:1024 sc1 lds\n"
    "s_add_u32 s68, s68, 0x4000\n"
    "s_add_u32 m0, s66, 24576\n"
    "s_nop 0\n"
    "buffer_load_dwordx4 v2, s[36:39], s68 offen sc1 lds\n"
    "buffer_load_dwordx4 v2, s[36:39], s68 offen offset:1024 sc1 lds\n"
    "s_waitcnt vmcnt(8)\n"
    "s_branch L_GU_PUBA%=\n"
    "L_GU_NOIA%=:\n"
    "s_waitcnt vmcnt(0)\n"
    "L_GU_PUBA%=:\n"
    "s_barrier\n"
    "buffer_store_dword v12, v5, s[36:39], 0 offen sc1\n"
    "s_cmp_lt_u32 s60, 64\n"
    "s_cbranch_scc1 L_GU_LOOPA%=\n"
    "s_branch L_GU_END%=\n"
    "L_GU_WAf%=:\n"
    "s_nop 0\n"
    "s_cmp_lg_u32 s65, 0\n"
    "s_cbranch_scc1 L_GU_WAok%=\n"
    "s_add_u32 s64, s64, 1\n"
    "s_and_b32 s70, s64, 255\n"
    "s_cbranch_scc1 L_GU_WA%=\n"
    "buffer_load_dword v10, off, s[36:39], s50 sc1\n"
    "s_waitcnt vmcnt(0)\n"
    "v_readfirstlane_b32 s65, v10\n"
    "s_cmp_lt_u32 s64, 0x40000\n"
    "s_cbranch_scc1 L_GU_WA%=\n"
    "s_mov_b32 s65, 1\n"
    "v_mov_b32 v10, 1\n"
    "buffer_store_dword v10, off, s[36:39], s50 sc1\n"
    "s_branch L_GU_WA%=\n"
    "L_GU_END%=:\n"
    "s_waitcnt vmcnt(0)\n"
    "L_GU_FIN:\ns_endpgm\n"
      GEMM_OPS : ROLE_CLOB);
  } else if constexpr (KIND == 2) {
    asm volatile("s_mov_b64 exec, -1\ns_and_b32 s37, s37, 0xffff\ns_mov_b32 s39, 0x20000\ns_nop 4\n"
    "s_mov_b32 s64, 0\n"
    "s_mov_b32 s65, 0\n"
    "s_add_u32 m0, s45, 0\n"
    "s_nop 0\n"
    "global_load_lds_dwordx4 v0, s[40:41] \n"
    "s_add_u32 s40, s40, s44\n"
    "s_addc_u32 s41, s41, 0\n"
    "s_add_u32 m0, s45, 1024\n"
    "s_nop 0\n"
    "global_load_lds_dwordx4 v0, s[40:41] \n"
    "s_add_u32 s40, s40, s44\n"
    "s_addc_u32 s41, s41, 0\n"
    "s_add_u32 m0, s45, 2048\n"
    "s_nop 0\n"
    "global_load_lds_dwordx4 v0, s[40:41] \n"
    "s_add_u32 s40, s40, s44\n"
    "s_addc_u32 s41, s41, 0\n"
    "s_add_u32 m0, s45, 3072\n"
    "s_nop 0\n"
    "global_load_lds_dwordx4 v0, s[40:41] \n"
    "s_add_u32 s40, s40, s44\n"
    "s_addc_u32 s41, s41, 0\n"
    "s_add_u32 m0, s45, 4096\n"
    "s_nop 0\n"
    "global_load_lds_dwordx4 v0, s[40:41] \n"
    "s_add_u32 s40, s40, s44\n"
    "s_addc_u32 s41, s41, 0\n"
    "s_add_u32 m0, s45, 5120\n"
    "s_nop 0\n"
    "global_load_lds_dwordx4 v0, s[40:41] \n"
    "s_add_u32 s40, s40, s44\n"
    "s_addc_u32 s41, s41, 0\n"
    "s_add_u32 m0, s45, 6144\n"
    "s_nop 0\n"
    "global_load_lds_dwordx4 v0, s[40:41] \n"
    "s_add_u32 s40, s40, s44\n"
    "s_addc_u32 s41, s41, 0\n"
    "s_add_u32 m0, s45, 7168\n"
    "s_nop 0\n"
    "global_load_lds_dwordx4 v0, s[40:41] \n"
    "s_add_u32 s40, s40, s44\n"
    "s_addc_u32 s41, s41, 0\n"
    "s_add_u32 m0, s45, 8192\n"
    "s_nop 0\n"
    "global_load_lds_dwordx4 v0, s[40:41] \n"
    "s_add_u32 s40, s40, s44\n"
    "s_addc_u32 s41, s41, 0\n"
    "s_add_u32 m0, s45, 9216\n"
    "s_nop 0\n"
    "global_load_lds_dwordx4 v0, s[40:41] \n"
    "s_add_u32 s40, s40, s44\n"
    "s_addc_u32 s41, s41, 0\n"
    "s_add_u32 m0, s45, 10240\n"
    "s_nop 0\n"
    "global_load_lds_dwordx4 v0, s[40:41] \n"
    "s_add_u32 s40, s40, s44\n"
    "s_addc_u32 s41, s41, 0\n"
    "s_add_u32 m0, s45, 11264\n"
    "s_nop 0\n"
    "global_load_lds_dwordx4 v0, s[40:41] \n"
    "s_add_u32 s40, s40, s44\n"
    "s_addc_u32 s41, s41, 0\n"
    "s_add_u32 m0, s45, 12288\n"
    "s_nop 0\n"
    "global_load_lds_dwordx4 v0, s[40:41] \n"
    "s_add_u32 s40, s40, s44\n"
    "s_addc_u32 s41, s41, 0\n"
    "s_add_u32 m0, s45, 13312\n"
    "s_nop 0\n"
    "global_load_lds_dwordx4 v0, s[40:41] \n"
    "s_add_u32 s40, s40, s44\n"
    "s_addc_u32 s41, s41, 0\n"
    "s_add_u32 m0, s45, 14336\n"
    "s_nop 0\n"
    "global_load_lds_dwordx4 v0, s[40:41] \n"
    "s_add_u32 s40, s40, s44\n"
    "s_addc_u32 s41, s41, 0\n"
    "s_add_u32 m0, s45, 15360\n"
    "s_nop 0\n"
    "global_load_lds_dwordx4 v0, s[40:41] \n"
    "s_add_u32 s40, s40, s44\n"
    "s_addc_u32 s41, s41, 0\n"
    "s_add_u32 m0, s45, 16384\n"
    "s_nop 0\n"
    "global_load_lds_dwordx4 v0, s[40:41] \n"
    "s_add_u32 s40, s40, s44\n"
    "s_addc_u32 s41, s41, 0\n"
    "s_add_u32 m0, s45, 17408\n"
    "s_nop 0\n"
    "global_load_lds_dwordx4 v0, s[40:41] \n"
    "s_add_u32 s40, s40, s44\n"
    "s_addc_u32 s41, s41, 0\n"
    "s_add_u32 m0, s45, 18432\n"
    "s_nop 0\n"
    "global_load_lds_dwordx4 v0, s[40:41] \n"
    "s_add_u32 s40, s40, s44\n"
    "s_addc_u32 s41, s41, 0\n"
    "s_add_u32 m0, s45, 19456\n"
    "s_nop 0\n"
    "global_load_lds_dwordx4 v0, s[40:41] \n"
    "s_add_u32 s40, s40, s44\n"
    "s_addc_u32 s41, s41, 0\n"
    "s_add_u32 m0, s45, 20480\n"
    "s_nop 0\n"
    "global_load_lds_dwordx4 v0, s[40:41] \n"
    "s_add_u32 s40, s40, s44\n"
    "s_addc_u32 s41, s41, 0\n"
    "s_add_u32 m0, s45, 21504\n"
    "s_nop 0\n"
    "global_load_lds_dwordx4 v0, s[40:41] \n"
    "s_add_u32 s40, s40, s44\n"
    "s_addc_u32 s41, s41, 0\n"
    "s_add_u32 m0, s45, 22528\n"
    "s_nop 0\n"
    "global_load_lds_dwordx4 v0, s[40:41] \n"
    "s_add_u32 s40, s40, s44\n"
    "s_addc_u32 s41, s41, 0\n"
    "s_add_u32 m0, s45, 23552\n"
    "s_nop 0\n"
    "global_load_lds_dwordx4 v0, s[40:41] \n"
    "s_add_u32 s40, s40, s44\n"
    "s_addc_u32 s41, s41, 0\n"
    "s_add_u32 m0, s45, 24576\n"
    "s_nop 0\n"
    "global_load_lds_dwordx4 v0, s[40:41] \n"
    "s_add_u32 s40, s40, s44\n"
    "s_addc_u32 s41, s41, 0\n"
    "s_add_u32 m0, s45, 25600\n"
    "s_nop 0\n"
    "global_load_lds_dwordx4 v0, s[40:41] \n"
    "s_add_u32 s40, s40, s44\n"
    "s_addc_u32 s41, s41, 0\n"
    "s_add_u32 m0, s45, 26624\n"
    "s_nop 0\n"
    "global_load_lds_dwordx4 v0, s[40:41] \n"
    "s_add_u32 s40, s40, s44\n"
    "s_addc_u32 s41, s41, 0\n"
    "s_add_u32 m0, s45, 27648\n"
    "s_nop 0\n"
    "global_load_lds_dwordx4 v0, s[40:41] \n"
    "s_add_u32 s40, s40, s44\n"
    "s_addc_u32 s41, s41, 0\n"
    "s_add_u32 m0, s45, 28672\n"
    "s_nop 0\n"
    "global_load_lds_dwordx4 v0, s[40:41] \n"
    "s_add_u32 s40, s40, s44\n"
    "s_addc_u32 s41, s41, 0\n"
    "s_add_u32 m0, s45, 29696\n"
    "s_nop 0\n"
    "global_load_lds_dwordx4 v0, s[40:41] \n"
    "s_add_u32 s40, s40, s44\n"
    "s_addc_u32 s41, s41, 0\n"
    "s_add_u32 m0, s45, 30720\n"
    "s_nop 0\n"
    "global_load_lds_dwordx4 v0, s[40:41] \n"
    "s_add_u32 s40, s40, s44\n"
    "s_addc_u32 s41, s41, 0\n"
    "s_add_u32 m0, s45, 31744\n"
    "s_nop 0\n"
    "global_load_lds_dwordx4 v0, s[40:41] \n"
    "s_add_u32 s40, s40, s44\n"
    "s_addc_u32 s41, s41, 0\n"
    "s_waitcnt vmcnt(28)\n"
    "ds_read_b64_tr_b16 a[0:1], v1 offset:0\n"
    "ds_read_b64_tr_b16 a[2:3], v1 offset:256\n"
    "ds_read_b64_tr_b16 a[4:5], v1 offset:8\n"
    "ds_read_b64_tr_b16 a[6:7], v1 offset:264\n"
    "ds_read_b64_tr_b16 a[8:9], v1 offset:2048\n"
    "ds_read_b64_tr_b16 a[10:11], v1 offset:2304\n"
    "ds_read_b64_tr_b16 a[12:13], v1 offset:2056\n"
    "ds_read_b64_tr_b16 a[14:15], v1 offset:2312\n"
    "s_waitcnt lgkmcnt(0)\n"
    "s_add_u32 m0, s45, 0\n"
    "s_nop 0\n"
    "global_load_lds_dwordx4 v0, s[42:43] \n"
    "s_add_u32 s42, s42, s44\n"
    "s_addc_u32 s43, s43, 0\n"
    "s_add_u32 m0, s45, 1024\n"
    "s_nop 0\n"
    "global_load_lds_dwordx4 v0, s[42:43] \n"
    "s_add_u32 s42, s42, s44\n"
    "s_addc_u32 s43, s43, 0\n"
    "s_add_u32 m0, s45, 2048\n"
    "s_nop 0\n"
    "global_load_lds_dwordx4 v0, s[42:43] \n"
    "s_add_u32 s42, s42, s44\n"
    "s_addc_u32 s43, s43, 0\n"
    "s_add_u32 m0, s45, 3072\n"
    "s_nop 0\n"
    "global_load_lds_dwordx4 v0, s[42:43] \n"
    "s_add_u32 s42, s42, s44\n"
    "s_addc_u32 s43, s43, 0\n"
    "s_waitcnt vmcnt(28)\n"
    "ds_read_b64_tr_b16 a[16:17], v1 offset:4096\n"
    "ds_read_b64_tr_b16 a[18:19], v1 offset:4352\n"
    "ds_read_b64_tr_b16 a[20:21], v1 offset:4104\n"
    "ds_read_b64_tr_b16 a[22:23], v1 offset:4360\n"
    "ds_read_b64_tr_b16 a[24:25], v1 offset:6144\n"
    "ds_read_b64_tr_b16 a[26:27], v1 offset:6400\n"
    "ds_read_b64_tr_b16 a[28:29], v1 offset:6152\n"
    "ds_read_b64_tr_b16 a[30:31], v1 offset:6408\n"
    "s_waitcnt lgkmcnt(0)\n"
    "s_add_u32 m0, s45, 4096\n"
    "s_nop 0\n"
    "global_load_lds_dwordx4 v0, s[42:43] \n"
    "s_add_u32 s42, s42, s44\n"
    "s_addc_u32 s43, s43, 0\n"
    "s_add_u32 m0, s45, 5120\n"
    "s_nop 0\n"
    "global_load_lds_dwordx4 v0, s[42:43] \n"
    "s_add_u32 s42, s42, s44\n"
    "s_addc_u32 s43, s43, 0\n"
    "s_add_u32 m0, s45, 6144\n"
    "s_nop 0\n"
    "global_load_lds_dwordx4 v0, s[42:43] \n"
    "s_add_u32 s42, s42, s44\n"
    "s_addc_u32 s43, s43, 0\n"
    "s_add_u32 m0, s45, 7168\n"
    "s_nop 0\n"
    "global_load_lds_dwordx4 v0, s[42:43] \n"
    "s_add_u32 s42, s42, s44\n"
    "s_addc_u32 s43, s43, 0\n"
    "s_waitcnt vmcnt(28)\n"
    "ds_read_b64_tr_b16 a[32:33], v1 offset:8192\n"
    "ds_read_b64_tr_b16 a[34:35], v1 offset:8448\n"
    "ds_read_b64_tr_b16 a[36:37], v1 offset:8200\n"
    "ds_read_b64_tr_b16 a[38:39], v1 offset:8456\n"
    "ds_read_b64_tr_b16 a[40:41], v1 offset:10240\n"
    "ds_read_b64_tr_b16 a[42:43], v1 offset:10496\n"
    "ds_read_b64_tr_b16 a[44:45], v1 offset:10248\n"
    "ds_read_b64_tr_b16 a[46:47], v1 offset:10504\n"
    "s_waitcnt lgkmcnt(0)\n"
    "s_add_u32 m0, s45, 8192\n"
    "s_nop 0\n"
    "global_load_lds_dwordx4 v0, s[42:43] \n"
    "s_add_u32 s42, s42, s44\n"
    "s_addc_u32 s43, s43, 0\n"
    "s_add_u32 m0, s45, 9216\n"
    "s_nop 0\n"
    "global_load_lds_dwordx4 v0, s[42:43] \n"
    "s_add_u32 s42, s42, s44\n"
    "s_addc_u32 s43, s43, 0\n"
    "s_add_u32 m0, s45, 10240\n"
    "s_nop 0\n"
    "global_load_lds_dwordx4 v0, s[42:43] \n"
    "s_add_u32 s42, s42, s44\n"
    "s_addc_u32 s43, s43, 0\n"
    "s_add_u32 m0, s45, 11264\n"
    "s_nop 0\n"
    "global_load_lds_dwordx4 v0, s[42:43] \n"
    "s_add_u32 s42, s42, s44\n"
    "s_addc_u32 s43, s43, 0\n"
    "s_waitcnt vmcnt(28)\n"
    "ds_read_b64_tr_b16 a[48:49], v1 offset:12288\n"
    "ds_read_b64_tr_b16 a[50:51], v1 offset:12544\n"
    "ds_read_b64_tr_b16 a[52:53], v1 offset:12296\n"
    "ds_read_b64_tr_b16 a[54:55], v1 offset:12552\n"
    "ds_read_b64_tr_b16 a[56:57], v1 offset:14336\n"
    "ds_read_b64_tr_b16 a[58:59], v1 offset:14592\n"
    "ds_read_b64_tr_b16 a[60:61], v1 offset:14344\n"
    "ds_read_b64_tr_b16 a[62:63], v1 offset:14600\n"
    "s_waitcnt lgkmcnt(0)\n"
    "s_add_u32 m0, s45, 12288\n"
    "s_nop 0\n"
    "global_load_lds_dwordx4 v0, s[42:43] \n"
    "s_add_u32 s42, s42, s44\n"
    "s_addc_u32 s43, s43, 0\n"
    "s_add_u32 m0, s45, 13312\n"
    "s_nop 0\n"
    "global_load_lds_dwordx4 v0, s[42:43] \n"
    "s_add_u32 s42, s42, s44\n"
    "s_addc_u32 s43, s43, 0\n"
    "s_add_u32 m0, s45, 14336\n"
    "s_nop 0\n"
    "global_load_lds_dwordx4 v0, s[42:43] \n"
    "s_add_u32 s42, s42, s44\n"
    "s_addc_u32 s43, s43, 0\n"
    "s_add_u32 m0, s45, 15360\n"
    "s_nop 0\n"
    "global_load_lds_dwordx4 v0, s[42:43] \n"
    "s_add_u32 s42, s42, s44\n"
    "s_addc_u32 s43, s43, 0\n"
    "s_waitcnt vmcnt(28)\n"
    "ds_read_b64_tr_b16 a[64:65], v1 offset:16384\n"
    "ds_read_b64_tr_b16 a[66:67], v1 offset:16640\n"
    "ds_read_b64_tr_b16 a[68:69], v1 offset:16392\n"
    "ds_read_b64_tr_b16 a[70:71], v1 offset:16648\n"
    "ds_read_b64_tr_b16 a[72:73], v1 offset:18432\n"
    "ds_read_b64_tr_b16 a[74:75], v1 offset:18688\n"
    "ds_read_b64_tr_b16 a[76:77], v1 offset:18440\n"
    "ds_read_b64_tr_b16 a[78:79], v1 offset:18696\n"
    "s_waitcnt lgkmcnt(0)\n"
    "s_add_u32 m0, s45, 16384\n"
    "s_nop 0\n"
    "global_load_lds_dwordx4 v0, s[42:43] \n"
    "s_add_u32 s42, s42, s44\n"
    "s_addc_u32 s43, s43, 0\n"
    "s_add_u32 m0, s45, 17408\n"
    "s_nop 0\n"
    "global_load_lds_dwordx4 v0, s[42:43] \n"
    "s_add_u32 s42, s42, s44\n"
    "s_addc_u32 s43, s43, 0\n"
    "s_add_u32 m0, s45, 18432\n"
    "s_nop 0\n"
    "global_load_lds_dwordx4 v0, s[42:43] \n"
    "s_add_u32 s42, s42, s44\n"
    "s_addc_u32 s43, s43, 0\n"
    "s_add_u32 m0, s45, 19456\n"
    "s_nop 0\n"
    "global_load_lds_dwordx4 v0, s[42:43] \n"
    "s_add_u32 s42, s42, s44\n"
    "s_addc_u32 s43, s43, 0\n"
    "s_waitcnt vmcnt(28)\n"
    "ds_read_b64_tr_b16 a[80:81], v1 offset:20480\n"
    "ds_read_b64_tr_b16 a[82:83], v1 offset:20736\n"
    "ds_read_b64_tr_b16 a[84:85], v1 offset:20488\n"
    "ds_read_b64_tr_b16 a[86:87], v1 offset:20744\n"
    "ds_read_b64_tr_b16 a[88:89], v1 offset:22528\n"
    "ds_read_b64_tr_b16 a[90:91], v1 offset:22784\n"
    "ds_read_b64_tr_b16 a[92:93], v1 offset:22536\n"
    "ds_read_b64_tr_b16 a[94:95], v1 offset:22792\n"
    "s_waitcnt lgkmcnt(0)\n"
    "s_add_u32 m0, s45, 20480\n"
    "s_nop 0\n"
    "global_load_lds_dwordx4 v0, s[42:43] \n"
    "s_add_u32 s42, s42, s44\n"
    "s_addc_u32 s43, s43, 0\n"
    "s_add_u32 m0, s45, 21504\n"
    "s_nop 0\n"
    "global_load_lds_dwordx4 v0, s[42:43] \n"
    "s_add_u32 s42, s42, s44\n"
    "s_addc_u32 s43, s43, 0\n"
    "s_add_u32 m0, s45, 22528\n"
    "s_nop 0\n"
    "global_load_lds_dwordx4 v0, s[42:43] \n"
    "s_add_u32 s42, s42, s44\n"
    "s_addc_u32 s43, s43, 0\n"
    "s_add_u32 m0, s45, 23552\n"
    "s_nop 0\n"
    "global_load_lds_dwordx4 v0, s[42:43] \n"
    "s_add_u32 s42, s42, s44\n"
    "s_addc_u32 s43, s43, 0\n"
    "s_waitcnt vmcnt(28)\n"
    "ds_read_b64_tr_b16 a[96:97], v1 offset:24576\n"
    "ds_read_b64_tr_b16 a[98:99], v1 offset:24832\n"
    "ds_read_b64_tr_b16 a[100:101], v1 offset:24584\n"
    "ds_read_b64_tr_b16 a[102:103], v1 offset:24840\n"
    "ds_read_b64_tr_b16 a[104:105], v1 offset:26624\n"
    "ds_read_b64_tr_b16 a[106:107], v1 offset:26880\n"
    "ds_read_b64_tr_b16 a[108:109], v1 offset:26632\n"
    "ds_read_b64_tr_b16 a[110:111], v1 offset:26888\n"
    "s_waitcnt lgkmcnt(0)\n"
    "s_add_u32 m0, s45, 24576\n"
    "s_nop 0\n"
    "global_load_lds_dwordx4 v0, s[42:43] \n"
    "s_add_u32 s42, s42, s44\n"
    "s_addc_u32 s43, s43, 0\n"
    "s_add_u32 m0, s45, 25600\n"
    "s_nop 0\n"
    "global_load_lds_dwordx4 v0, s[42:43] \n"
    "s_add_u32 s42, s42, s44\n"
    "s_addc_u32 s43, s43, 0\n"
    "s_add_u32 m0, s45, 26624\n"
    "s_nop 0\n"
    "global_load_lds_dwordx4 v0, s[42:43] \n"
    "s_add_u32 s42, s42, s44\n"
    "s_addc_u32 s43, s43, 0\n"
    "s_add_u32 m0, s45, 27648\n"
    "s_nop 0\n"
    "global_load_lds_dwordx4 v0, s[42:43] \n"
    "s_add_u32 s42, s42, s44\n"
    "s_addc_u32 s43, s43, 0\n"
    "s_waitcnt vmcnt(28)\n"
    "ds_read_b64_tr_b16 a[112:113], v1 offset:28672\n"
    "ds_read_b64_tr_b16 a[114:115], v1 offset:28928\n"
    "ds_read_b64_tr_b16 a[116:117], v1 offset:28680\n"
    "ds_read_b64_tr_b16 a[118:119], v1 offset:28936\n"
    "ds_read_b64_tr_b16 a[120:121], v1 offset:30720\n"
    "ds_read_b64_tr_b16 a[122:123], v1 offset:30976\n"
    "ds_read_b64_tr_b16 a[124:125], v1 offset:30728\n"
    "ds_read_b64_tr_b16 a[126:127], v1 offset:30984\n"
    "s_waitcnt lgkmcnt(0)\n"
    "s_add_u32 m0, s45, 28672\n"
    "s_nop 0\n"
    "global_load_lds_dwordx4 v0, s[42:43] \n"
    "s_add_u32 s42, s42, s44\n"
    "s_addc_u32 s43, s43, 0\n"
    "s_add_u32 m0, s45, 29696\n"
    "s_nop 0\n"
    "global_load_lds_dwordx4 v0, s[42:43] \n"
    "s_add_u32 s42, s42, s44\n"
    "s_addc_u32 s43, s43, 0\n"
    "s_add_u32 m0, s45, 30720\n"
    "s_nop 0\n"
    "global_load_lds_dwordx4 v0, s[42:43] \n"
    "s_add_u32 s42, s42, s44\n"
    "s_addc_u32 s43, s43, 0\n"
    "s_add_u32 m0, s45, 31744\n"
    "s_nop 0\n"
    "global_load_lds_dwordx4 v0, s[42:43] \n"
    "s_add_u32 s42, s42, s44\n"
    "s_addc_u32 s43, s43, 0\n"
    "s_waitcnt vmcnt(28)\n"
    "ds_read_b64_tr_b16 a[128:129], v1 offset:0\n"
    "ds_read_b64_tr_b16 a[130:131], v1 offset:256\n"
    "ds_read_b64_tr_b16 a[132:133], v1 offset:8\n"
    "ds_read_b64_tr_b16 a[134:135], v1 offset:264\n"
    "ds_read_b64_tr_b16 a[136:137], v1 offset:2048\n"
    "ds_read_b64_tr_b16 a[138:139], v1 offset:2304\n"
    "ds_read_b64_tr_b16 a[140:141], v1 offset:2056\n"
    "ds_read_b64_tr_b16 a[142:143], v1 offset:2312\n"
    "s_waitcnt vmcnt(24)\n"
    "ds_read_b64_tr_b16 a[144:145], v1 offset:4096\n"
    "ds_read_b64_tr_b16 a[146:147], v1 offset:4352\n"
    "ds_read_b64_tr_b16 a[148:149], v1 offset:4104\n"
    "ds_read_b64_tr_b16 a[150:151], v1 offset:4360\n"
    "ds_read_b64_tr_b16 a[152:153], v1 offset:6144\n"
    "ds_read_b64_tr_b16 a[154:155], v1 offset:6400\n"
    "ds_read_b64_tr_b16 a[156:157], v1 offset:6152\n"
    "ds_read_b64_tr_b16 a[158:159], v1 offset:6408\n"
    "s_waitcnt vmcnt(20)\n"
    "ds_read_b64_tr_b16 a[160:161], v1 offset:8192\n"
    "ds_read_b64_tr_b16 a[162:163], v1 offset:8448\n"
    "ds_read_b64_tr_b16 a[164:165], v1 offset:8200\n"
    "ds_read_b64_tr_b16 a[166:167], v1 offset:8456\n"
    "ds_read_b64_tr_b16 a[168:169], v1 offset:10240\n"
    "ds_read_b64_tr_b16 a[170:171], v1 offset:10496\n"
    "ds_read_b64_tr_b16 a[172:173], v1 offset:10248\n"
    "ds_read_b64_tr_b16 a[174:175], v1 offset:10504\n"
    "s_waitcnt vmcnt(16)\n"
    "ds_read_b64_tr_b16 a[176:177], v1 offset:12288\n"
    "ds_read_b64_tr_b16 a[178:179], v1 offset:12544\n"
    "ds_read_b64_tr_b16 a[180:181], v1 offset:12296\n"
    "ds_read_b64_tr_b16 a[182:183], v1 offset:12552\n"
    "ds_read_b64_tr_b16 a[184:185], v1 offset:14336\n"
    "ds_read_b64_tr_b16 a[186:187], v1 offset:14592\n"
    "ds_read_b64_tr_b16 a[188:189], v1 offset:14344\n"
    "ds_read_b64_tr_b16 a[190:191], v1 offset:14600\n"
    "s_waitcnt vmcnt(12)\n"
    "ds_read_b64_tr_b16 a[192:193], v1 offset:16384\n"
    "ds_read_b64_tr_b16 a[194:195], v1 offset:16640\n"
    "ds_read_b64_tr_b16 a[196:197], v1 offset:16392\n"
    "ds_read_b64_tr_b16 a[198:199], v1 offset:16648\n"
    "ds_read_b64_tr_b16 a[200:201], v1 offset:18432\n"
    "ds_read_b64_tr_b16 a[202:203], v1 offset:18688\n"
    "ds_read_b64_tr_b16 a[204:205], v1 offset:18440\n"
    "ds_read_b64_tr_b16 a[206:207], v1 offset:18696\n"
    "s_waitcnt vmcnt(8)\n"
    "ds_read_b64_tr_b16 a[208:209], v1 offset:20480\n"
    "ds_read_b64_tr_b16 a[210:211], v1 offset:20736\n"
    "ds_read_b64_tr_b16 a[212:213], v1 offset:20488\n"
    "ds_read_b64_tr_b16 a[214:215], v1 offset:20744\n"
    "ds_read_b64_tr_b16 a[216:217], v1 offset:22528\n"
    "ds_read_b64_tr_b16 a[218:219], v1 offset:22784\n"
    "ds_read_b64_tr_b16 a[220:221], v1 offset:22536\n"
    "ds_read_b64_tr_b16 a[222:223], v1 offset:22792\n"
    "s_waitcnt vmcnt(4)\n"
    "ds_read_b64_tr_b16 a[224:225], v1 offset:24576\n"
    "ds_read_b64_tr_b16 a[226:227], v1 offset:24832\n"
    "ds_read_b64_tr_b16 a[228:229], v1 offset:24584\n"
    "ds_read_b64_tr_b16 a[230:231], v1 offset:24840\n"
    "ds_read_b64_tr_b16 a[232:233], v1 offset:26624\n"
    "ds_read_b64_tr_b16 a[234:235], v1 offset:26880\n"
    "ds_read_b64_tr_b16 a[236:237], v1 offset:26632\n"
    "ds_read_b64_tr_b16 a[238:239], v1 offset:26888\n"
    "s_waitcnt vmcnt(0)\n"
    "ds_read_b64_tr_b16 a[240:241], v1 offset:28672\n"
    "ds_read_b64_tr_b16 a[242:243], v1 offset:28928\n"
    "ds_read_b64_tr_b16 a[244:245], v1 offset:28680\n"
    "ds_read_b64_tr_b16 a[246:247], v1 offset:28936\n"
    "ds_read_b64_tr_b16 a[248:249], v1 offset:30720\n"
    "ds_read_b64_tr_b16 a[250:251], v1 offset:30976\n"
    "ds_read_b64_tr_b16 a[252:253], v1 offset:30728\n"
    "ds_read_b64_tr_b16 a[254:255], v1 offset:30984\n"
    "s_waitcnt lgkmcnt(0)\n"
    "s_barrier\n"
    "v_mov_b32 v8, 0\n"
    "v_mov_b32 v13, 0x80000000\n"
    "s_sub_u32 s71, 1, s54\n"
    "s_mov_b32 s60, 0\n"
    "s_mov_b32 s61, 1\n"
    "s_mov_b32 s62, 0\n"
    "s_mov_b32 s63, 0\n"
    "s_bfe_u32 s17, s45, 0x2000f\n"
    "s_cmp_eq_u32 s17, 1\n"
    "s_cselect_b32 s17, 1, 0\n"
    "L_WO_GO%=:\n"
    "s_lshr_b32 s68, s46, 8\n"
    "s_xor_b32 s68, s68, s51\n"
    "s_and_b32 s68, s68, 0x2000\n"
    "s_or_b32 s68, s68, s46\n"
    "s_add_u32 s66, s53, s62\n"
    "s_mov_b32 m0, s66\n"
    "v_add_u32 v11, s62, v3\n"
    "s_add_u32 s46, s46, s47\n"
    "v_mov_b64 v[20:21], -1\n"
    "v_mov_b64 v[22:23], -1\n"
    "s_lshr_b32 s67, s48, 8\n"
    "s_xor_b32 s67, s67, s52\n"
    "s_and_b32 s67, s67, 0x2000\n"
    "s_or_b32 s67, s67, s48\n"
    "buffer_store_dwordx4 v[20:23], v6, s[36:39], s67 offen offset:0 sc1\n"
    "buffer_store_dwordx4 v[20:23], v6, s[36:39], s67 offen offset:256 sc1\n"
    "L_WO_LOOPA%=:\n"
    "s_lshr_b32 s69, s48, 8\n"
    "s_xor_b32 s69, s69, s52\n"
    "s_and_b32 s69, s69, 0x2000\n"
    "s_or_b32 s69, s69, s48\n"
    "s_add_u32 s48, s48, s49\n"
    "v_mov_b32 v12, s61\n"
    "s_cmp_eq_u32 s17, 0\n"
    "s_cbranch_scc1 L_WO_WAok%=\n"
    "v_cmp_gt_i32 vcc, s71, v8\n"
    "s_cbranch_vccz L_WO_WAok%=\n"
    "L_WO_WA%=:\n"
    "buffer_load_dword v8, v4, s[36:39], 0 offen sc1\n"
    "s_waitcnt vmcnt(0)\n"
    "v_cmp_gt_i32 vcc, s71, v8\n"
    "s_cbranch_vccnz L_WO_WAf%=\n"
    "L_WO_WAok%=:\n"
    "s_cmp_lg_u32 s63, 0\n"
    "s_barrier\n"
    "s_cbranch_scc1 L_WO_HAVEA%=\n"
    "buffer_load_dwordx4 v2, s[36:39], s68 offen sc1 lds\n"
    "buffer_load_dwordx4 v2, s[36:39], s68 offen offset:1024 sc1 lds\n"
    "s_add_u32 s68, s68, 0x4000\n"
    "s_add_u32 m0, s66, 8192\n"
    "s_nop 0\n"
    "buffer_load_dwordx4 v2, s[36:39], s68 offen sc1 lds\n"
    "buffer_load_dwordx4 v2, s[36:39], s68 offen offset:1024 sc1 lds\n"
    "s_add_u32 s68, s68, 0x4000\n"
    "s_add_u32 m0, s66, 16384\n"
    "s_nop 0\n"
    "buffer_load_dwordx4 v2, s[36:39], s68 offen sc1 lds\n"
    "buffer_load_dwordx4 v2, s[36:39], s68 offen offset:1024 sc1 lds\n"
    "s_add_u32 s68, s68, 0x4000\n"
    "s_add_u32 m0, s66, 24576\n"
    "s_nop 0\n"
    "buffer_load_dwordx4 v2, s[36:39], s68 offen sc1 lds\n"
    "buffer_load_dwordx4 v2, s[36:39], s68 offen offset:1024 sc1 lds\n"
    "s_add_u32 s68, s68, 0x4000\n"
    "buffer_load_dword v10, v13, s[36:39], 0 offen\n"
    "L_WO_HAVEA%=:\n"
    "s_waitcnt vmcnt(7)\n"
    "s_barrier\n"
    "ds_read_b128 v[128:131], v11 offset:0\n"
    "ds_read_b128 v[132:135], v11 offset:256\n"
    "ds_read_b128 v[136:139], v11 offset:2048\n"
    "ds_read_b128 v[140:143], v11 offset:2304\n"
    "ds_read_b128 v[144:147], v11 offset:4096\n"
    "ds_read_b128 v[148:151], v11 offset:4352\n"
    "s_waitcnt lgkmcnt(5)\n"
    "v_mfma_f32_16x16x32_bf16 v[32:35], a[0:3], v[128:131], 0\n"
    "s_add_u32 m0, s66, 32768\n"
    "s_waitcnt lgkmcnt(4)\n"
    "v_mfma_f32_16x16x32_bf16 v[40:43], a[0:3], v[132:135], 0\n"
    "buffer_load_dwordx4 v2, s[36:39], s68 offen sc1 lds\n"
    "v_mfma_f32_16x16x32_bf16 v[44:47], a[4:7], v[132:135], 0\n"
    "ds_read_b128 v[152:155], v11 offset:6144\n"
    "v_mfma_f32_16x16x32_bf16 v[36:39], a[4:7], v[128:131], 0\n"
    "ds_read_b128 v[156:159], v11 offset:6400\n"
    "s_waitcnt lgkmcnt(5)\n"
    "v_mfma_f32_16x16x32_bf16 v[32:35], a[8:11], v[136:139], v[32:35]\n"
    "s_waitcnt lgkmcnt(4)\n"
    "v_mfma_f32_16x16x32_bf16 v[40:43], a[8:11], v[140:143], v[40:43]\n"
    "buffer_load_dwordx4 v2, s[36:39], s68 offen offset:1024 sc1 lds\n"
    "s_add_u32 s68, s68, 0x4000\n"
    "v_mfma_f32_16x16x32_bf16 v[44:47], a[12:15], v[140:143], v[44:47]\n"
    "s_waitcnt vmcnt(7)\n"
    "s_barrier\n"
    "ds_read_b128 v[160:163], v11 offset:8192\n"
    "v_mfma_f32_16x16x32_bf16 v[36:39], a[12:15], v[136:139], v[36:39]\n"
    "ds_read_b128 v[164:167], v11 offset:8448\n"
    "s_waitcnt lgkmcnt(5)\n"
    "v_mfma_f32_16x16x32_bf16 v[32:35], a[16:19], v[144:147], v[32:35]\n"
    "s_add_u32 m0, s66, 40960\n"
    "s_waitcnt lgkmcnt(4)\n"
    "v_mfma_f32_16x16x32_bf16 v[40:43], a[16:19], v[148:151], v[40:43]\n"
    "buffer_load_dwordx4 v2, s[36:39], s68 offen sc1 lds\n"
    "v_mfma_f32_16x16x32_bf16 v[44:47], a[20:23], v[148:151], v[44:47]\n"
    "ds_read_b128 v[168:171], v11 offset:10240\n"
    "v_mfma_f32_16x16x32_bf16 v[36:39], a[20:23], v[144:147], v[36:39]\n"
    "ds_read_b128 v[172:175], v11 offset:10496\n"
    "s_waitcnt lgkmcnt(5)\n"
    "v_mfma_f32_16x16x32_bf16 v[32:35], a[24:27], v[152:155], v[32:35]\n"
    "s_waitcnt lgkmcnt(4)\n"
    "v_mfma_f32_16x16x32_bf16 v[40:43], a[24:27], v[156:159], v[40:43]\n"
    "buffer_load_dwordx4 v2, s[36:39], s68 offen offset:1024 sc1 lds\n"
    "s_add_u32 s68, s68, 0x4000\n"
    "v_mfma_f32_16x16x32_bf16 v[44:47], a[28:31], v[156:159], v[44:47]\n"
    "ds_read_b128 v[176:179], v11 offset:12288\n"
    "v_mfma_f32_16x16x32_bf16 v[36:39], a[28:31], v[152:155], v[36:39]\n"
    "ds_read_b128 v[180:183], v11 offset:12544\n"
    "s_waitcnt lgkmcnt(5)\n"
    "v_mfma_f32_16x16x32_bf16 v[32:35], a[32:35], v[160:163], v[32:35]\n"
    "s_add_u32 m0, s66, 49152\n"
    "s_waitcnt lgkmcnt(4)\n"
    "v_mfma_f32_16x16x32_bf16 v[40:43], a[32:35], v[164:167], v[40:43]\n"
    "buffer_load_dwordx4 v2, s[36:39], s68 offen sc1 lds\n"
    "v_mfma_f32_16x16x32_bf16 v[44:47], a[36:39], v[164:167], v[44:47]\n"
    "ds_read_b128 v[184:187], v11 offset:14336\n"
    "v_mfma_f32_16x16x32_bf16 v[36:39], a[36:39], v[160:163], v[36:39]\n"
    "ds_read_b128 v[188:191], v11 offset:14592\n"
    "s_waitcnt lgkmcnt(5)\n"
    "v_mfma_f32_16x16x32_bf16 v[32:35], a[40:43], v[168:171], v[32:35]\n"
    "s_waitcnt lgkmcnt(4)\n"
    "v_mfma_f32_16x16x32_bf16 v[40:43], a[40:43], v[172:175], v[40:43]\n"
    "buffer_load_dwordx4 v2, s[36:39], s68 offen offset:1024 sc1 lds\n"
    "s_add_u32 s68, s68, 0x4000\n"
    "v_mfma_f32_16x16x32_bf16 v[44:47], a[44:47], v[172:175], v[44:47]\n"
    "s_waitcnt vmcnt(9)\n"
    "s_barrier\n"
    "ds_read_b128 v[192:195], v11 offset:16384\n"
    "v_mfma_f32_16x16x32_bf16 v[36:39], a[44:47], v[168:171], v[36:39]\n"
    "ds_read_b128 v[196:199], v11 offset:16640\n"
    "s_waitcnt lgkmcnt(5)\n"
    "v_mfma_f32_16x16x32_bf16 v[32:35], a[48:51], v[176:179], v[32:35]\n"
    "s_add_u32 m0, s66, 57344\n"
    "s_waitcnt lgkmcnt(4)\n"
    "v_mfma_f32_16x16x32_bf16 v[40:43], a[48:51], v[180:183], v[40:43]\n"
    "buffer_load_dwordx4 v2, s[36:39], s68 offen sc1 lds\n"
    "v_mfma_f32_16x16x32_bf16 v[44:47], a[52:55], v[180:183], v[44:47]\n"
    "ds_read_b128 v[200:203], v11 offset:18432\n"
    "v_mfma_f32_16x16x32_bf16 v[36:39], a[52:55], v[176:179], v[36:39]\n"
    "ds_read_b128 v[204:207], v11 offset:18688\n"
    "s_waitcnt lgkmcnt(5)\n"
    "v_mfma_f32_16x16x32_bf16 v[32:35], a[56:59], v[184:187], v[32:35]\n"
    "s_waitcnt lgkmcnt(4)\n"
    "v_mfma_f32_16x16x32_bf16 v[40:43], a[56:59], v[188:191], v[40:43]\n"
    "buffer_load_dwordx4 v2, s[36:39], s68 offen offset:1024 sc1 lds\n"
    "s_add_u32 s68, s68, 0x4000\n"
    "v_mfma_f32_16x16x32_bf16 v[44:47], a[60:63], v[188:191], v[44:47]\n"
    "ds_read_b128 v[208:211], v11 offset:20480\n"
    "v_mfma_f32_16x16x32_bf16 v[36:39], a[60:63], v[184:187], v[36:39]\n"
    "ds_read_b128 v[212:215], v11 offset:20736\n"
    "s_waitcnt lgkmcnt(5)\n"
    "v_mfma_f32_16x16x32_bf16 v[32:35], a[64:67], v[192:195], v[32:35]\n"
    "s_waitcnt lgkmcnt(4)\n"
    "v_mfma_f32_16x16x32_bf16 v[40:43], a[64:67], v[196:199], v[40:43]\n"
    "v_mfma_f32_16x16x32_bf16 v[44:47], a[68:71], v[196:199], v[44:47]\n"
    "ds_read_b128 v[216:219], v11 offset:22528\n"
    "v_mfma_f32_16x16x32_bf16 v[36:39], a[68:71], v[192:195], v[36:39]\n"
    "ds_read_b128 v[220:223], v11 offset:22784\n"
    "s_waitcnt lgkmcnt(5)\n"
    "v_mfma_f32_16x16x32_bf16 v[32:35], a[72:75], v[200:203], v[32:35]\n"
    "s_waitcnt lgkmcnt(4)\n"
    "v_mfma_f32_16x16x32_bf16 v[40:43], a[72:75], v[204:207], v[40:43]\n"
    "v_mfma_f32_16x16x32_bf16 v[44:47], a[76:79], v[204:207], v[44:47]\n"
    "s_waitcnt vmcnt(9)\n"
    "s_barrier\n"
    "ds_read_b128 v[224:227], v11 offset:24576\n"
    "v_mfma_f32_16x16x32_bf16 v[36:39], a[76:79], v[200:203], v[36:39]\n"
    "ds_read_b128 v[228:231], v11 offset:24832\n"
    "s_waitcnt lgkmcnt(5)\n"
    "v_mfma_f32_16x16x32_bf16 v[32:35], a[80:83], v[208:211], v[32:35]\n"
    "s_waitcnt lgkmcnt(4)\n"
    "v_mfma_f32_16x16x32_bf16 v[40:43], a[80:83], v[212:215], v[40:43]\n"
    "v_mfma_f32_16x16x32_bf16 v[44:47], a[84:87], v[212:215], v[44:47]\n"
    "ds_read_b128 v[232:235], v11 offset:26624\n"
    "v_mfma_f32_16x16x32_bf16 v[36:39], a[84:87], v[208:211], v[36:39]\n"
    "ds_read_b128 v[236:239], v11 offset:26880\n"
    "s_waitcnt lgkmcnt(5)\n"
    "v_mfma_f32_16x16x32_bf16 v[32:35], a[88:91], v[216:219], v[32:35]\n"
    "s_waitcnt lgkmcnt(4)\n"
    "v_mfma_f32_16x16x32_bf16 v[40:43], a[88:91], v[220:223], v[40:43]\n"
    "v_mfma_f32_16x16x32_bf16 v[44:47], a[92:95], v[220:223], v[44:47]\n"
    "ds_read_b128 v[240:243], v11 offset:28672\n"
    "v_mfma_f32_16x16x32_bf16 v[36:39], a[92:95], v[216:219], v[36:39]\n"
    "ds_read_b128 v[244:247], v11 offset:28928\n"
    "s_waitcnt lgkmcnt(5)\n"
    "v_mfma_f32_16x16x32_bf16 v[32:35], a[96:99], v[224:227], v[32:35]\n"
    "s_waitcnt lgkmcnt(4)\n"
    "v_mfma_f32_16x16x32_bf16 v[40:43], a[96:99], v[228:231], v[40:43]\n"
    "v_mfma_f32_16x16x32_bf16 v[44:47], a[100:103], v[228:231], v[44:47]\n"
    "ds_read_b128 v[132:135], v11 offset:30720\n"
    "v_mfma_f32_16x16x32_bf16 v[36:39], a[100:103], v[224:227], v[36:39]\n"
    "ds_read_b128 v[128:131], v11 offset:30976\n"
    "s_waitcnt lgkmcnt(5)\n"
    "v_mfma_f32_16x16x32_bf16 v[32:35], a[104:107], v[232:235], v[32:35]\n"
    "s_waitcnt lgkmcnt(4)\n"
    "v_mfma_f32_16x16x32_bf16 v[40:43], a[104:107], v[236:239], v[40:43]\n"
    "v_mfma_f32_16x16x32_bf16 v[44:47], a[108:111], v[236:239], v[44:47]\n"
    "s_waitcnt vmcnt(6)\n"
    "s_barrier\n"
    "ds_read_b128 v[140:143], v11 offset:32768\n"
    "v_mfma_f32_16x16x32_bf16 v[36:39], a[108:111], v[232:235], v[36:39]\n"
    "ds_read_b128 v[136:139], v11 offset:33024\n"
    "s_waitcnt lgkmcnt(5)\n"
    "v_mfma_f32_16x16x32_bf16 v[32:35], a[112:115], v[240:243], v[32:35]\n"
    "s_waitcnt lgkmcnt(4)\n"
    "v_mfma_f32_16x16x32_bf16 v[40:43], a[112:115], v[244:247], v[40:43]\n"
    "v_mfma_f32_16x16x32_bf16 v[44:47], a[116:119], v[244:247], v[44:47]\n"
    "ds_read_b128 v[148:151], v11 offset:34816\n"
    "v_mfma_f32_16x16x32_bf16 v[36:39], a[116:119], v[240:243], v[36:39]\n"
    "ds_read_b128 v[144:147], v11 offset:35072\n"
    "s_waitcnt lgkmcnt(5)\n"
    "v_mfma_f32_16x16x32_bf16 v[32:35], a[120:123], v[132:135], v[32:35]\n"
    "s_waitcnt lgkmcnt(4)\n"
    "v_mfma_f32_16x16x32_bf16 v[40:43], a[120:123], v[128:131], v[40:43]\n"
    "s_lshr_b32 s67, s48, 8\n"
    "s_xor_b32 s67, s67, s52\n"
    "v_mfma_f32_16x16x32_bf16 v[44:47], a[124:127], v[128:131], v[44:47]\n"
    "ds_read_b128 v[156:159], v11 offset:36864\n"
    "v_mfma_f32_16x16x32_bf16 v[36:39], a[124:127], v[132:135], v[36:39]\n"
    "ds_read_b128 v[152:155], v11 offset:37120\n"
    "s_and_b32 s67, s67, 0x2000\n"
    "s_or_b32 s67, s67, s48\n"
    "s_waitcnt lgkmcnt(5)\n"
    "v_mfma_f32_16x16x32_bf16 v[32:35], a[128:131], v[140:143], v[32:35]\n"
    "s_waitcnt lgkmcnt(4)\n"
    "v_mfma_f32_16x16x32_bf16 v[40:43], a[128:131], v[136:139], v[40:43]\n"
    "s_cmp_eq_u32 s60, 63\n"
    "s_cselect_b32 s67, s38, s67\n"
    "v_mfma_f32_16x16x32_bf16 v[44:47], a[132:135], v[136:139], v[44:47]\n"
    "ds_read_b128 v[164:167], v11 offset:38912\n"
    "v_mfma_f32_16x16x32_bf16 v[36:39], a[132:135], v[140:143], v[36:39]\n"
    "ds_read_b128 v[160:163], v11 offset:39168\n"
    "buffer_store_dwordx4 v[20:23], v6, s[36:39], s67 offen offset:0 sc1\n"
    "s_waitcnt lgkmcnt(5)\n"
    "v_mfma_f32_16x16x32_bf16 v[32:35], a[136:139], v[148:151], v[32:35]\n"
    "s_waitcnt lgkmcnt(4)\n"
    "v_mfma_f32_16x16x32_bf16 v[40:43], a[136:139], v[144:147], v[40:43]\n"
    "v_mfma_f32_16x16x32_bf16 v[44:47], a[140:143], v[144:147], v[44:47]\n"
    "s_waitcnt vmcnt(5)\n"
    "s_barrier\n"
    "ds_read_b128 v[172:175], v11 offset:40960\n"
    "v_mfma_f32_16x16x32_bf16 v[36:39], a[140:143], v[148:151], v[36:39]\n"
    "ds_read_b128 v[168:171], v11 offset:41216\n"
    "s_waitcnt lgkmcnt(5)\n"
    "v_mfma_f32_16x16x32_bf16 v[32:35], a[144:147], v[156:159], v[32:35]\n"
    "s_waitcnt lgkmcnt(4)\n"
    "v_mfma_f32_16x16x32_bf16 v[40:43], a[144:147], v[152:155], v[40:43]\n"
    "buffer_store_dwordx4 v[20:23], v6, s[36:39], s67 offen offset:256 sc1\n"
    "v_mfma_f32_16x16x32_bf16 v[44:47], a[148:151], v[152:155], v[44:47]\n"
    "ds_read_b128 v[180:183], v11 offset:43008\n"
    "v_mfma_f32_16x16x32_bf16 v[36:39], a[148:151], v[156:159], v[36:39]\n"
    "ds_read_b128 v[176:179], v11 offset:43264\n"
    "s_waitcnt lgkmcnt(5)\n"
    "v_mfma_f32_16x16x32_bf16 v[32:35], a[152:155], v[164:167], v[32:35]\n"
    "s_waitcnt lgkmcnt(4)\n"
    "v_mfma_f32_16x16x32_bf16 v[40:43], a[152:155], v[160:163], v[40:43]\n"
    "v_mfma_f32_16x16x32_bf16 v[44:47], a[156:159], v[160:163], v[44:47]\n"
    "ds_read_b128 v[188:191], v11 offset:45056\n"
    "v_mfma_f32_16x16x32_bf16 v[36:39], a[156:159], v[164:167], v[36:39]\n"
    "ds_read_b128 v[184:187], v11 offset:45312\n"
    "s_waitcnt lgkmcnt(5)\n"
    "v_mfma_f32_16x16x32_bf16 v[32:35], a[160:163], v[172:175], v[32:35]\n"
    "s_waitcnt lgkmcnt(4)\n"
    "v_mfma_f32_16x16x32_bf16 v[40:43], a[160:163], v[168:171], v[40:43]\n"
    "v_mfma_f32_16x16x32_bf16 v[44:47], a[164:167], v[168:171], v[44:47]\n"
    "ds_read_b128 v[196:199], v11 offset:47104\n"
    "v_mfma_f32_16x16x32_bf16 v[36:39], a[164:167], v[172:175], v[36:39]\n"
    "ds_read_b128 v[192:195], v11 offset:47360\n"
    "s_waitcnt lgkmcnt(5)\n"
    "v_mfma_f32_16x16x32_bf16 v[32:35], a[168:171], v[180:183], v[32:35]\n"
    "s_waitcnt lgkmcnt(4)\n"
    "v_mfma_f32_16x16x32_bf16 v[40:43], a[168:171], v[176:179], v[40:43]\n"
    "v_mfma_f32_16x16x32_bf16 v[44:47], a[172:175], v[176:179], v[44:47]\n"
    "s_waitcnt vmcnt(4)\n"
    "s_barrier\n"
    "ds_read_b128 v[204:207], v11 offset:49152\n"
    "v_mfma_f32_16x16x32_bf16 v[36:39], a[172:175], v[180:183], v[36:39]\n"
    "ds_read_b128 v[200:203], v11 offset:49408\n"
    "s_waitcnt lgkmcnt(5)\n"
    "v_mfma_f32_16x16x32_bf16 v[32:35], a[176:179], v[188:191], v[32:35]\n"
    "s_waitcnt lgkmcnt(4)\n"
    "v_mfma_f32_16x16x32_bf16 v[40:43], a[176:179], v[184:187], v[40:43]\n"
    "v_mfma_f32_16x16x32_bf16 v[44:47], a[180:183], v[184:187], v[44:47]\n"
    "ds_read_b128 v[212:215], v11 offset:51200\n"
    "v_mfma_f32_16x16x32_bf16 v[36:39], a[180:183], v[188:191], v[36:39]\n"
    "ds_read_b128 v[208:211], v11 offset:51456\n"
    "s_waitcnt lgkmcnt(5)\n"
    "v_mfma_f32_16x16x32_bf16 v[32:35], a[184:187], v[196:199], v[32:35]\n"
    "s_waitcnt lgkmcnt(4)\n"
    "v_mfma_f32_16x16x32_bf16 v[40:43], a[184:187], v[192:195], v[40:43]\n"
    "v_mfma_f32_16x16x32_bf16 v[44:47], a[188:191], v[192:195], v[44:47]\n"
    "ds_read_b128 v[220:223], v11 offset:53248\n"
    "v_mfma_f32_16x16x32_bf16 v[36:39], a[188:191], v[196:199], v[36:39]\n"
    "ds_read_b128 v[216:219], v11 offset:53504\n"
    "s_waitcnt lgkmcnt(5)\n"
    "v_mfma_f32_16x16x32_bf16 v[32:35], a[192:195], v[204:207], v[32:35]\n"
    "s_waitcnt lgkmcnt(4)\n"
    "v_mfma_f32_16x16x32_bf16 v[40:43], a[192:195], v[200:203], v[40:43]\n"
    "v_mfma_f32_16x16x32_bf16 v[44:47], a[196:199], v[200:203], v[44:47]\n"
    "ds_read_b128 v[228:231], v11 offset:55296\n"
    "v_mfma_f32_16x16x32_bf16 v[36:39], a[196:199], v[204:207], v[36:39]\n"
    "ds_read_b128 v[224:227], v11 offset:55552\n"
    "s_waitcnt lgkmcnt(5)\n"
    "v_mfma_f32_16x16x32_bf16 v[32:35], a[200:203], v[212:215], v[32:35]\n"
    "s_waitcnt lgkmcnt(4)\n"
    "v_mfma_f32_16x16x32_bf16 v[40:43], a[200:203], v[208:211], v[40:43]\n"
    "v_mfma_f32_16x16x32_bf16 v[44:47], a[204:207], v[208:211], v[44:47]\n"
    "s_waitcnt vmcnt(2)\n"
    "s_barrier\n"
    "ds_read_b128 v[236:239], v11 offset:57344\n"
    "v_mfma_f32_16x16x32_bf16 v[36:39], a[204:207], v[212:215], v[36:39]\n"
    "ds_read_b128 v[232:235], v11 offset:57600\n"
    "s_waitcnt lgkmcnt(5)\n"
    "v_mfma_f32_16x16x32_bf16 v[32:35], a[208:211], v[220:223], v[32:35]\n"
    "s_waitcnt lgkmcnt(4)\n"
    "v_mfma_f32_16x16x32_bf16 v[40:43], a[208:211], v[216:219], v[40:43]\n"
    "v_mfma_f32_16x16x32_bf16 v[44:47], a[212:215], v[216:219], v[44:47]\n"
    "ds_read_b128 v[244:247], v11 offset:59392\n"
    "v_mfma_f32_16x16x32_bf16 v[36:39], a[212:215], v[220:223], v[36:39]\n"
    "s_waitcnt lgkmcnt(4)\n"
    "v_mfma_f32_16x16x32_bf16 v[32:35], a[216:219], v[228:231], v[32:35]\n"
    "ds_read_b128 v[240:243], v11 offset:61440\n"
    "s_waitcnt lgkmcnt(4)\n"
    "v_mfma_f32_16x16x32_bf16 v[40:43], a[216:219], v[224:227], v[40:43]\n"
    "v_mfma_f32_16x16x32_bf16 v[44:47], a[220:223], v[224:227], v[44:47]\n"
    "ds_read_b128 v[128:131], v11 offset:63488\n"
    "v_mfma_f32_16x16x32_bf16 v[36:39], a[220:223], v[228:231], v[36:39]\n"
    "s_waitcnt lgkmcnt(4)\n"
    "v_mfma_f32_16x16x32_bf16 v[32:35], a[224:227], v[236:239], v[32:35]\n"
    "ds_read_b128 v[132:135], v11 offset:59648\n"
    "s_waitcnt lgkmcnt(4)\n"
    "v_mfma_f32_16x16x32_bf16 v[40:43], a[224:227], v[232:235], v[40:43]\n"
    "v_mfma_f32_16x16x32_bf16 v[44:47], a[228:231], v[232:235], v[44:47]\n"
    "ds_read_b128 v[136:139], v11 offset:61696\n"
    "v_mfma_f32_16x16x32_bf16 v[36:39], a[228:231], v[236:239], v[36:39]\n"
    "s_waitcnt lgkmcnt(4)\n"
    "v_mfma_f32_16x16x32_bf16 v[32:35], a[232:235], v[244:247], v[32:35]\n"
    "ds_read_b128 v[140:143], v11 offset:63744\n"
    "v_mfma_f32_16x16x32_bf16 v[36:39], a[236:239], v[244:247], v[36:39]\n"
    "buffer_load_dword v9, v4, s[36:39], 0 offen sc1\n"
    "s_waitcnt lgkmcnt(4)\n"
    "v_mfma_f32_16x16x32_bf16 v[32:35], a[240:243], v[240:243], v[32:35]\n"
    "v_mfma_f32_16x16x32_bf16 v[36:39], a[244:247], v[240:243], v[36:39]\n"
    "s_waitcnt lgkmcnt(3)\n"
    "v_mfma_f32_16x16x32_bf16 v[32:35], a[248:251], v[128:131], v[32:35]\n"
    "v_mfma_f32_16x16x32_bf16 v[36:39], a[252:255], v[128:131], v[36:39]\n"
    "s_waitcnt lgkmcnt(2)\n"
    "v_mfma_f32_16x16x32_bf16 v[40:43], a[232:235], v[132:135], v[40:43]\n"
    "v_mfma_f32_16x16x32_bf16 v[44:47], a[236:239], v[132:135], v[44:47]\n"
    "v_cvt_pk_bf16_f32 v96, v32, v33\n"
    "v_cvt_pk_bf16_f32 v97, v34, v35\n"
    "s_waitcnt lgkmcnt(1)\n"
    "v_mfma_f32_16x16x32_bf16 v[40:43], a[240:243], v[136:139], v[40:43]\n"
    "v_cvt_pk_bf16_f32 v98, v36, v37\n"
    "v_cvt_pk_bf16_f32 v99, v38, v39\n"
    "v_mfma_f32_16x16x32_bf16 v[44:47], a[244:247], v[136:139], v[44:47]\n"
    "buffer_store_dwordx4 v[96:99], v6, s[36:39], s69 offen offset:0 sc1\n"
    "s_waitcnt lgkmcnt(0)\n"
    "v_mfma_f32_16x16x32_bf16 v[40:43], a[248:251], v[140:143], v[40:43]\n"
    "v_mfma_f32_16x16x32_bf16 v[44:47], a[252:255], v[140:143], v[44:47]\n"
    "s_nop 3\n"
    "v_cvt_pk_bf16_f32 v100, v40, v41\n"
    "v_cvt_pk_bf16_f32 v101, v42, v43\n"
    "s_nop 1\n"
    "v_cvt_pk_bf16_f32 v102, v44, v45\n"
    "v_cvt_pk_bf16_f32 v103, v46, v47\n"
    "buffer_store_dwordx4 v[100:103], v6, s[36:39], s69 offen offset:256 sc1\n"
    "s_barrier\n"
    "buffer_store_dword v12, v5, s[36:39], 0 offen sc1\n"
    "s_add_u32 s60, s60, 1\n"
    "s_add_u32 s61, s61, 1\n"
    "s_xor_b32 s62, s62, 0x10000\n"
    "s_mov_b32 s63, 0\n"
    "s_add_u32 s71, s71, 1\n"
    "s_cmp_eq_u32 s60, 64\n"
    "s_cselect_b32 s71, 1000, s71\n"
    "s_lshr_b32 s68, s46, 8\n"
    "s_xor_b32 s68, s68, s51\n"
    "s_and_b32 s68, s68, 0x2000\n"
    "s_or_b32 s68, s68, s46\n"
    "s_add_u32 s66, s53, s62\n"
    "s_mov_b32 m0, s66\n"
    "v_add_u32 v11, s62, v3\n"
    "s_add_u32 s46, s46, s47\n"
    "s_waitcnt vmcnt(3)\n"
    "v_max_u32 v8, v8, v9\n"
    "s_cmp_lt_u32 s60, 64\n"
    "s_cbranch_scc1 L_WO_LOOPA%=\n"
    "s_branch L_WO_END%=\n"
    "L_WO_WAf%=:\n"
    "s_nop 0\n"
    "s_cmp_lg_u32 s65, 0\n"
    "s_cbranch_scc1 L_WO_WAok%=\n"
    "s_add_u32 s64, s64, 1\n"
    "s_and_b32 s70, s64, 255\n"
    "s_cbranch_scc1 L_WO_WA%=\n"
    "buffer_load_dword v10, off, s[36:39], s50 sc1\n"
    "s_waitcnt vmcnt(0)\n"
    "v_readfirstlane_b32 s65, v10\n"
    "s_cmp_lt_u32 s64, 0x40000\n"
    "s_cbranch_scc1 L_WO_WA%=\n"
    "s_mov_b32 s65, 1\n"
    "v_mov_b32 v10, 1\n"
    "buffer_store_dword v10, off, s[36:39], s50 sc1\n"
    "s_branch L_WO_WA%=\n"
    "L_WO_END%=:\n"
    "s_waitcnt vmcnt(0)\n"
    "s_endpgm\n"
      GEMM_OPS : ROLE_CLOB);
  } else if constexpr (KIND == 3) {
    asm volatile("s_mov_b64 exec, -1\ns_and_b32 s37, s37, 0xffff\ns_mov_b32 s39, 0x20000\ns_nop 4\n"
    "s_mov_b32 s64, 0\n"
    "s_mov_b32 s65, 0\n"
    "s_add_u32 m0, s45, 0\n"
    "s_nop 0\n"
    "global_load_lds_dwordx4 v0, s[40:41] \n"
    "s_add_u32 s40, s40, s44\n"
    "s_addc_u32 s41, s41, 0\n"
    "s_add_u32 m0, s45, 1024\n"
    "s_nop 0\n"
    "global_load_lds_dwordx4 v0, s[40:41] \n"
    "s_add_u32 s40, s40, s44\n"
    "s_addc_u32 s41, s41, 0\n"
    "s_add_u32 m0, s45, 2048\n"
    "s_nop 0\n"
    "global_load_lds_dwordx4 v0, s[40:41] \n"
    "s_add_u32 s40, s40, s44\n"
    "s_addc_u32 s41, s41, 0\n"
    "s_add_u32 m0, s45, 3072\n"
    "s_nop 0\n"
    "global_load_lds_dwordx4 v0, s[40:41] \n"
    "s_add_u32 s40, s40, s44\n"
    "s_addc_u32 s41, s41, 0\n"
    "s_add_u32 m0, s45, 4096\n"
    "s_nop 0\n"
    "global_load_lds_dwordx4 v0, s[40:41] \n"
    "s_add_u32 s40, s40, s44\n"
    "s_addc_u32 s41, s41, 0\n"
    "s_add_u32 m0, s45, 5120\n"
    "s_nop 0\n"
    "global_load_lds_dwordx4 v0, s[40:41] \n"
    "s_add_u32 s40, s40, s44\n"
    "s_addc_u32 s41, s41, 0\n"
    "s_add_u32 m0, s45, 6144\n"
    "s_nop 0\n"
    "global_load_lds_dwordx4 v0, s[40:41] \n"
    "s_add_u32 s40, s40, s44\n"
    "s_addc_u32 s41, s41, 0\n"
    "s_add_u32 m0, s45, 7168\n"
    "s_nop 0\n"
    "global_load_lds_dwordx4 v0, s[40:41] \n"
    "s_add_u32 s40, s40, s44\n"
    "s_addc_u32 s41, s41, 0\n"
    "s_add_u32 m0, s45, 8192\n"
    "s_nop 0\n"
    "global_load_lds_dwordx4 v0, s[40:41] \n"
    "s_add_u32 s40, s40, s44\n"
    "s_addc_u32 s41, s41, 0\n"
    "s_add_u32 m0, s45, 9216\n"
    "s_nop 0\n"
    "global_load_lds_dwordx4 v0, s[40:41] \n"
    "s_add_u32 s40, s40, s44\n"
    "s_addc_u32 s41, s41, 0\n"
    "s_add_u32 m0, s45, 10240\n"
    "s_nop 0\n"
    "global_load_lds_dwordx4 v0, s[40:41] \n"
    "s_add_u32 s40, s40, s44\n"
    "s_addc_u32 s41, s41, 0\n"
    "s_add_u32 m0, s45, 11264\n"
    "s_nop 0\n"
    "global_load_lds_dwordx4 v0, s[40:41] \n"
    "s_add_u32 s40, s40, s44\n"
    "s_addc_u32 s41, s41, 0\n"
    "s_add_u32 m0, s45, 12288\n"
    "s_nop 0\n"
    "global_load_lds_dwordx4 v0, s[40:41] \n"
    "s_add_u32 s40, s40, s44\n"
    "s_addc_u32 s41, s41, 0\n"
    "s_add_u32 m0, s45, 13312\n"
    "s_nop 0\n"
    "global_load_lds_dwordx4 v0, s[40:41] \n"
    "s_add_u32 s40, s40, s44\n"
    "s_addc_u32 s41, s41, 0\n"
    "s_add_u32 m0, s45, 14336\n"
    "s_nop 0\n"
    "global_load_lds_dwordx4 v0, s[40:41] \n"
    "s_add_u32 s40, s40, s44\n"
    "s_addc_u32 s41, s41, 0\n"
    "s_add_u32 m0, s45, 15360\n"
    "s_nop 0\n"
    "global_load_lds_dwordx4 v0, s[40:41] \n"
    "s_add_u32 s40, s40, s44\n"
    "s_addc_u32 s41, s41, 0\n"
    "s_add_u32 m0, s45, 16384\n"
    "s_nop 0\n"
    "global_load_lds_dwordx4 v0, s[40:41] \n"
    "s_add_u32 s40, s40, s44\n"
    "s_addc_u32 s41, s41, 0\n"
    "s_add_u32 m0, s45, 17408\n"
    "s_nop 0\n"
    "global_load_lds_dwordx4 v0, s[40:41] \n"
    "s_add_u32 s40, s40, s44\n"
    "s_addc_u32 s41, s41, 0\n"
    "s_add_u32 m0, s45, 18432\n"
    "s_nop 0\n"
    "global_load_lds_dwordx4 v0, s[40:41] \n"
    "s_add_u32 s40, s40, s44\n"
    "s_addc_u32 s41, s41, 0\n"
    "s_add_u32 m0, s45, 19456\n"
    "s_nop 0\n"
    "global_load_lds_dwordx4 v0, s[40:41] \n"
    "s_add_u32 s40, s40, s44\n"
    "s_addc_u32 s41, s41, 0\n"
    "s_add_u32 m0, s45, 20480\n"
    "s_nop 0\n"
    "global_load_lds_dwordx4 v0, s[40:41] \n"
    "s_add_u32 s40, s40, s44\n"
    "s_addc_u32 s41, s41, 0\n"
    "s_add_u32 m0, s45, 21504\n"
    "s_nop 0\n"
    "global_load_lds_dwordx4 v0, s[40:41] \n"
    "s_add_u32 s40, s40, s44\n"
    "s_addc_u32 s41, s41, 0\n"
    "s_add_u32 m0, s45, 22528\n"
    "s_nop 0\n"
    "global_load_lds_dwordx4 v0, s[40:41] \n"
    "s_add_u32 s40, s40, s44\n"
    "s_addc_u32 s41, s41, 0\n"
    "s_add_u32 m0, s45, 23552\n"
    "s_nop 0\n"
    "global_load_lds_dwordx4 v0, s[40:41] \n"
    "s_add_u32 s40, s40, s44\n"
    "s_addc_u32 s41, s41, 0\n"
    "s_add_u32 m0, s45, 24576\n"
    "s_nop 0\n"
    "global_load_lds_dwordx4 v0, s[40:41] \n"
    "s_add_u32 s40, s40, s44\n"
    "s_addc_u32 s41, s41, 0\n"
    "s_add_u32 m0, s45, 25600\n"
    "s_nop 0\n"
    "global_load_lds_dwordx4 v0, s[40:41] \n"
    "s_add_u32 s40, s40, s44\n"
    "s_addc_u32 s41, s41, 0\n"
    "s_add_u32 m0, s45, 26624\n"
    "s_nop 0\n"
    "global_load_lds_dwordx4 v0, s[40:41] \n"
    "s_add_u32 s40, s40, s44\n"
    "s_addc_u32 s41, s41, 0\n"
    "s_add_u32 m0, s45, 27648\n"
    "s_nop 0\n"
    "global_load_lds_dwordx4 v0, s[40:41] \n"
    "s_add_u32 s40, s40, s44\n"
    "s_addc_u32 s41, s41, 0\n"
    "s_add_u32 m0, s45, 28672\n"
    "s_nop 0\n"
    "global_load_lds_dwordx4 v0, s[40:41] \n"
    "s_add_u32 s40, s40, s44\n"
    "s_addc_u32 s41, s41, 0\n"
    "s_add_u32 m0, s45, 29696\n"
    "s_nop 0\n"
    "global_load_lds_dwordx4 v0, s[40:41] \n"
    "s_add_u32 s40, s40, s44\n"
    "s_addc_u32 s41, s41, 0\n"
    "s_add_u32 m0, s45, 30720\n"
    "s_nop 0\n"
    "global_load_lds_dwordx4 v0, s[40:41] \n"
    "s_add_u32 s40, s40, s44\n"
    "s_addc_u32 s41, s41, 0\n"
    "s_add_u32 m0, s45, 31744\n"
    "s_nop 0\n"
    "global_load_lds_dwordx4 v0, s[40:41] \n"
    "s_add_u32 s40, s40, s44\n"
    "s_addc_u32 s41, s41, 0\n"
    "s_waitcnt vmcnt(28)\n"
    "ds_read_b64_tr_b16 a[0:1], v1 offset:0\n"
    "ds_read_b64_tr_b16 a[2:3], v1 offset:256\n"
    "ds_read_b64_tr_b16 a[4:5], v1 offset:8\n"
    "ds_read_b64_tr_b16 a[6:7], v1 offset:264\n"
    "ds_read_b64_tr_b16 a[8:9], v1 offset:2048\n"
    "ds_read_b64_tr_b16 a[10:11], v1 offset:2304\n"
    "ds_read_b64_tr_b16 a[12:13], v1 offset:2056\n"
    "ds_read_b64_tr_b16 a[14:15], v1 offset:2312\n"
    "s_waitcnt lgkmcnt(0)\n"
    "s_add_u32 m0, s45, 0\n"
    "s_nop 0\n"
    "global_load_lds_dwordx4 v0, s[42:43] \n"
    "s_add_u32 s42, s42, s44\n"
    "s_addc_u32 s43, s43, 0\n"
    "s_add_u32 m0, s45, 1024\n"
    "s_nop 0\n"
    "global_load_lds_dwordx4 v0, s[42:43] \n"
    "s_add_u32 s42, s42, s44\n"
    "s_addc_u32 s43, s43, 0\n"
    "s_add_u32 m0, s45, 2048\n"
    "s_nop 0\n"
    "global_load_lds_dwordx4 v0, s[42:43] \n"
    "s_add_u32 s42, s42, s44\n"
    "s_addc_u32 s43, s43, 0\n"
    "s_add_u32 m0, s45, 3072\n"
    "s_nop 0\n"
    "global_load_lds_dwordx4 v0, s[42:43] \n"
    "s_add_u32 s42, s42, s44\n"
    "s_addc_u32 s43, s43, 0\n"
    "s_waitcnt vmcnt(28)\n"
    "ds_read_b64_tr_b16 a[16:17], v1 offset:4096\n"
    "ds_read_b64_tr_b16 a[18:19], v1 offset:4352\n"
    "ds_read_b64_tr_b16 a[20:21], v1 offset:4104\n"
    "ds_read_b64_tr_b16 a[22:23], v1 offset:4360\n"
    "ds_read_b64_tr_b16 a[24:25], v1 offset:6144\n"
    "ds_read_b64_tr_b16 a[26:27], v1 offset:6400\n"
    "ds_read_b64_tr_b16 a[28:29], v1 offset:6152\n"
    "ds_read_b64_tr_b16 a[30:31], v1 offset:6408\n"
    "s_waitcnt lgkmcnt(0)\n"
    "s_add_u32 m0, s45, 4096\n"
    "s_nop 0\n"
    "global_load_lds_dwordx4 v0, s[42:43] \n"
    "s_add_u32 s42, s42, s44\n"
    "s_addc_u32 s43, s43, 0\n"
    "s_add_u32 m0, s45, 5120\n"
    "s_nop 0\n"
    "global_load_lds_dwordx4 v0, s[42:43] \n"
    "s_add_u32 s42, s42, s44\n"
    "s_addc_u32 s43, s43, 0\n"
    "s_add_u32 m0, s45, 6144\n"
    "s_nop 0\n"
    "global_load_lds_dwordx4 v0, s[42:43] \n"
    "s_add_u32 s42, s42, s44\n"
    "s_addc_u32 s43, s43, 0\n"
    "s_add_u32 m0, s45, 7168\n"
    "s_nop 0\n"
    "global_load_lds_dwordx4 v0, s[42:43] \n"
    "s_add_u32 s42, s42, s44\n"
    "s_addc_u32 s43, s43, 0\n"
    "s_waitcnt vmcnt(28)\n"
    "ds_read_b64_tr_b16 a[32:33], v1 offset:8192\n"
    "ds_read_b64_tr_b16 a[34:35], v1 offset:8448\n"
    "ds_read_b64_tr_b16 a[36:37], v1 offset:8200\n"
    "ds_read_b64_tr_b16 a[38:39], v1 offset:8456\n"
    "ds_read_b64_tr_b16 a[40:41], v1 offset:10240\n"
    "ds_read_b64_tr_b16 a[42:43], v1 offset:10496\n"
    "ds_read_b64_tr_b16 a[44:45], v1 offset:10248\n"
    "ds_read_b64_tr_b16 a[46:47], v1 offset:10504\n"
    "s_waitcnt lgkmcnt(0)\n"
    "s_add_u32 m0, s45, 8192\n"
    "s_nop 0\n"
    "global_load_lds_dwordx4 v0, s[42:43] \n"
    "s_add_u32 s42, s42, s44\n"
    "s_addc_u32 s43, s43, 0\n"
    "s_add_u32 m0, s45, 9216\n"
    "s_nop 0\n"
    "global_load_lds_dwordx4 v0, s[42:43] \n"
    "s_add_u32 s42, s42, s44\n"
    "s_addc_u32 s43, s43, 0\n"
    "s_add_u32 m0, s45, 10240\n"
    "s_nop 0\n"
    "global_load_lds_dwordx4 v0, s[42:43] \n"
    "s_add_u32 s42, s42, s44\n"
    "s_addc_u32 s43, s43, 0\n"
    "s_add_u32 m0, s45, 11264\n"
    "s_nop 0\n"
    "global_load_lds_dwordx4 v0, s[42:43] \n"
    "s_add_u32 s42, s42, s44\n"
    "s_addc_u32 s43, s43, 0\n"
    "s_waitcnt vmcnt(28)\n"
    "ds_read_b64_tr_b16 a[48:49], v1 offset:12288\n"
    "ds_read_b64_tr_b16 a[50:51], v1 offset:12544\n"
    "ds_read_b64_tr_b16 a[52:53], v1 offset:12296\n"
    "ds_read_b64_tr_b16 a[54:55], v1 offset:12552\n"
    "ds_read_b64_tr_b16 a[56:57], v1 offset:14336\n"
    "ds_read_b64_tr_b16 a[58:59], v1 offset:14592\n"
    "ds_read_b64_tr_b16 a[60:61], v1 offset:14344\n"
    "ds_read_b64_tr_b16 a[62:63], v1 offset:14600\n"
    "s_waitcnt lgkmcnt(0)\n"
    "s_add_u32 m0, s45, 12288\n"
    "s_nop 0\n"
    "global_load_lds_dwordx4 v0, s[42:43] \n"
    "s_add_u32 s42, s42, s44\n"
    "s_addc_u32 s43, s43, 0\n"
    "s_add_u32 m0, s45, 13312\n"
    "s_nop 0\n"
    "global_load_lds_dwordx4 v0, s[42:43] \n"
    "s_add_u32 s42, s42, s44\n"
    "s_addc_u32 s43, s43, 0\n"
    "s_add_u32 m0, s45, 14336\n"
    "s_nop 0\n"
    "global_load_lds_dwordx4 v0, s[42:43] \n"
    "s_add_u32 s42, s42, s44\n"
    "s_addc_u32 s43, s43, 0\n"
    "s_add_u32 m0, s45, 15360\n"
    "s_nop 0\n"
    "global_load_lds_dwordx4 v0, s[42:43] \n"
    "s_add_u32 s42, s42, s44\n"
    "s_addc_u32 s43, s43, 0\n"
    "s_waitcnt vmcnt(28)\n"
    "ds_read_b64_tr_b16 a[64:65], v1 offset:16384\n"
    "ds_read_b64_tr_b16 a[66:67], v1 offset:16640\n"
    "ds_read_b64_tr_b16 a[68:69], v1 offset:16392\n"
    "ds_read_b64_tr_b16 a[70:71], v1 offset:16648\n"
    "ds_read_b64_tr_b16 a[72:73], v1 offset:18432\n"
    "ds_read_b64_tr_b16 a[74:75], v1 offset:18688\n"
    "ds_read_b64_tr_b16 a[76:77], v1 offset:18440\n"
    "ds_read_b64_tr_b16 a[78:79], v1 offset:18696\n"
    "s_waitcnt lgkmcnt(0)\n"
    "s_add_u32 m0, s45, 16384\n"
    "s_nop 0\n"
    "global_load_lds_dwordx4 v0, s[42:43] \n"
    "s_add_u32 s42, s42, s44\n"
    "s_addc_u32 s43, s43, 0\n"
    "s_add_u32 m0, s45, 17408\n"
    "s_nop 0\n"
    "global_load_lds_dwordx4 v0, s[42:43] \n"
    "s_add_u32 s42, s42, s44\n"
    "s_addc_u32 s43, s43, 0\n"
    "s_add_u32 m0, s45, 18432\n"
    "s_nop 0\n"
    "global_load_lds_dwordx4 v0, s[42:43] \n"
    "s_add_u32 s42, s42, s44\n"
    "s_addc_u32 s43, s43, 0\n"
    "s_add_u32 m0, s45, 19456\n"
    "s_nop 0\n"
    "global_load_lds_dwordx4 v0, s[42:43] \n"
    "s_add_u32 s42, s42, s44\n"
    "s_addc_u32 s43, s43, 0\n"
    "s_waitcnt vmcnt(28)\n"
    "ds_read_b64_tr_b16 a[80:81], v1 offset:20480\n"
    "ds_read_b64_tr_b16 a[82:83], v1 offset:20736\n"
    "ds_read_b64_tr_b16 a[84:85], v1 offset:20488\n"
    "ds_read_b64_tr_b16 a[86:87], v1 offset:20744\n"
    "ds_read_b64_tr_b16 a[88:89], v1 offset:22528\n"
    "ds_read_b64_tr_b16 a[90:91], v1 offset:22784\n"
    "ds_read_b64_tr_b16 a[92:93], v1 offset:22536\n"
    "ds_read_b64_tr_b16 a[94:95], v1 offset:22792\n"
    "s_waitcnt lgkmcnt(0)\n"
    "s_add_u32 m0, s45, 20480\n"
    "s_nop 0\n"
    "global_load_lds_dwordx4 v0, s[42:43] \n"
    "s_add_u32 s42, s42, s44\n"
    "s_addc_u32 s43, s43, 0\n"
    "s_add_u32 m0, s45, 21504\n"
    "s_nop 0\n"
    "global_load_lds_dwordx4 v0, s[42:43] \n"
    "s_add_u32 s42, s42, s44\n"
    "s_addc_u32 s43, s43, 0\n"
    "s_add_u32 m0, s45, 22528\n"
    "s_nop 0\n"
    "global_load_lds_dwordx4 v0, s[42:43] \n"
    "s_add_u32 s42, s42, s44\n"
    "s_addc_u32 s43, s43, 0\n"
    "s_add_u32 m0, s45, 23552\n"
    "s_nop 0\n"
    "global_load_lds_dwordx4 v0, s[42:43] \n"
    "s_add_u32 s42, s42, s44\n"
    "s_addc_u32 s43, s43, 0\n"
    "s_waitcnt vmcnt(28)\n"
    "ds_read_b64_tr_b16 a[96:97], v1 offset:24576\n"
    "ds_read_b64_tr_b16 a[98:99], v1 offset:24832\n"
    "ds_read_b64_tr_b16 a[100:101], v1 offset:24584\n"
    "ds_read_b64_tr_b16 a[102:103], v1 offset:24840\n"
    "ds_read_b64_tr_b16 a[104:105], v1 offset:26624\n"
    "ds_read_b64_tr_b16 a[106:107], v1 offset:26880\n"
    "ds_read_b64_tr_b16 a[108:109], v1 offset:26632\n"
    "ds_read_b64_tr_b16 a[110:111], v1 offset:26888\n"
    "s_waitcnt vmcnt(24)\n"
    "ds_read_b64_tr_b16 a[112:113], v1 offset:28672\n"
    "ds_read_b64_tr_b16 a[114:115], v1 offset:28928\n"
    "ds_read_b64_tr_b16 a[116:117], v1 offset:28680\n"
    "ds_read_b64_tr_b16 a[118:119], v1 offset:28936\n"
    "ds_read_b64_tr_b16 a[120:121], v1 offset:30720\n"
    "ds_read_b64_tr_b16 a[122:123], v1 offset:30976\n"
    "ds_read_b64_tr_b16 a[124:125], v1 offset:30728\n"
    "ds_read_b64_tr_b16 a[126:127], v1 offset:30984\n"
    "s_waitcnt vmcnt(20)\n"
    "ds_read_b64_tr_b16 a[128:129], v1 offset:0\n"
    "ds_read_b64_tr_b16 a[130:131], v1 offset:256\n"
    "ds_read_b64_tr_b16 a[132:133], v1 offset:8\n"
    "ds_read_b64_tr_b16 a[134:135], v1 offset:264\n"
    "ds_read_b64_tr_b16 a[136:137], v1 offset:2048\n"
    "ds_read_b64_tr_b16 a[138:139], v1 offset:2304\n"
    "ds_read_b64_tr_b16 a[140:141], v1 offset:2056\n"
    "ds_read_b64_tr_b16 a[142:143], v1 offset:2312\n"
    "s_waitcnt vmcnt(16)\n"
    "ds_read_b64_tr_b16 a[144:145], v1 offset:4096\n"
    "ds_read_b64_tr_b16 a[146:147], v1 offset:4352\n"
    "ds_read_b64_tr_b16 a[148:149], v1 offset:4104\n"
    "ds_read_b64_tr_b16 a[150:151], v1 offset:4360\n"
    "ds_read_b64_tr_b16 a[152:153], v1 offset:6144\n"
    "ds_read_b64_tr_b16 a[154:155], v1 offset:6400\n"
    "ds_read_b64_tr_b16 a[156:157], v1 offset:6152\n"
    "ds_read_b64_tr_b16 a[158:159], v1 offset:6408\n"
    "s_waitcnt vmcnt(12)\n"
    "ds_read_b64_tr_b16 a[160:161], v1 offset:8192\n"
    "ds_read_b64_tr_b16 a[162:163], v1 offset:8448\n"
    "ds_read_b64_tr_b16 a[164:165], v1 offset:8200\n"
    "ds_read_b64_tr_b16 a[166:167], v1 offset:8456\n"
    "ds_read_b64_tr_b16 a[168:169], v1 offset:10240\n"
    "ds_read_b64_tr_b16 a[170:171], v1 offset:10496\n"
    "ds_read_b64_tr_b16 a[172:173], v1 offset:10248\n"
    "ds_read_b64_tr_b16 a[174:175], v1 offset:10504\n"
    "s_waitcnt vmcnt(8)\n"
    "ds_read_b64_tr_b16 a[176:177], v1 offset:12288\n"
    "ds_read_b64_tr_b16 a[178:179], v1 offset:12544\n"
    "ds_read_b64_tr_b16 a[180:181], v1 offset:12296\n"
    "ds_read_b64_tr_b16 a[182:183], v1 offset:12552\n"
    "ds_read_b64_tr_b16 a[184:185], v1 offset:14336\n"
    "ds_read_b64_tr_b16 a[186:187], v1 offset:14592\n"
    "ds_read_b64_tr_b16 a[188:189], v1 offset:14344\n"
    "ds_read_b64_tr_b16 a[190:191], v1 offset:14600\n"
    "s_waitcnt vmcnt(4)\n"
    "ds_read_b64_tr_b16 a[192:193], v1 offset:16384\n"
    "ds_read_b64_tr_b16 a[194:195], v1 offset:16640\n"
    "ds_read_b64_tr_b16 a[196:197], v1 offset:16392\n"
    "ds_read_b64_tr_b16 a[198:199], v1 offset:16648\n"
    "ds_read_b64_tr_b16 a[200:201], v1 offset:18432\n"
    "ds_read_b64_tr_b16 a[202:203], v1 offset:18688\n"
    "ds_read_b64_tr_b16 a[204:205], v1 offset:18440\n"
    "ds_read_b64_tr_b16 a[206:207], v1 offset:18696\n"
    "s_waitcnt vmcnt(0)\n"
    "ds_read_b64_tr_b16 a[208:209], v1 offset:20480\n"
    "ds_read_b64_tr_b16 a[210:211], v1 offset:20736\n"
    "ds_read_b64_tr_b16 a[212:213], v1 offset:20488\n"
    "ds_read_b64_tr_b16 a[214:215], v1 offset:20744\n"
    "ds_read_b64_tr_b16 a[216:217], v1 offset:22528\n"
    "ds_read_b64_tr_b16 a[218:219], v1 offset:22784\n"
    "ds_read_b64_tr_b16 a[220:221], v1 offset:22536\n"
    "ds_read_b64_tr_b16 a[222:223], v1 offset:22792\n"
    "s_waitcnt lgkmcnt(0)\n"
    "s_barrier\n"
    "v_mov_b32 v8, 0\n"
    "v_mov_b32 v13, 0x80000000\n"
    "s_sub_u32 s71, 1, s54\n"
    "s_mov_b32 s60, 0\n"
    "s_mov_b32 s61, 1\n"
    "s_mov_b32 s62, 0\n"
    "s_mov_b32 s63, 0\n"
    "s_bfe_u32 s17, s45, 0x2000f\n"
    "s_cmp_eq_u32 s17, 1\n"
    "s_cselect_b32 s17, 1, 0\n"
    "L_DN_GO%=:\n"
    "s_lshr_b32 s68, s46, 8\n"
    "s_xor_b32 s68, s68, s51\n"
    "s_and_b32 s68, s68, 0x2000\n"
    "s_or_b32 s68, s68, s46\n"
    "s_add_u32 s66, s53, s62\n"
    "s_mov_b32 m0, s66\n"
    "v_add_u32 v11, s62, v3\n"
    "s_add_u32 s46, s46, s47\n"
    "v_mov_b64 v[20:21], -1\n"
    "v_mov_b64 v[22:23], -1\n"
    "s_lshr_b32 s67, s48, 8\n"
    "s_xor_b32 s67, s67, s52\n"
    "s_and_b32 s67, s67, 0x2000\n"
    "s_or_b32 s67, s67, s48\n"
    "buffer_store_dwordx4 v[20:23], v6, s[36:39], s67 offen offset:0 sc1\n"
    "buffer_store_dwordx4 v[20:23], v6, s[36:39], s67 offen offset:256 sc1\n"
    "L_DN_LOOPA%=:\n"
    "s_lshr_b32 s69, s48, 8\n"
    "s_xor_b32 s69, s69, s52\n"
    "s_and_b32 s69, s69, 0x2000\n"
    "s_or_b32 s69, s69, s48\n"
    "s_add_u32 s48, s48, s49\n"
    "v_mov_b32 v12, s61\n"
    "s_cmp_eq_u32 s17, 0\n"
    "s_cbranch_scc1 L_DN_WAok%=\n"
    "v_cmp_gt_i32 vcc, s71, v8\n"
    "s_cbranch_vccz L_DN_WAok%=\n"
    "L_DN_WA%=:\n"
    "buffer_load_dword v8, v4, s[36:39], 0 offen sc1\n"
    "s_waitcnt vmcnt(0)\n"
    "v_cmp_gt_i32 vcc, s71, v8\n"
    "s_cbranch_vccnz L_DN_WAf%=\n"
    "L_DN_WAok%=:\n"
    "s_cmp_lg_u32 s63, 0\n"
    "s_barrier\n"
    "s_cbranch_scc1 L_DN_HAVEA%=\n"
    "buffer_load_dwordx4 v2, s[36:39], s68 offen sc1 lds\n"
    "buffer_load_dwordx4 v2, s[36:39], s68 offen offset:1024 sc1 lds\n"
    "s_add_u32 s68, s68, 0x4000\n"
    "s_add_u32 m0, s66, 8192\n"
    "s_nop 0\n"
    "buffer_load_dwordx4 v2, s[36:39], s68 offen sc1 lds\n"
    "buffer_load_dwordx4 v2, s[36:39], s68 offen offset:1024 sc1 lds\n"
    "s_add_u32 s68, s68, 0x4000\n"
    "s_add_u32 m0, s66, 16384\n"
    "s_nop 0\n"
    "buffer_load_dwordx4 v2, s[36:39], s68 offen sc1 lds\n"
    "buffer_load_dwordx4 v2, s[36:39], s68 offen offset:1024 sc1 lds\n"
    "s_add_u32 s68, s68, 0x4000\n"
    "s_add_u32 m0, s66, 24576\n"
    "s_nop 0\n"
    "buffer_load_dwordx4 v2, s[36:39], s68 offen sc1 lds\n"
    "buffer_load_dwordx4 v2, s[36:39], s68 offen offset:1024 sc1 lds\n"
    "s_add_u32 s68, s68, 0x4000\n"
    "buffer_load_dword v10, v13, s[36:39], 0 offen\n"
    "L_DN_HAVEA%=:\n"
    "s_waitcnt vmcnt(7)\n"
    "s_barrier\n"
    "ds_read_b128 v[128:131], v11 offset:0\n"
    "ds_read_b128 v[132:135], v11 offset:256\n"
    "ds_read_b128 v[136:139], v11 offset:2048\n"
    "ds_read_b128 v[140:143], v11 offset:2304\n"
    "ds_read_b128 v[144:147], v11 offset:4096\n"
    "ds_read_b128 v[148:151], v11 offset:4352\n"
    "s_waitcnt lgkmcnt(5)\n"
    "v_mfma_f32_16x16x32_bf16 v[32:35], a[0:3], v[128:131], 0\n"
    "s_add_u32 m0, s66, 32768\n"
    "s_waitcnt lgkmcnt(4)\n"
    "v_mfma_f32_16x16x32_bf16 v[40:43], a[0:3], v[132:135], 0\n"
    "buffer_load_dwordx4 v2, s[36:39], s68 offen sc1 lds\n"
    "v_mfma_f32_16x16x32_bf16 v[44:47], a[4:7], v[132:135], 0\n"
    "ds_read_b128 v[152:155], v11 offset:6144\n"
    "v_mfma_f32_16x16x32_bf16 v[36:39], a[4:7], v[128:131], 0\n"
    "ds_read_b128 v[156:159], v11 offset:6400\n"
    "s_waitcnt lgkmcnt(5)\n"
    "v_mfma_f32_16x16x32_bf16 v[32:35], a[8:11], v[136:139], v[32:35]\n"
    "s_waitcnt lgkmcnt(4)\n"
    "v_mfma_f32_16x16x32_bf16 v[40:43], a[8:11], v[140:143], v[40:43]\n"
    "buffer_load_dwordx4 v2, s[36:39], s68 offen offset:1024 sc1 lds\n"
    "s_add_u32 s68, s68, 0x4000\n"
    "v_mfma_f32_16x16x32_bf16 v[44:47], a[12:15], v[140:143], v[44:47]\n"
    "s_waitcnt vmcnt(7)\n"
    "s_barrier\n"
    "ds_read_b128 v[160:163], v11 offset:8192\n"
    "v_mfma_f32_16x16x32_bf16 v[36:39], a[12:15], v[136:139], v[36:39]\n"
    "ds_read_b128 v[164:167], v11 offset:8448\n"
    "s_waitcnt lgkmcnt(5)\n"
    "v_mfma_f32_16x16x32_bf16 v[32:35], a[16:19], v[144:147], v[32:35]\n"
    "s_add_u32 m0, s66, 40960\n"
    "s_waitcnt lgkmcnt(4)\n"
    "v_mfma_f32_16x16x32_bf16 v[40:43], a[16:19], v[148:151], v[40:43]\n"
    "buffer_load_dwordx4 v2, s[36:39], s68 offen sc1 lds\n"
    "v_mfma_f32_16x16x32_bf16 v[44:47], a[20:23], v[148:151], v[44:47]\n"
    "ds_read_b128 v[168:171], v11 offset:10240\n"
    "v_mfma_f32_16x16x32_bf16 v[36:39], a[20:23], v[144:147], v[36:39]\n"
    "ds_read_b128 v[172:175], v11 offset:10496\n"
    "s_waitcnt lgkmcnt(5)\n"
    "v_mfma_f32_16x16x32_bf16 v[32:35], a[24:27], v[152:155], v[32:35]\n"
    "s_waitcnt lgkmcnt(4)\n"
    "v_mfma_f32_16x16x32_bf16 v[40:43], a[24:27], v[156:159], v[40:43]\n"
    "buffer_load_dwordx4 v2, s[36:39], s68 offen offset:1024 sc1 lds\n"
    "s_add_u32 s68, s68, 0x4000\n"
    "v_mfma_f32_16x16x32_bf16 v[44:47], a[28:31], v[156:159], v[44:47]\n"
    "ds_read_b128 v[176:179], v11 offset:12288\n"
    "v_mfma_f32_16x16x32_bf16 v[36:39], a[28:31], v[152:155], v[36:39]\n"
    "ds_read_b128 v[180:183], v11 offset:12544\n"
    "s_waitcnt lgkmcnt(5)\n"
    "v_mfma_f32_16x16x32_bf16 v[32:35], a[32:35], v[160:163], v[32:35]\n"
    "s_add_u32 m0, s66, 49152\n"
    "s_waitcnt lgkmcnt(4)\n"
    "v_mfma_f32_16x16x32_bf16 v[40:43], a[32:35], v[164:167], v[40:43]\n"
    "buffer_load_dwordx4 v2, s[36:39], s68 offen sc1 lds\n"
    "v_mfma_f32_16x16x32_bf16 v[44:47], a[36:39], v[164:167], v[44:47]\n"
    "ds_read_b128 v[184:187], v11 offset:14336\n"
    "v_mfma_f32_16x16x32_bf16 v[36:39], a[36:39], v[160:163], v[36:39]\n"
    "ds_read_b128 v[188:191], v11 offset:14592\n"
    "s_waitcnt lgkmcnt(5)\n"
    "v_mfma_f32_16x16x32_bf16 v[32:35], a[40:43], v[168:171], v[32:35]\n"
    "s_waitcnt lgkmcnt(4)\n"
    "v_mfma_f32_16x16x32_bf16 v[40:43], a[40:43], v[172:175], v[40:43]\n"
    "buffer_load_dwordx4 v2, s[36:39], s68 offen offset:1024 sc1 lds\n"
    "s_add_u32 s68, s68, 0x4000\n"
    "v_mfma_f32_16x16x32_bf16 v[44:47], a[44:47], v[172:175], v[44:47]\n"
    "s_waitcnt vmcnt(9)\n"
    "s_barrier\n"
    "ds_read_b128 v[192:195], v11 offset:16384\n"
    "v_mfma_f32_16x16x32_bf16 v[36:39], a[44:47], v[168:171], v[36:39]\n"
    "ds_read_b128 v[196:199], v11 offset:16640\n"
    "s_waitcnt lgkmcnt(5)\n"
    "v_mfma_f32_16x16x32_bf16 v[32:35], a[48:51], v[176:179], v[32:35]\n"
    "s_waitcnt lgkmcnt(4)\n"
    "v_mfma_f32_16x16x32_bf16 v[40:43], a[48:51], v[180:183], v[40:43]\n"
    "v_mfma_f32_16x16x32_bf16 v[44:47], a[52:55], v[180:183], v[44:47]\n"
    "ds_read_b128 v[200:203], v11 offset:18432\n"
    "v_mfma_f32_16x16x32_bf16 v[36:39], a[52:55], v[176:179], v[36:39]\n"
    "ds_read_b128 v[204:207], v11 offset:18688\n"
    "s_waitcnt lgkmcnt(5)\n"
    "v_mfma_f32_16x16x32_bf16 v[32:35], a[56:59], v[184:187], v[32:35]\n"
    "s_waitcnt lgkmcnt(4)\n"
    "v_mfma_f32_16x16x32_bf16 v[40:43], a[56:59], v[188:191], v[40:43]\n"
    "v_mfma_f32_16x16x32_bf16 v[44:47], a[60:63], v[188:191], v[44:47]\n"
    "ds_read_b128 v[208:211], v11 offset:20480\n"
    "v_mfma_f32_16x16x32_bf16 v[36:39], a[60:63], v[184:187], v[36:39]\n"
    "ds_read_b128 v[212:215], v11 offset:20736\n"
    "s_waitcnt lgkmcnt(5)\n"
    "v_mfma_f32_16x16x32_bf16 v[32:35], a[64:67], v[192:195], v[32:35]\n"
    "s_waitcnt lgkmcnt(4)\n"
    "v_mfma_f32_16x16x32_bf16 v[40:43], a[64:67], v[196:199], v[40:43]\n"
    "v_mfma_f32_16x16x32_bf16 v[44:47], a[68:71], v[196:199], v[44:47]\n"
    "ds_read_b128 v[216:219], v11 offset:22528\n"
    "v_mfma_f32_16x16x32_bf16 v[36:39], a[68:71], v[192:195], v[36:39]\n"
    "ds_read_b128 v[220:223], v11 offset:22784\n"
    "s_waitcnt lgkmcnt(5)\n"
    "v_mfma_f32_16x16x32_bf16 v[32:35], a[72:75], v[200:203], v[32:35]\n"
    "s_waitcnt lgkmcnt(4)\n"
    "v_mfma_f32_16x16x32_bf16 v[40:43], a[72:75], v[204:207], v[40:43]\n"
    "v_mfma_f32_16x16x32_bf16 v[44:47], a[76:79], v[204:207], v[44:47]\n"
    "s_waitcnt vmcnt(7)\n"
    "s_barrier\n"
    "ds_read_b128 v[224:227], v11 offset:24576\n"
    "v_mfma_f32_16x16x32_bf16 v[36:39], a[76:79], v[200:203], v[36:39]\n"
    "ds_read_b128 v[228:231], v11 offset:24832\n"
    "s_waitcnt lgkmcnt(5)\n"
    "v_mfma_f32_16x16x32_bf16 v[32:35], a[80:83], v[208:211], v[32:35]\n"
    "s_waitcnt lgkmcnt(4)\n"
    "v_mfma_f32_16x16x32_bf16 v[40:43], a[80:83], v[212:215], v[40:43]\n"
    "v_mfma_f32_16x16x32_bf16 v[44:47], a[84:87], v[212:215], v[44:47]\n"
    "ds_read_b128 v[232:235], v11 offset:26624\n"
    "v_mfma_f32_16x16x32_bf16 v[36:39], a[84:87], v[208:211], v[36:39]\n"
    "ds_read_b128 v[236:239], v11 offset:26880\n"
    "s_waitcnt lgkmcnt(5)\n"
    "v_mfma_f32_16x16x32_bf16 v[32:35], a[88:91], v[216:219], v[32:35]\n"
    "s_waitcnt lgkmcnt(4)\n"
    "v_mfma_f32_16x16x32_bf16 v[40:43], a[88:91], v[220:223], v[40:43]\n"
    "s_lshr_b32 s67, s48, 8\n"
    "s_xor_b32 s67, s67, s52\n"
    "v_mfma_f32_16x16x32_bf16 v[44:47], a[92:95], v[220:223], v[44:47]\n"
    "ds_read_b128 v[240:243], v11 offset:28672\n"
    "v_mfma_f32_16x16x32_bf16 v[36:39], a[92:95], v[216:219], v[36:39]\n"
    "ds_read_b128 v[244:247], v11 offset:28928\n"
    "s_and_b32 s67, s67, 0x2000\n"
    "s_or_b32 s67, s67, s48\n"
    "s_waitcnt lgkmcnt(5)\n"
    "v_mfma_f32_16x16x32_bf16 v[32:35], a[96:99], v[224:227], v[32:35]\n"
    "s_waitcnt lgkmcnt(4)\n"
    "v_mfma_f32_16x16x32_bf16 v[40:43], a[96:99], v[228:231], v[40:43]\n"
    "s_cmp_eq_u32 s60, 63\n"
    "s_cselect_b32 s67, s38, s67\n"
    "v_mfma_f32_16x16x32_bf16 v[44:47], a[100:103], v[228:231], v[44:47]\n"
    "ds_read_b128 v[132:135], v11 offset:30720\n"
    "v_mfma_f32_16x16x32_bf16 v[36:39], a[100:103], v[224:227], v[36:39]\n"
    "ds_read_b128 v[128:131], v11 offset:30976\n"
    "buffer_store_dwordx4 v[20:23], v6, s[36:39], s67 offen offset:0 sc1\n"
    "s_waitcnt lgkmcnt(5)\n"
    "v_mfma_f32_16x16x32_bf16 v[32:35], a[104:107], v[232:235], v[32:35]\n"
    "s_waitcnt lgkmcnt(4)\n"
    "v_mfma_f32_16x16x32_bf16 v[40:43], a[104:107], v[236:239], v[40:43]\n"
    "v_mfma_f32_16x16x32_bf16 v[44:47], a[108:111], v[236:239], v[44:47]\n"
    "s_waitcnt vmcnt(5)\n"
    "s_barrier\n"
    "ds_read_b128 v[140:143], v11 offset:32768\n"
    "v_mfma_f32_16x16x32_bf16 v[36:39], a[108:111], v[232:235], v[36:39]\n"
    "ds_read_b128 v[136:139], v11 offset:33024\n"
    "s_waitcnt lgkmcnt(5)\n"
    "v_mfma_f32_16x16x32_bf16 v[32:35], a[112:115], v[240:243], v[32:35]\n"
    "s_waitcnt lgkmcnt(4)\n"
    "v_mfma_f32_16x16x32_bf16 v[40:43], a[112:115], v[244:247], v[40:43]\n"
    "buffer_store_dwordx4 v[20:23], v6, s[36:39], s67 offen offset:256 sc1\n"
    "v_mfma_f32_16x16x32_bf16 v[44:47], a[116:119], v[244:247], v[44:47]\n"
    "ds_read_b128 v[148:151], v11 offset:34816\n"
    "v_mfma_f32_16x16x32_bf16 v[36:39], a[116:119], v[240:243], v[36:39]\n"
    "ds_read_b128 v[144:147], v11 offset:35072\n"
    "s_waitcnt lgkmcnt(5)\n"
    "v_mfma_f32_16x16x32_bf16 v[32:35], a[120:123], v[132:135], v[32:35]\n"
    "s_waitcnt lgkmcnt(4)\n"
    "v_mfma_f32_16x16x32_bf16 v[40:43], a[120:123], v[128:131], v[40:43]\n"
    "v_mfma_f32_16x16x32_bf16 v[44:47], a[124:127], v[128:131], v[44:47]\n"
    "ds_read_b128 v[156:159], v11 offset:36864\n"
    "v_mfma_f32_16x16x32_bf16 v[36:39], a[124:127], v[132:135], v[36:39]\n"
    "ds_read_b128 v[152:155], v11 offset:37120\n"
    "s_waitcnt lgkmcnt(5)\n"
    "v_mfma_f32_16x16x32_bf16 v[32:35], a[128:131], v[140:143], v[32:35]\n"
    "s_waitcnt lgkmcnt(4)\n"
    "v_mfma_f32_16x16x32_bf16 v[40:43], a[128:131], v[136:139], v[40:43]\n"
    "v_mfma_f32_16x16x32_bf16 v[44:47], a[132:135], v[136:139], v[44:47]\n"
    "ds_read_b128 v[164:167], v11 offset:38912\n"
    "v_mfma_f32_16x16x32_bf16 v[36:39], a[132:135], v[140:143], v[36:39]\n"
    "ds_read_b128 v[160:163], v11 offset:39168\n"
    "s_waitcnt lgkmcnt(5)\n"
    "v_mfma_f32_16x16x32_bf16 v[32:35], a[136:139], v[148:151], v[32:35]\n"
    "s_waitcnt lgkmcnt(4)\n"
    "v_mfma_f32_16x16x32_bf16 v[40:43], a[136:139], v[144:147], v[40:43]\n"
    "v_mfma_f32_16x16x32_bf16 v[44:47], a[140:143], v[144:147], v[44:47]\n"
    "s_waitcnt vmcnt(4)\n"
    "s_barrier\n"
    "ds_read_b128 v[172:175], v11 offset:40960\n"
    "v_mfma_f32_16x16x32_bf16 v[36:39], a[140:143], v[148:151], v[36:39]\n"
    "ds_read_b128 v[168:171], v11 offset:41216\n"
    "s_waitcnt lgkmcnt(5)\n"
    "v_mfma_f32_16x16x32_bf16 v[32:35], a[144:147], v[156:159], v[32:35]\n"
    "s_waitcnt lgkmcnt(4)\n"
    "v_mfma_f32_16x16x32_bf16 v[40:43], a[144:147], v[152:155], v[40:43]\n"
    "v_mfma_f32_16x16x32_bf16 v[44:47], a[148:151], v[152:155], v[44:47]\n"
    "ds_read_b128 v[180:183], v11 offset:43008\n"
    "v_mfma_f32_16x16x32_bf16 v[36:39], a[148:151], v[156:159], v[36:39]\n"
    "ds_read_b128 v[176:179], v11 offset:43264\n"
    "s_waitcnt lgkmcnt(5)\n"
    "v_mfma_f32_16x16x32_bf16 v[32:35], a[152:155], v[164:167], v[32:35]\n"
    "s_waitcnt lgkmcnt(4)\n"
    "v_mfma_f32_16x16x32_bf16 v[40:43], a[152:155], v[160:163], v[40:43]\n"
    "v_mfma_f32_16x16x32_bf16 v[44:47], a[156:159], v[160:163], v[44:47]\n"
    "ds_read_b128 v[188:191], v11 offset:45056\n"
    "v_mfma_f32_16x16x32_bf16 v[36:39], a[156:159], v[164:167], v[36:39]\n"
    "ds_read_b128 v[184:187], v11 offset:45312\n"
    "s_waitcnt lgkmcnt(5)\n"
    "v_mfma_f32_16x16x32_bf16 v[32:35], a[160:163], v[172:175], v[32:35]\n"
    "s_waitcnt lgkmcnt(4)\n"
    "v_mfma_f32_16x16x32_bf16 v[40:43], a[160:163], v[168:171], v[40:43]\n"
    "v_mfma_f32_16x16x32_bf16 v[44:47], a[164:167], v[168:171], v[44:47]\n"
    "ds_read_b128 v[196:199], v11 offset:47104\n"
    "v_mfma_f32_16x16x32_bf16 v[36:39], a[164:167], v[172:175], v[36:39]\n"
    "ds_read_b128 v[192:195], v11 offset:47360\n"
    "s_waitcnt lgkmcnt(5)\n"
    "v_mfma_f32_16x16x32_bf16 v[32:35], a[168:171], v[180:183], v[32:35]\n"
    "s_waitcnt lgkmcnt(4)\n"
    "v_mfma_f32_16x16x32_bf16 v[40:43], a[168:171], v[176:179], v[40:43]\n"
    "v_mfma_f32_16x16x32_bf16 v[44:47], a[172:175], v[176:179], v[44:47]\n"
    "s_waitcnt vmcnt(2)\n"
    "s_barrier\n"
    "ds_read_b128 v[204:207], v11 offset:49152\n"
    "v_mfma_f32_16x16x32_bf16 v[36:39], a[172:175], v[180:183], v[36:39]\n"
    "ds_read_b128 v[200:203], v11 offset:49408\n"
    "s_waitcnt lgkmcnt(5)\n"
    "v_mfma_f32_16x16x32_bf16 v[32:35], a[176:179], v[188:191], v[32:35]\n"
    "s_waitcnt lgkmcnt(4)\n"
    "v_mfma_f32_16x16x32_bf16 v[40:43], a[176:179], v[184:187], v[40:43]\n"
    "v_mfma_f32_16x16x32_bf16 v[44:47], a[180:183], v[184:187], v[44:47]\n"
    "ds_read_b128 v[212:215], v11 offset:51200\n"
    "v_mfma_f32_16x16x32_bf16 v[36:39], a[180:183], v[188:191], v[36:39]\n"
    "s_waitcnt lgkmcnt(4)\n"
    "v_mfma_f32_16x16x32_bf16 v[32:35], a[184:187], v[196:199], v[32:35]\n"
    "ds_read_b128 v[208:211], v11 offset:53248\n"
    "s_waitcnt lgkmcnt(4)\n"
    "v_mfma_f32_16x16x32_bf16 v[40:43], a[184:187], v[192:195], v[40:43]\n"
    "v_mfma_f32_16x16x32_bf16 v[44:47], a[188:191], v[192:195], v[44:47]\n"
    "ds_read_b128 v[220:223], v11 offset:55296\n"
    "v_mfma_f32_16x16x32_bf16 v[36:39], a[188:191], v[196:199], v[36:39]\n"
    "s_waitcnt lgkmcnt(4)\n"
    "v_mfma_f32_16x16x32_bf16 v[32:35], a[192:195], v[204:207], v[32:35]\n"
    "ds_read_b128 v[216:219], v11 offset:51456\n"
    "s_waitcnt lgkmcnt(4)\n"
    "v_mfma_f32_16x16x32_bf16 v[40:43], a[192:195], v[200:203], v[40:43]\n"
    "v_mfma_f32_16x16x32_bf16 v[44:47], a[196:199], v[200:203], v[44:47]\n"
    "ds_read_b128 v[228:231], v11 offset:53504\n"
    "v_mfma_f32_16x16x32_bf16 v[36:39], a[196:199], v[204:207], v[36:39]\n"
    "s_waitcnt lgkmcnt(4)\n"
    "v_mfma_f32_16x16x32_bf16 v[32:35], a[200:203], v[212:215], v[32:35]\n"
    "ds_read_b128 v[224:227], v11 offset:55552\n"
    "v_mfma_f32_16x16x32_bf16 v[36:39], a[204:207], v[212:215], v[36:39]\n"
    "buffer_load_dword v9, v4, s[36:39], 0 offen sc1\n"
    "s_waitcnt lgkmcnt(4)\n"
    "v_mfma_f32_16x16x32_bf16 v[32:35], a[208:211], v[208:211], v[32:35]\n"
    "v_mfma_f32_16x16x32_bf16 v[36:39], a[212:215], v[208:211], v[36:39]\n"
    "s_waitcnt lgkmcnt(3)\n"
    "v_mfma_f32_16x16x32_bf16 v[32:35], a[216:219], v[220:223], v[32:35]\n"
    "v_mfma_f32_16x16x32_bf16 v[36:39], a[220:223], v[220:223], v[36:39]\n"
    "s_waitcnt lgkmcnt(2)\n"
    "v_mfma_f32_16x16x32_bf16 v[40:43], a[200:203], v[216:219], v[40:43]\n"
    "v_mfma_f32_16x16x32_bf16 v[44:47], a[204:207], v[216:219], v[44:47]\n"
    "v_cvt_pk_bf16_f32 v96, v32, v33\n"
    "v_cvt_pk_bf16_f32 v97, v34, v35\n"
    "s_waitcnt lgkmcnt(1)\n"
    "v_mfma_f32_16x16x32_bf16 v[40:43], a[208:211], v[228:231], v[40:43]\n"
    "v_cvt_pk_bf16_f32 v98, v36, v37\n"
    "v_cvt_pk_bf16_f32 v99, v38, v39\n"
    "v_mfma_f32_16x16x32_bf16 v[44:47], a[212:215], v[228:231], v[44:47]\n"
    "buffer_store_dwordx4 v[96:99], v6, s[36:39], s69 offen offset:0 sc1\n"
    "s_waitcnt lgkmcnt(0)\n"
    "v_mfma_f32_16x16x32_bf16 v[40:43], a[216:219], v[224:227], v[40:43]\n"
    "v_mfma_f32_16x16x32_bf16 v[44:47], a[220:223], v[224:227], v[44:47]\n"
    "s_nop 3\n"
    "v_cvt_pk_bf16_f32 v100, v40, v41\n"
    "v_cvt_pk_bf16_f32 v101, v42, v43\n"
    "s_nop 1\n"
    "v_cvt_pk_bf16_f32 v102, v44, v45\n"
    "v_cvt_pk_bf16_f32 v103, v46, v47\n"
    "buffer_store_dwordx4 v[100:103], v6, s[36:39], s69 offen offset:256 sc1\n"
    "s_barrier\n"
    "buffer_store_dword v12, v5, s[36:39], 0 offen sc1\n"
    "s_add_u32 s60, s60, 1\n"
    "s_add_u32 s61, s61, 1\n"
    "s_xor_b32 s62, s62, 0x10000\n"
    "s_mov_b32 s63, 0\n"
    "s_add_u32 s71, s71, 1\n"
    "s_cmp_eq_u32 s60, 64\n"
    "s_cselect_b32 s71, 1000, s71\n"
    "s_lshr_b32 s68, s46, 8\n"
    "s_xor_b32 s68, s68, s51\n"
    "s_and_b32 s68, s68, 0x2000\n"
    "s_or_b32 s68, s68, s46\n"
    "s_add_u32 s66, s53, s62\n"
    "s_mov_b32 m0, s66\n"
    "v_add_u32 v11, s62, v3\n"
    "s_add_u32 s46, s46, s47\n"
    "s_waitcnt vmcnt(3)\n"
    "v_max_u32 v8, v8, v9\n"
    "s_cmp_lt_u32 s60, 64\n"
    "s_cbranch_scc1 L_DN_LOOPA%=\n"
    "s_branch L_DN_END%=\n"
    "L_DN_WAf%=:\n"
    "s_nop 0\n"
    "s_cmp_lg_u32 s65, 0\n"
    "s_cbranch_scc1 L_DN_WAok%=\n"
    "s_add_u32 s64, s64, 1\n"
    "s_and_b32 s70, s64, 255\n"
    "s_cbranch_scc1 L_DN_WA%=\n"
    "buffer_load_dword v10, off, s[36:39], s50 sc1\n"
    "s_waitcnt vmcnt(0)\n"
    "v_readfirstlane_b32 s65, v10\n"
    "s_cmp_lt_u32 s64, 0x40000\n"
    "s_cbranch_scc1 L_DN_WA%=\n"
    "s_mov_b32 s65, 1\n"
    "v_mov_b32 v10, 1\n"
    "buffer_store_dword v10, off, s[36:39], s50 sc1\n"
    "s_branch L_DN_WA%=\n"
    "L_DN_END%=:\n"
    "s_waitcnt vmcnt(0)\n"
    "s_endpgm\n"
      GEMM_OPS : ROLE_CLOB);
  } else {
    const unsigned k = lane & 7, nl = nloc(tok);
    const v8u vb = {k == 2 ? 2u : 1u, (tok >> 3) * 256 + hh * 128 + (tok & 7) * 16, nl * 16 + hh * 8,
                    lb + 65536 + w * 5120 + lane * 16, lb + 65536 + (w ^ 1) * 5120 + lane * 16, tok - 4 * hh, k == 2 ? 1024u : 16384u, k >= 4 ? flag(F_XN + (k & 3), 0) : 0u};
    asm volatile("s_mov_b64 exec, -1\ns_and_b32 s37, s37, 0xffff\ns_mov_b32 s39, 0x20000\ns_nop 4\n"
    "s_mov_b32 s64, 0\n"
    "s_mov_b32 s65, 0\n"
    "s_mov_b32 s70, 0x224000\n"
    "s_load_dwordx2 s[72:73], s[36:37], s70\n"
    "s_mov_b32 s78, 0\n"
    "s_mov_b32 s79, 0x220000\n"
    "L_QA_PRO%=:\n"
    "v_xor_b32 v250, 64, v1\n"
    "s_add_u32 m0, s45, 0\n"
    "s_nop 0\n"
    "global_load_lds_dwordx4 v0, s[40:41] \n"
    "s_add_u32 s40, s40, s44\n"
    "s_addc_u32 s41, s41, 0\n"
    "s_add_u32 m0, s45, 1024\n"
    "s_nop 0\n"
    "global_load_lds_dwordx4 v0, s[40:41] \n"
    "s_add_u32 s40, s40, s44\n"
    "s_addc_u32 s41, s41, 0\n"
    "s_add_u32 m0, s45, 2048\n"
    "s_nop 0\n"
    "global_load_lds_dwordx4 v0, s[40:41] \n"
    "s_add_u32 s40, s40, s44\n"
    "s_addc_u32 s41, s41, 0\n"
    "s_add_u32 m0, s45, 3072\n"
    "s_nop 0\n"
    "global_load_lds_dwordx4 v0, s[40:41] \n"
    "s_add_u32 s40, s40, s44\n"
    "s_addc_u32 s41, s41, 0\n"
    "s_add_u32 m0, s45, 4096\n"
    "s_nop 0\n"
    "global_load_lds_dwordx4 v0, s[40:41] \n"
    "s_add_u32 s40, s40, s44\n"
    "s_addc_u32 s41, s41, 0\n"
    "s_add_u32 m0, s45, 5120\n"
    "s_nop 0\n"
    "global_load_lds_dwordx4 v0, s[40:41] \n"
    "s_add_u32 s40, s40, s44\n"
    "s_addc_u32 s41, s41, 0\n"
    "s_add_u32 m0, s45, 6144\n"
    "s_nop 0\n"
    "global_load_lds_dwordx4 v0, s[40:41] \n"
    "s_add_u32 s40, s40, s44\n"
    "s_addc_u32 s41, s41, 0\n"
    "s_add_u32 m0, s45, 7168\n"
    "s_nop 0\n"
    "global_load_lds_dwordx4 v0, s[40:41] \n"
    "s_add_u32 s40, s40, 0x90000\n"
    "s_addc_u32 s41, s41, 0\n"
    "s_add_u32 m0, s45, 8192\n"
    "s_nop 0\n"
    "global_load_lds_dwordx4 v0, s[40:41] \n"
    "s_add_u32 s40, s40, s44\n"
    "s_addc_u32 s41, s41, 0\n"
    "s_add_u32 m0, s45, 9216\n"
    "s_nop 0\n"
    "global_load_lds_dwordx4 v0, s[40:41] \n"
    "s_add_u32 s40, s40, s44\n"
    "s_addc_u32 s41, s41, 0\n"
    "s_add_u32 m0, s45, 10240\n"
    "s_nop 0\n"
    "global_load_lds_dwordx4 v0, s[40:41] \n"
    "s_add_u32 s40, s40, s44\n"
    "s_addc_u32 s41, s41, 0\n"
    "s_add_u32 m0, s45, 11264\n"
    "s_nop 0\n"
    "global_load_lds_dwordx4 v0, s[40:41] \n"
    "s_add_u32 s40, s40, s44\n"
    "s_addc_u32 s41, s41, 0\n"
    "s_add_u32 m0, s45, 12288\n"
    "s_nop 0\n"
    "global_load_lds_dwordx4 v0, s[40:41] \n"
    "s_add_u32 s40, s40, s44\n"
    "s_addc_u32 s41, s41, 0\n"
    "s_add_u32 m0, s45, 13312\n"
    "s_nop 0\n"
    "global_load_lds_dwordx4 v0, s[40:41] \n"
    "s_add_u32 s40, s40, s44\n"
    "s_addc_u32 s41, s41, 0\n"
    "s_add_u32 m0, s45, 14336\n"
    "s_nop 0\n"
    "global_load_lds_dwordx4 v0, s[40:41] \n"
    "s_add_u32 s40, s40, s44\n"
    "s_addc_u32 s41, s41, 0\n"
    "s_add_u32 m0, s45, 15360\n"
    "s_nop 0\n"
    "global_load_lds_dwordx4 v0, s[40:41] \n"
    "s_add_u32 s40, s40, 0x90000\n"
    "s_addc_u32 s41, s41, 0\n"
    "s_add_u32 m0, s45, 16384\n"
    "s_nop 0\n"
    "global_load_lds_dwordx4 v0, s[40:41] \n"
    "s_add_u32 s40, s40, s44\n"
    "s_addc_u32 s41, s41, 0\n"
    "s_add_u32 m0, s45, 17408\n"
    "s_nop 0\n"
    "global_load_lds_dwordx4 v0, s[40:41] \n"
    "s_add_u32 s40, s40, s44\n"
    "s_addc_u32 s41, s41, 0\n"
    "s_add_u32 m0, s45, 18432\n"
    "s_nop 0\n"
    "global_load_lds_dwordx4 v0, s[40:41] \n"
    "s_add_u32 s40, s40, s44\n"
    "s_addc_u32 s41, s41, 0\n"
    "s_add_u32 m0, s45, 19456\n"
    "s_nop 0\n"
    "global_load_lds_dwordx4 v0, s[40:41] \n"
    "s_add_u32 s40, s40, s44\n"
    "s_addc_u32 s41, s41, 0\n"
    "s_add_u32 m0, s45, 20480\n"
    "s_nop 0\n"
    "global_load_lds_dwordx4 v0, s[40:41] \n"
    "s_add_u32 s40, s40, s44\n"
    "s_addc_u32 s41, s41, 0\n"
    "s_add_u32 m0, s45, 21504\n"
    "s_nop 0\n"
    "global_load_lds_dwordx4 v0, s[40:41] \n"
    "s_add_u32 s40, s40, s44\n"
    "s_addc_u32 s41, s41, 0\n"
    "s_add_u32 m0, s45, 22528\n"
    "s_nop 0\n"
    "global_load_lds_dwordx4 v0, s[40:41] \n"
    "s_add_u32 s40, s40, s44\n"
    "s_addc_u32 s41, s41, 0\n"
    "s_add_u32 m0, s45, 23552\n"
    "s_nop 0\n"
    "global_load_lds_dwordx4 v0, s[40:41] \n"
    "s_add_u32 s40, s40, 0x90000\n"
    "s_addc_u32 s41, s41, 0\n"
    "s_add_u32 m0, s45, 24576\n"
    "s_nop 0\n"
    "global_load_lds_dwordx4 v0, s[40:41] \n"
    "s_add_u32 s40, s40, s44\n"
    "s_addc_u32 s41, s41, 0\n"
    "s_add_u32 m0, s45, 25600\n"
    "s_nop 0\n"
    "global_load_lds_dwordx4 v0, s[40:41] \n"
    "s_add_u32 s40, s40, s44\n"
    "s_addc_u32 s41, s41, 0\n"
    "s_add_u32 m0, s45, 26624\n"
    "s_nop 0\n"
    "global_load_lds_dwordx4 v0, s[40:41] \n"
    "s_add_u32 s40, s40, s44\n"
    "s_addc_u32 s41, s41, 0\n"
    "s_add_u32 m0, s45, 27648\n"
    "s_nop 0\n"
    "global_load_lds_dwordx4 v0, s[40:41] \n"
    "s_add_u32 s40, s40, s44\n"
    "s_addc_u32 s41, s41, 0\n"
    "s_add_u32 m0, s45, 28672\n"
    "s_nop 0\n"
    "global_load_lds_dwordx4 v0, s[40:41] \n"
    "s_add_u32 s40, s40, s44\n"
    "s_addc_u32 s41, s41, 0\n"
    "s_add_u32 m0, s45, 29696\n"
    "s_nop 0\n"
    "global_load_lds_dwordx4 v0, s[40:41] \n"
    "s_add_u32 s40, s40, s44\n"
    "s_addc_u32 s41, s41, 0\n"
    "s_add_u32 m0, s45, 30720\n"
    "s_nop 0\n"
    "global_load_lds_dwordx4 v0, s[40:41] \n"
    "s_add_u32 s40, s40, s44\n"
    "s_addc_u32 s41, s41, 0\n"
    "s_add_u32 m0, s45, 31744\n"
    "s_nop 0\n"
    "global_load_lds_dwordx4 v0, s[40:41] \n"
    "s_sub_u32 s40, s40, 0x2f0000\n"
    "s_subb_u32 s41, s41, 0\n"
    "s_waitcnt vmcnt(28)\n"
    "ds_read_b64_tr_b16 a[0:1], v1 offset:0\n"
    "ds_read_b64_tr_b16 a[2:3], v1 offset:512\n"
    "ds_read_b64_tr_b16 a[128:129], v250 offset:0\n"
    "ds_read_b64_tr_b16 a[130:131], v250 offset:512\n"
    "ds_read_b64_tr_b16 a[4:5], v1 offset:2048\n"
    "ds_read_b64_tr_b16 a[6:7], v1 offset:2560\n"
    "ds_read_b64_tr_b16 a[132:133], v250 offset:2048\n"
    "ds_read_b64_tr_b16 a[134:135], v250 offset:2560\n"
    "s_waitcnt lgkmcnt(0)\n"
    "s_add_u32 m0, s45, 0\n"
    "s_nop 0\n"
    "global_load_lds_dwordx4 v0, s[40:41] \n"
    "s_add_u32 s40, s40, s44\n"
    "s_addc_u32 s41, s41, 0\n"
    "s_add_u32 m0, s45, 1024\n"
    "s_nop 0\n"
    "global_load_lds_dwordx4 v0, s[40:41] \n"
    "s_add_u32 s40, s40, s44\n"
    "s_addc_u32 s41, s41, 0\n"
    "s_add_u32 m0, s45, 2048\n"
    "s_nop 0\n"
    "global_load_lds_dwordx4 v0, s[40:41] \n"
    "s_add_u32 s40, s40, s44\n"
    "s_addc_u32 s41, s41, 0\n"
    "s_add_u32 m0, s45, 3072\n"
    "s_nop 0\n"
    "global_load_lds_dwordx4 v0, s[40:41] \n"
    "s_add_u32 s40, s40, s44\n"
    "s_addc_u32 s41, s41, 0\n"
    "s_waitcnt vmcnt(28)\n"
    "ds_read_b64_tr_b16 a[8:9], v1 offset:4096\n"
    "ds_read_b64_tr_b16 a[10:11], v1 offset:4608\n"
    "ds_read_b64_tr_b16 a[136:137], v250 offset:4096\n"
    "ds_read_b64_tr_b16 a[138:139], v250 offset:4608\n"
    "ds_read_b64_tr_b16 a[12:13], v1 offset:6144\n"
    "ds_read_b64_tr_b16 a[14:15], v1 offset:6656\n"
    "ds_read_b64_tr_b16 a[140:141], v250 offset:6144\n"
    "ds_read_b64_tr_b16 a[142:143], v250 offset:6656\n"
    "s_waitcnt lgkmcnt(0)\n"
    "s_add_u32 m0, s45, 4096\n"
    "s_nop 0\n"
    "global_load_lds_dwordx4 v0, s[40:41] \n"
    "s_add_u32 s40, s40, s44\n"
    "s_addc_u32 s41, s41, 0\n"
    "s_add_u32 m0, s45, 5120\n"
    "s_nop 0\n"
    "global_load_lds_dwordx4 v0, s[40:41] \n"
    "s_add_u32 s40, s40, s44\n"
    "s_addc_u32 s41, s41, 0\n"
    "s_add_u32 m0, s45, 6144\n"
    "s_nop 0\n"
    "global_load_lds_dwordx4 v0, s[40:41] \n"
    "s_add_u32 s40, s40, s44\n"
    "s_addc_u32 s41, s41, 0\n"
    "s_add_u32 m0, s45, 7168\n"
    "s_nop 0\n"
    "global_load_lds_dwordx4 v0, s[40:41] \n"
    "s_add_u32 s40, s40, 0x90000\n"
    "s_addc_u32 s41, s41, 0\n"
    "s_waitcnt vmcnt(28)\n"
    "ds_read_b64_tr_b16 a[32:33], v1 offset:8192\n"
    "ds_read_b64_tr_b16 a[34:35], v1 offset:8704\n"
    "ds_read_b64_tr_b16 a[160:161], v250 offset:8192\n"
    "ds_read_b64_tr_b16 a[162:163], v250 offset:8704\n"
    "ds_read_b64_tr_b16 a[36:37], v1 offset:10240\n"
    "ds_read_b64_tr_b16 a[38:39], v1 offset:10752\n"
    "ds_read_b64_tr_b16 a[164:165], v250 offset:10240\n"
    "ds_read_b64_tr_b16 a[166:167], v250 offset:10752\n"
    "s_waitcnt lgkmcnt(0)\n"
    "s_add_u32 m0, s45, 8192\n"
    "s_nop 0\n"
    "global_load_lds_dwordx4 v0, s[40:41] \n"
    "s_add_u32 s40, s40, s44\n"
    "s_addc_u32 s41, s41, 0\n"
    "s_add_u32 m0, s45, 9216\n"
    "s_nop 0\n"
    "global_load_lds_dwordx4 v0, s[40:41] \n"
    "s_add_u32 s40, s40, s44\n"
    "s_addc_u32 s41, s41, 0\n"
    "s_add_u32 m0, s45, 10240\n"
    "s_nop 0\n"
    "global_load_lds_dwordx4 v0, s[40:41] \n"
    "s_add_u32 s40, s40, s44\n"
    "s_addc_u32 s41, s41, 0\n"
    "s_add_u32 m0, s45, 11264\n"
    "s_nop 0\n"
    "global_load_lds_dwordx4 v0, s[40:41] \n"
    "s_add_u32 s40, s40, s44\n"
    "s_addc_u32 s41, s41, 0\n"
    "s_waitcnt vmcnt(28)\n"
    "ds_read_b64_tr_b16 a[40:41], v1 offset:12288\n"
    "ds_read_b64_tr_b16 a[42:43], v1 offset:12800\n"
    "ds_read_b64_tr_b16 a[168:169], v250 offset:12288\n"
    "ds_read_b64_tr_b16 a[170:171], v250 offset:12800\n"
    "ds_read_b64_tr_b16 a[44:45], v1 offset:14336\n"
    "ds_read_b64_tr_b16 a[46:47], v1 offset:14848\n"
    "ds_read_b64_tr_b16 a[172:173], v250 offset:14336\n"
    "ds_read_b64_tr_b16 a[174:175], v250 offset:14848\n"
    "s_waitcnt lgkmcnt(0)\n"
    "s_add_u32 m0, s45, 12288\n"
    "s_nop 0\n"
    "global_load_lds_dwordx4 v0, s[40:41] \n"
    "s_add_u32 s40, s40, s44\n"
    "s_addc_u32 s41, s41, 0\n"
    "s_add_u32 m0, s45, 13312\n"
    "s_nop 0\n"
    "global_load_lds_dwordx4 v0, s[40:41] \n"
    "s_add_u32 s40, s40, s44\n"
    "s_addc_u32 s41, s41, 0\n"
    "s_add_u32 m0, s45, 14336\n"
    "s_nop 0\n"
    "global_load_lds_dwordx4 v0, s[40:41] \n"
    "s_add_u32 s40, s40, s44\n"
    "s_addc_u32 s41, s41, 0\n"
    "s_add_u32 m0, s45, 15360\n"
    "s_nop 0\n"
    "global_load_lds_dwordx4 v0, s[40:41] \n"
    "s_add_u32 s40, s40, 0x90000\n"
    "s_addc_u32 s41, s41, 0\n"
    "s_waitcnt vmcnt(28)\n"
    "ds_read_b64_tr_b16 a[64:65], v1 offset:16384\n"
    "ds_read_b64_tr_b16 a[66:67], v1 offset:16896\n"
    "ds_read_b64_tr_b16 a[192:193], v250 offset:16384\n"
    "ds_read_b64_tr_b16 a[194:195], v250 offset:16896\n"
    "ds_read_b64_tr_b16 a[68:69], v1 offset:18432\n"
    "ds_read_b64_tr_b16 a[70:71], v1 offset:18944\n"
    "ds_read_b64_tr_b16 a[196:197], v250 offset:18432\n"
    "ds_read_b64_tr_b16 a[198:199], v250 offset:18944\n"
    "s_waitcnt lgkmcnt(0)\n"
    "s_add_u32 m0, s45, 16384\n"
    "s_nop 0\n"
    "global_load_lds_dwordx4 v0, s[40:41] \n"
    "s_add_u32 s40, s40, s44\n"
    "s_addc_u32 s41, s41, 0\n"
    "s_add_u32 m0, s45, 17408\n"
    "s_nop 0\n"
    "global_load_lds_dwordx4 v0, s[40:41] \n"
    "s_add_u32 s40, s40, s44\n"
    "s_addc_u32 s41, s41, 0\n"
    "s_add_u32 m0, s45, 18432\n"
    "s_nop 0\n"
    "global_load_lds_dwordx4 v0, s[40:41] \n"
    "s_add_u32 s40, s40, s44\n"
    "s_addc_u32 s41, s41, 0\n"
    "s_add_u32 m0, s45, 19456\n"
    "s_nop 0\n"
    "global_load_lds_dwordx4 v0, s[40:41] \n"
    "s_add_u32 s40, s40, s44\n"
    "s_addc_u32 s41, s41, 0\n"
    "s_waitcnt vmcnt(28)\n"
    "ds_read_b64_tr_b16 a[72:73], v1 offset:20480\n"
    "ds_read_b64_tr_b16 a[74:75], v1 offset:20992\n"
    "ds_read_b64_tr_b16 a[200:201], v250 offset:20480\n"
    "ds_read_b64_tr_b16 a[202:203], v250 offset:20992\n"
    "ds_read_b64_tr_b16 a[76:77], v1 offset:22528\n"
    "ds_read_b64_tr_b16 a[78:79], v1 offset:23040\n"
    "ds_read_b64_tr_b16 a[204:205], v250 offset:22528\n"
    "ds_read_b64_tr_b16 a[206:207], v250 offset:23040\n"
    "s_waitcnt lgkmcnt(0)\n"
    "s_add_u32 m0, s45, 20480\n"
    "s_nop 0\n"
    "global_load_lds_dwordx4 v0, s[40:41] \n"
    "s_add_u32 s40, s40, s44\n"
    "s_addc_u32 s41, s41, 0\n"
    "s_add_u32 m0, s45, 21504\n"
    "s_nop 0\n"
    "global_load_lds_dwordx4 v0, s[40:41] \n"
    "s_add_u32 s40, s40, s44\n"
    "s_addc_u32 s41, s41, 0\n"
    "s_add_u32 m0, s45, 22528\n"
    "s_nop 0\n"
    "global_load_lds_dwordx4 v0, s[40:41] \n"
    "s_add_u32 s40, s40, s44\n"
    "s_addc_u32 s41, s41, 0\n"
    "s_add_u32 m0, s45, 23552\n"
    "s_nop 0\n"
    "global_load_lds_dwordx4 v0, s[40:41] \n"
    "s_add_u32 s40, s40, 0x90000\n"
    "s_addc_u32 s41, s41, 0\n"
    "s_waitcnt vmcnt(28)\n"
    "ds_read_b64_tr_b16 a[96:97], v1 offset:24576\n"
    "ds_read_b64_tr_b16 a[98:99], v1 offset:25088\n"
    "ds_read_b64_tr_b16 a[224:225], v250 offset:24576\n"
    "ds_read_b64_tr_b16 a[226:227], v250 offset:25088\n"
    "ds_read_b64_tr_b16 a[100:101], v1 offset:26624\n"
    "ds_read_b64_tr_b16 a[102:103], v1 offset:27136\n"
    "ds_read_b64_tr_b16 a[228:229], v250 offset:26624\n"
    "ds_read_b64_tr_b16 a[230:231], v250 offset:27136\n"
    "s_waitcnt lgkmcnt(0)\n"
    "s_add_u32 m0, s45, 24576\n"
    "s_nop 0\n"
    "global_load_lds_dwordx4 v0, s[40:41] \n"
    "s_add_u32 s40, s40, s44\n"
    "s_addc_u32 s41, s41, 0\n"
    "s_add_u32 m0, s45, 25600\n"
    "s_nop 0\n"
    "global_load_lds_dwordx4 v0, s[40:41] \n"
    "s_add_u32 s40, s40, s44\n"
    "s_addc_u32 s41, s41, 0\n"
    "s_add_u32 m0, s45, 26624\n"
    "s_nop 0\n"
    "global_load_lds_dwordx4 v0, s[40:41] \n"
    "s_add_u32 s40, s40, s44\n"
    "s_addc_u32 s41, s41, 0\n"
    "s_add_u32 m0, s45, 27648\n"
    "s_nop 0\n"
    "global_load_lds_dwordx4 v0, s[40:41] \n"
    "s_add_u32 s40, s40, s44\n"
    "s_addc_u32 s41, s41, 0\n"
    "s_waitcnt vmcnt(28)\n"
    "ds_read_b64_tr_b16 a[104:105], v1 offset:28672\n"
    "ds_read_b64_tr_b16 a[106:107], v1 offset:29184\n"
    "ds_read_b64_tr_b16 a[232:233], v250 offset:28672\n"
    "ds_read_b64_tr_b16 a[234:235], v250 offset:29184\n"
    "ds_read_b64_tr_b16 a[108:109], v1 offset:30720\n"
    "ds_read_b64_tr_b16 a[110:111], v1 offset:31232\n"
    "ds_read_b64_tr_b16 a[236:237], v250 offset:30720\n"
    "ds_read_b64_tr_b16 a[238:239], v250 offset:31232\n"
    "s_waitcnt lgkmcnt(0)\n"
    "s_add_u32 m0, s45, 28672\n"
    "s_nop 0\n"
    "global_load_lds_dwordx4 v0, s[40:41] \n"
    "s_add_u32 s40, s40, s44\n"
    "s_addc_u32 s41, s41, 0\n"
    "s_add_u32 m0, s45, 29696\n"
    "s_nop 0\n"
    "global_load_lds_dwordx4 v0, s[40:41] \n"
    "s_add_u32 s40, s40, s44\n"
    "s_addc_u32 s41, s41, 0\n"
    "s_add_u32 m0, s45, 30720\n"
    "s_nop 0\n"
    "global_load_lds_dwordx4 v0, s[40:41] \n"
    "s_add_u32 s40, s40, s44\n"
    "s_addc_u32 s41, s41, 0\n"
    "s_add_u32 m0, s45, 31744\n"
    "s_nop 0\n"
    "global_load_lds_dwordx4 v0, s[40:41] \n"
    "s_add_u32 s40, s40, s44\n"
    "s_addc_u32 s41, s41, 0\n"
    "s_waitcnt vmcnt(28)\n"
    "ds_read_b64_tr_b16 a[16:17], v1 offset:0\n"
    "ds_read_b64_tr_b16 a[18:19], v1 offset:512\n"
    "ds_read_b64_tr_b16 a[144:145], v250 offset:0\n"
    "ds_read_b64_tr_b16 a[146:147], v250 offset:512\n"
    "ds_read_b64_tr_b16 a[20:21], v1 offset:2048\n"
    "ds_read_b64_tr_b16 a[22:23], v1 offset:2560\n"
    "ds_read_b64_tr_b16 a[148:149], v250 offset:2048\n"
    "ds_read_b64_tr_b16 a[150:151], v250 offset:2560\n"
    "s_waitcnt vmcnt(24)\n"
    "ds_read_b64_tr_b16 a[24:25], v1 offset:4096\n"
    "ds_read_b64_tr_b16 a[26:27], v1 offset:4608\n"
    "ds_read_b64_tr_b16 a[152:153], v250 offset:4096\n"
    "ds_read_b64_tr_b16 a[154:155], v250 offset:4608\n"
    "ds_read_b64_tr_b16 a[28:29], v1 offset:6144\n"
    "ds_read_b64_tr_b16 a[30:31], v1 offset:6656\n"
    "ds_read_b64_tr_b16 a[156:157], v250 offset:6144\n"
    "ds_read_b64_tr_b16 a[158:159], v250 offset:6656\n"
    "s_waitcnt vmcnt(20)\n"
    "ds_read_b64_tr_b16 a[48:49], v1 offset:8192\n"
    "ds_read_b64_tr_b16 a[50:51], v1 offset:8704\n"
    "ds_read_b64_tr_b16 a[176:177], v250 offset:8192\n"
    "ds_read_b64_tr_b16 a[178:179], v250 offset:8704\n"
    "ds_read_b64_tr_b16 a[52:53], v1 offset:10240\n"
    "ds_read_b64_tr_b16 a[54:55], v1 offset:10752\n"
    "ds_read_b64_tr_b16 a[180:181], v250 offset:10240\n"
    "ds_read_b64_tr_b16 a[182:183], v250 offset:10752\n"
    "s_waitcnt vmcnt(16)\n"
    "ds_read_b64_tr_b16 a[56:57], v1 offset:12288\n"
    "ds_read_b64_tr_b16 a[58:59], v1 offset:12800\n"
    "ds_read_b64_tr_b16 a[184:185], v250 offset:12288\n"
    "ds_read_b64_tr_b16 a[186:187], v250 offset:12800\n"
    "ds_read_b64_tr_b16 a[60:61], v1 offset:14336\n"
    "ds_read_b64_tr_b16 a[62:63], v1 offset:14848\n"
    "ds_read_b64_tr_b16 a[188:189], v250 offset:14336\n"
    "ds_read_b64_tr_b16 a[190:191], v250 offset:14848\n"
    "s_waitcnt vmcnt(12)\n"
    "ds_read_b64_tr_b16 a[80:81], v1 offset:16384\n"
    "ds_read_b64_tr_b16 a[82:83], v1 offset:16896\n"
    "ds_read_b64_tr_b16 a[208:209], v250 offset:16384\n"
    "ds_read_b64_tr_b16 a[210:211], v250 offset:16896\n"
    "ds_read_b64_tr_b16 a[84:85], v1 offset:18432\n"
    "ds_read_b64_tr_b16 a[86:87], v1 offset:18944\n"
    "ds_read_b64_tr_b16 a[212:213], v250 offset:18432\n"
    "ds_read_b64_tr_b16 a[214:215], v250 offset:18944\n"
    "s_waitcnt vmcnt(8)\n"
    "ds_read_b64_tr_b16 a[88:89], v1 offset:20480\n"
    "ds_read_b64_tr_b16 a[90:91], v1 offset:20992\n"
    "ds_read_b64_tr_b16 a[216:217], v250 offset:20480\n"
    "ds_read_b64_tr_b16 a[218:219], v250 offset:20992\n"
    "ds_read_b64_tr_b16 a[92:93], v1 offset:22528\n"
    "ds_read_b64_tr_b16 a[94:95], v1 offset:23040\n"
    "ds_read_b64_tr_b16 a[220:221], v250 offset:22528\n"
    "ds_read_b64_tr_b16 a[222:223], v250 offset:23040\n"
    "s_waitcnt vmcnt(4)\n"
    "ds_read_b64_tr_b16 a[112:113], v1 offset:24576\n"
    "ds_read_b64_tr_b16 a[114:115], v1 offset:25088\n"
    "ds_read_b64_tr_b16 a[240:241], v250 offset:24576\n"
    "ds_read_b64_tr_b16 a[242:243], v250 offset:25088\n"
    "ds_read_b64_tr_b16 a[116:117], v1 offset:26624\n"
    "ds_read_b64_tr_b16 a[118:119], v1 offset:27136\n"
    "ds_read_b64_tr_b16 a[244:245], v250 offset:26624\n"
    "ds_read_b64_tr_b16 a[246:247], v250 offset:27136\n"
    "s_waitcnt vmcnt(0)\n"
    "ds_read_b64_tr_b16 a[120:121], v1 offset:28672\n"
    "ds_read_b64_tr_b16 a[122:123], v1 offset:29184\n"
    "ds_read_b64_tr_b16 a[248:249], v250 offset:28672\n"
    "ds_read_b64_tr_b16 a[250:251], v250 offset:29184\n"
    "ds_read_b64_tr_b16 a[124:125], v1 offset:30720\n"
    "ds_read_b64_tr_b16 a[126:127], v1 offset:31232\n"
    "ds_read_b64_tr_b16 a[252:253], v250 offset:30720\n"
    "ds_read_b64_tr_b16 a[254:255], v250 offset:31232\n"
    "s_waitcnt lgkmcnt(0)\n"
    "s_barrier\n"
    "s_branch L_QA_GLD%=\n"
    "L_QA_GATE%=:\n"
    "s_sleep 10\n"
    "s_add_u32 s78, s78, 1\n"
    "s_cmp_lt_u32 s78, 0x10000\n"
    "s_cbranch_scc1 L_QA_GLD%=\n"
    "s_endpgm\n"
    "L_QA_GLD%=:\n"
    "v_lshlrev_b32 v16, 3, v7\n"
    "v_add_u32 v18, 0x2000, v7\n"
    "buffer_load_dwordx2 v[16:17], v16, s[36:39], s79 offen sc1\n"
    "buffer_load_dwordx2 v[18:19], v18, s[36:39], s79 offen sc1\n"
    "s_waitcnt vmcnt(0)\n"
    "L_QA_GCHK%=:\n"
    "s_waitcnt lgkmcnt(0)\n"
    "s_add_u32 s76, s72, 0x7F4A7C15\n"
    "s_addc_u32 s77, s73, 0x9E3779B9\n"
    "s_andn2_b32 s77, s77, 255\n"
    "v_xor_b32 v16, s76, v16\n"
    "v_xor_b32 v18, s76, v18\n"
    "v_xor_b32 v17, s77, v17\n"
    "v_xor_b32 v19, s77, v19\n"
    "v_or_b32 v16, v16, v18\n"
    "v_or_b32 v18, v17, v19\n"
    "v_lshrrev_b32 v18, 8, v18\n"
    "v_or_b32 v16, v16, v18\n"
    "v_cmp_ne_u32 vcc, 0, v16\n"
    "s_cbranch_vccnz L_QA_GATE%=\n"
    "v_readfirstlane_b32 s70, v17\n"
    "v_and_b32 v16, 8, v17\n"
    "v_cmp_ne_u32 vcc, 0, v16\n"
    "s_bcnt1_i32_b64 s76, vcc\n"
    "s_cmp_gt_u32 s76, 4\n"
    "s_cselect_b32 s77, 0, 4\n"
    "s_add_u32 s71, s70, s74\n"
    "s_xor_b32 s77, s77, s71\n"
    "s_and_b32 s77, s77, 4\n"
    "s_lshl_b32 s77, s77, 11\n"
    "s_and_b32 s76, s71, 3\n"
    "s_and_b32 s70, s75, 3\n"
    "s_and_b32 s75, s71, 7\n"
    "s_sub_i32 s70, s76, s70\n"
    "s_cmp_lg_u32 s70, 0\n"
    "s_cbranch_scc1 L_QA_REDO%=\n"
    "s_lshl_b32 s70, s76, 17\n"
    "s_add_u32 s48, s48, s70\n"
    "s_lshl_b32 s70, s76, 17\n"
    "s_add_u32 s57, s57, s70\n"
    "s_lshl_b32 s70, s76, 15\n"
    "s_add_u32 s58, s58, s70\n"
    "s_lshl_b32 s70, s76, 9\n"
    "v_add_u32 v5, s70, v5\n"
    "v_mad_u32_u24 v4, v14, s76, v4\n"
    "s_mov_b32 s52, s77\n"
    "v_cmp_ne_u32 vcc, 0, v15\n"
    "s_cmp_gt_u32 s75, 3\n"
    "s_cselect_b32 s51, s77, s51\n"
    "s_mov_b32 s46, 0x400000\n"
    "s_cselect_b32 s46, 0x800000, s46\n"
    "s_cselect_b64 vcc, vcc, 0\n"
    "v_cndmask_b32 v4, v4, v15, vcc\n"
    "v_cndmask_b32_e64 v8, v8, 3, vcc\n"
    "v_xor_b32 v4, s77, v4\n"
    "v_xor_b32 v5, s77, v5\n"
    "v_cmp_le_i32 vcc, 0, v13\n"
    "v_mov_b32 v16, 0xff800000\n"
    "s_nop 0\n"
    "v_cndmask_b32_e64 v16, v16, 0, vcc\n"
    "v_cmp_le_i32 vcc, 1, v13\n"
    "v_mov_b32 v17, 0xff800000\n"
    "s_nop 0\n"
    "v_cndmask_b32_e64 v17, v17, 0, vcc\n"
    "v_cmp_le_i32 vcc, 2, v13\n"
    "v_mov_b32 v18, 0xff800000\n"
    "s_nop 0\n"
    "v_cndmask_b32_e64 v18, v18, 0, vcc\n"
    "v_cmp_le_i32 vcc, 3, v13\n"
    "v_mov_b32 v19, 0xff800000\n"
    "s_nop 0\n"
    "v_cndmask_b32_e64 v19, v19, 0, vcc\n"
    "v_cmp_le_i32 vcc, 8, v13\n"
    "v_mov_b32 v20, 0xff800000\n"
    "s_nop 0\n"
    "v_cndmask_b32_e64 v20, v20, 0, vcc\n"
    "v_cmp_le_i32 vcc, 9, v13\n"
    "v_mov_b32 v21, 0xff800000\n"
    "s_nop 0\n"
    "v_cndmask_b32_e64 v21, v21, 0, vcc\n"
    "v_cmp_le_i32 vcc, 10, v13\n"
    "v_mov_b32 v22, 0xff800000\n"
    "s_nop 0\n"
    "v_cndmask_b32_e64 v22, v22, 0, vcc\n"
    "v_cmp_le_i32 vcc, 11, v13\n"
    "v_mov_b32 v23, 0xff800000\n"
    "s_nop 0\n"
    "v_cndmask_b32_e64 v23, v23, 0, vcc\n"
    "v_cmp_le_i32 vcc, 16, v13\n"
    "v_mov_b32 v24, 0xff800000\n"
    "s_nop 0\n"
    "v_cndmask_b32_e64 v24, v24, 0, vcc\n"
    "v_cmp_le_i32 vcc, 17, v13\n"
    "v_mov_b32 v25, 0xff800000\n"
    "s_nop 0\n"
    "v_cndmask_b32_e64 v25, v25, 0, vcc\n"
    "v_cmp_le_i32 vcc, 18, v13\n"
    "v_mov_b32 v26, 0xff800000\n"
    "s_nop 0\n"
    "v_cndmask_b32_e64 v26, v26, 0, vcc\n"
    "v_cmp_le_i32 vcc, 19, v13\n"
    "v_mov_b32 v27, 0xff800000\n"
    "s_nop 0\n"
    "v_cndmask_b32_e64 v27, v27, 0, vcc\n"
    "v_cmp_le_i32 vcc, 24, v13\n"
    "v_mov_b32 v28, 0xff800000\n"
    "s_nop 0\n"
    "v_cndmask_b32_e64 v28, v28, 0, vcc\n"
    "v_cmp_le_i32 vcc, 25, v13\n"
    "v_mov_b32 v29, 0xff800000\n"
    "s_nop 0\n"
    "v_cndmask_b32_e64 v29, v29, 0, vcc\n"
    "v_cmp_le_i32 vcc, 26, v13\n"
    "v_mov_b32 v30, 0xff800000\n"
    "s_nop 0\n"
    "v_cndmask_b32_e64 v30, v30, 0, vcc\n"
    "v_cmp_le_i32 vcc, 27, v13\n"
    "v_mov_b32 v31, 0xff800000\n"
    "s_nop 0\n"
    "v_cndmask_b32_e64 v31, v31, 0, vcc\n"
    "s_mov_b32 s18, 0x3e0293ee\n"
    "s_mov_b32 s19, 0x3e0293ee\n"
    "v_mov_b32 v236, 0\n"
    "s_mov_b32 s20, 0xf7f7f7f7\n"
    "s_mov_b32 s21, 0xf7f7f7f7\n"
    "v_subrev_u32 v237, 2, v8\n"
    "v_max_i32 v237, 0, v237\n"
    "L_QA_W0%=:\n"
    "buffer_load_dword v236, v4, s[36:39], 0 offen sc1\n"
    "s_waitcnt vmcnt(0)\n"
    "v_cmp_gt_u32 vcc, v237, v236\n"
    "s_cbranch_vccnz L_QA_W0f%=\n"
    "L_QA_W0ok%=:\n"
    "s_mov_b32 s63, 0\n"
    "s_lshr_b32 s67, s46, 8\n"
    "s_xor_b32 s67, s67, s51\n"
    "s_and_b32 s67, s67, 0x2000\n"
    "s_or_b32 s67, s67, s46\n"
    "s_add_u32 s66, s53, s63\n"
    "s_add_u32 s46, s46, s47\n"
    "s_add_u32 m0, s66, 0\n"
    "s_nop 0\n"
    "buffer_load_dwordx4 v2, s[36:39], s67 offen sc1 lds\n"
    "buffer_load_dwordx4 v2, s[36:39], s67 offen offset:1024 sc1 lds\n"
    "s_add_u32 s67, s67, 0x4000\n"
    "s_add_u32 m0, s66, 8192\n"
    "s_nop 0\n"
    "buffer_load_dwordx4 v2, s[36:39], s67 offen sc1 lds\n"
    "buffer_load_dwordx4 v2, s[36:39], s67 offen offset:1024 sc1 lds\n"
    "s_add_u32 s67, s67, 0x4000\n"
    "s_add_u32 m0, s66, 16384\n"
    "s_nop 0\n"
    "buffer_load_dwordx4 v2, s[36:39], s67 offen sc1 lds\n"
    "buffer_load_dwordx4 v2, s[36:39], s67 offen offset:1024 sc1 lds\n"
    "s_add_u32 s67, s67, 0x4000\n"
    "s_add_u32 m0, s66, 24576\n"
    "s_nop 0\n"
    "buffer_load_dwordx4 v2, s[36:39], s67 offen sc1 lds\n"
    "buffer_load_dwordx4 v2, s[36:39], s67 offen offset:1024 sc1 lds\n"
    "s_waitcnt vmcnt(0)\n"
    "s_barrier\n"
    "s_mov_b32 s60, -1\n"
    "s_mov_b32 s62, 0\n"
    "s_mov_b32 s63, 0x8000\n"
    "s_sub_u32 s58, s58, 0x20000\n"
    "v_mov_b32 v243, v6\n"
    "v_mov_b32 v6, 0x80000000\n"
    "s_sub_u32 s48, s48, s49\n"
    "v_mov_b32 v239, v3\n"
    "ds_read_b128 v[64:67], v239 offset:0\n"
    "ds_read_b128 v[68:71], v239 offset:1024\n"
    "ds_read_b128 v[72:75], v239 offset:2048\n"
    "s_waitcnt lgkmcnt(2)\n"
    "v_mfma_f32_32x32x16_bf16 v[32:47], a[0:3], v[64:67], 0\n"
    "ds_read_b128 v[76:79], v239 offset:3072\n"
    "v_mfma_f32_32x32x16_bf16 v[48:63], a[128:131], v[64:67], 0\n"
    "s_waitcnt lgkmcnt(2)\n"
    "v_mfma_f32_32x32x16_bf16 v[32:47], a[4:7], v[68:71], v[32:47]\n"
    "ds_read_b128 v[64:67], v239 offset:4096\n"
    "v_mfma_f32_32x32x16_bf16 v[48:63], a[132:135], v[68:71], v[48:63]\n"
    "s_waitcnt lgkmcnt(2)\n"
    "v_mfma_f32_32x32x16_bf16 v[32:47], a[8:11], v[72:75], v[32:47]\n"
    "ds_read_b128 v[68:71], v239 offset:5120\n"
    "v_mfma_f32_32x32x16_bf16 v[48:63], a[136:139], v[72:75], v[48:63]\n"
    "s_waitcnt lgkmcnt(2)\n"
    "v_mfma_f32_32x32x16_bf16 v[32:47], a[12:15], v[76:79], v[32:47]\n"
    "ds_read_b128 v[72:75], v239 offset:6144\n"
    "v_mfma_f32_32x32x16_bf16 v[48:63], a[140:143], v[76:79], v[48:63]\n"
    "s_waitcnt lgkmcnt(2)\n"
    "v_mfma_f32_32x32x16_bf16 v[32:47], a[16:19], v[64:67], v[32:47]\n"
    "ds_read_b128 v[76:79], v239 offset:7168\n"
    "v_mfma_f32_32x32x16_bf16 v[48:63], a[144:147], v[64:67], v[48:63]\n"
    "s_waitcnt lgkmcnt(2)\n"
    "v_mfma_f32_32x32x16_bf16 v[32:47], a[20:23], v[68:71], v[32:47]\n"
    "ds_read_b128 v[64:67], v239 offset:8192\n"
    "v_mfma_f32_32x32x16_bf16 v[48:63], a[148:151], v[68:71], v[48:63]\n"
    "s_waitcnt lgkmcnt(2)\n"
    "v_mfma_f32_32x32x16_bf16 v[32:47], a[24:27], v[72:75], v[32:47]\n"
    "ds_read_b128 v[68:71], v239 offset:9216\n"
    "v_mfma_f32_32x32x16_bf16 v[48:63], a[152:155], v[72:75], v[48:63]\n"
    "s_waitcnt lgkmcnt(2)\n"
    "v_mfma_f32_32x32x16_bf16 v[32:47], a[28:31], v[76:79], v[32:47]\n"
    "ds_read_b128 v[72:75], v239 offset:10240\n"
    "v_mfma_f32_32x32x16_bf16 v[48:63], a[156:159], v[76:79], v[48:63]\n"
    "s_waitcnt lgkmcnt(2)\n"
    "v_mfma_f32_32x32x16_bf16 v[32:47], a[32:35], v[64:67], v[32:47]\n"
    "ds_read_b128 v[76:79], v239 offset:11264\n"
    "v_mfma_f32_32x32x16_bf16 v[48:63], a[160:163], v[64:67], v[48:63]\n"
    "s_waitcnt lgkmcnt(2)\n"
    "v_mfma_f32_32x32x16_bf16 v[32:47], a[36:39], v[68:71], v[32:47]\n"
    "ds_read_b128 v[64:67], v239 offset:12288\n"
    "v_mfma_f32_32x32x16_bf16 v[48:63], a[164:167], v[68:71], v[48:63]\n"
    "s_waitcnt lgkmcnt(2)\n"
    "v_mfma_f32_32x32x16_bf16 v[32:47], a[40:43], v[72:75], v[32:47]\n"
    "ds_read_b128 v[68:71], v239 offset:13312\n"
    "v_mfma_f32_32x32x16_bf16 v[48:63], a[168:171], v[72:75], v[48:63]\n"
    "s_waitcnt lgkmcnt(2)\n"
    "v_mfma_f32_32x32x16_bf16 v[32:47], a[44:47], v[76:79], v[32:47]\n"
    "v_mfma_f32_32x32x16_bf16 v[48:63], a[172:175], v[76:79], v[48:63]\n"
    "s_waitcnt lgkmcnt(1)\n"
    "v_mfma_f32_32x32x16_bf16 v[32:47], a[48:51], v[64:67], v[32:47]\n"
    "v_mfma_f32_32x32x16_bf16 v[48:63], a[176:179], v[64:67], v[48:63]\n"
    "s_waitcnt lgkmcnt(0)\n"
    "v_mfma_f32_32x32x16_bf16 v[32:47], a[52:55], v[68:71], v[32:47]\n"
    "v_mfma_f32_32x32x16_bf16 v[48:63], a[180:183], v[68:71], v[48:63]\n"
    "v_add_u32 v237, s60, v8\n"
    "v_min_u32 v237, 64, v237\n"
    "s_lshr_b32 s71, s58, 8\n"
    "s_xor_b32 s71, s71, s52\n"
    "s_and_b32 s71, s71, 0x2000\n"
    "s_or_b32 s71, s71, s58\n"
    "s_add_u32 s58, s58, 0x20000\n"
    "s_add_u32 s59, s71, 0x800000\n"
    "s_lshr_b32 s69, s48, 8\n"
    "s_xor_b32 s69, s69, s52\n"
    "s_and_b32 s69, s69, 0x2000\n"
    "s_or_b32 s69, s69, s48\n"
    "s_add_u32 s48, s48, s49\n"
    "s_lshr_b32 s68, s57, 8\n"
    "s_xor_b32 s68, s68, s52\n"
    "s_and_b32 s68, s68, 0x2000\n"
    "s_or_b32 s68, s68, s57\n"
    "s_add_u32 s57, s57, 0x80000\n"
    "s_add_u32 s99, s68, 0x2000000\n"
    "s_add_u32 s60, s60, 1\n"
    "v_mov_b32 v240, s60\n"
    "s_lshr_b32 s67, s46, 8\n"
    "s_xor_b32 s67, s67, s51\n"
    "s_and_b32 s67, s67, 0x2000\n"
    "s_or_b32 s67, s67, s46\n"
    "s_add_u32 s66, s53, s63\n"
    "s_add_u32 s46, s46, s47\n"
    "ds_read_b128 v[64:67], v239 offset:14336\n"
    "ds_read_b128 v[68:71], v239 offset:15360\n"
    "ds_read_b128 v[72:75], v239 offset:16384\n"
    "L_QA_LOOP%=:\n"
    "v_cmp_gt_u32 vcc, v237, v236\n"
    "s_and_b64 vcc, vcc, s[20:21]\n"
    "s_cbranch_scc0 L_QA_Vok%=\n"
    "L_QA_V%=:\n"
    "buffer_load_dword v236, v4, s[36:39], 0 offen sc1\n"
    "s_waitcnt vmcnt(0)\n"
    "v_cmp_gt_u32 vcc, v237, v236\n"
    "s_and_b64 vcc, vcc, s[20:21]\n"
    "s_cbranch_scc1 L_QA_Vf%=\n"
    "L_QA_Vok%=:\n"
    "buffer_load_dwordx4 v[244:247], v9, s[36:39], s71 offen sc1\n"
    "buffer_load_dwordx2 v[208:209], v10, s[36:39], s59 offen offset:0 sc1\n"
    "buffer_load_dwordx2 v[210:211], v10, s[36:39], s59 offen offset:512 sc1\n"
    "v_cmp_gt_u32 vcc, v237, v236\n"
    "s_cbranch_vccz L_QA_Wok%=\n"
    "L_QA_W%=:\n"
    "buffer_load_dword v236, v4, s[36:39], 0 offen sc1\n"
    "s_waitcnt vmcnt(0)\n"
    "v_cmp_gt_u32 vcc, v237, v236\n"
    "s_cbranch_vccnz L_QA_Wf%=\n"
    "L_QA_Wok%=:\n"
    "s_cmp_eq_u32 s60, 64\n"
    "s_cbranch_scc1 L_QA_TAIL%=\n"
    "s_waitcnt lgkmcnt(2)\n"
    "v_mfma_f32_32x32x16_bf16 v[32:47], a[56:59], v[64:67], v[32:47]\n"
    "ds_read_b128 v[76:79], v239 offset:17408\n"
    "v_mfma_f32_32x32x16_bf16 v[48:63], a[184:187], v[64:67], v[48:63]\n"
    "s_add_u32 m0, s66, 0\n"
    "s_nop 0\n"
    "buffer_load_dwordx4 v2, s[36:39], s67 offen sc1 lds\n"
    "s_waitcnt lgkmcnt(2)\n"
    "v_mfma_f32_32x32x16_bf16 v[32:47], a[60:63], v[68:71], v[32:47]\n"
    "ds_read_b128 v[64:67], v239 offset:18432\n"
    "v_mfma_f32_32x32x16_bf16 v[48:63], a[188:191], v[68:71], v[48:63]\n"
    "buffer_load_dwordx4 v2, s[36:39], s67 offen offset:1024 sc1 lds\n"
    "s_add_u32 s67, s67, 0x4000\n"
    "s_waitcnt lgkmcnt(2)\n"
    "v_mfma_f32_32x32x16_bf16 v[32:47], a[64:67], v[72:75], v[32:47]\n"
    "ds_read_b128 v[68:71], v239 offset:19456\n"
    "v_mfma_f32_32x32x16_bf16 v[48:63], a[192:195], v[72:75], v[48:63]\n"
    "s_add_u32 m0, s66, 8192\n"
    "s_nop 0\n"
    "buffer_load_dwordx4 v2, s[36:39], s67 offen sc1 lds\n"
    "s_waitcnt lgkmcnt(2)\n"
    "v_mfma_f32_32x32x16_bf16 v[32:47], a[68:71], v[76:79], v[32:47]\n"
    "ds_read_b128 v[72:75], v239 offset:20480\n"
    "v_mfma_f32_32x32x16_bf16 v[48:63], a[196:199], v[76:79], v[48:63]\n"
    "buffer_load_dwordx4 v2, s[36:39], s67 offen offset:1024 sc1 lds\n"
    "s_add_u32 s67, s67, 0x4000\n"
    "s_waitcnt lgkmcnt(2)\n"
    "v_mfma_f32_32x32x16_bf16 v[32:47], a[72:75], v[64:67], v[32:47]\n"
    "ds_read_b128 v[76:79], v239 offset:21504\n"
    "v_mfma_f32_32x32x16_bf16 v[48:63], a[200:203], v[64:67], v[48:63]\n"
    "s_waitcnt lgkmcnt(2)\n"
    "v_mfma_f32_32x32x16_bf16 v[32:47], a[76:79], v[68:71], v[32:47]\n"
    "v_mfma_f32_32x32x16_bf16 v[48:63], a[204:207], v[68:71], v[48:63]\n"
    "s_waitcnt lgkmcnt(1)\n"
    "v_mfma_f32_32x32x16_bf16 v[32:47], a[80:83], v[72:75], v[32:47]\n"
    "v_mfma_f32_32x32x16_bf16 v[48:63], a[208:211], v[72:75], v[48:63]\n"
    "s_waitcnt lgkmcnt(0)\n"
    "v_mfma_f32_32x32x16_bf16 v[32:47], a[84:87], v[76:79], v[32:47]\n"
    "v_mfma_f32_32x32x16_bf16 v[48:63], a[212:215], v[76:79], v[48:63]\n"
    "s_waitcnt vmcnt(6)\n"
    "v_mfma_f32_32x32x16_bf16 v[144:159], v[244:247], v[80:83], 0\n"
    "ds_read_b128 v[64:67], v239 offset:22528\n"
    "v_mfma_f32_32x32x16_bf16 v[128:143], v[244:247], v[84:87], 0\n"
    "ds_read_b128 v[68:71], v239 offset:23552\n"
    "s_add_u32 m0, s66, 16384\n"
    "s_nop 0\n"
    "buffer_load_dwordx4 v2, s[36:39], s67 offen sc1 lds\n"
    "v_mfma_f32_32x32x16_bf16 v[112:127], v[244:247], v[88:91], 0\n"
    "ds_read_b128 v[72:75], v239 offset:24576\n"
    "v_mfma_f32_32x32x16_bf16 v[192:207], v[244:247], v[92:95], 0\n"
    "buffer_load_dwordx4 v2, s[36:39], s67 offen offset:1024 sc1 lds\n"
    "s_add_u32 s67, s67, 0x4000\n"
    "s_waitcnt lgkmcnt(2)\n"
    "v_mfma_f32_32x32x16_bf16 v[32:47], a[88:91], v[64:67], v[32:47]\n"
    "ds_read_b128 v[76:79], v239 offset:25600\n"
    "v_add_f32 v144, v144, v132\n"
    "v_add_f32 v145, v145, v133\n"
    "v_add_f32 v146, v146, v134\n"
    "v_add_f32 v147, v147, v135\n"
    "v_mfma_f32_32x32x16_bf16 v[48:63], a[216:219], v[64:67], v[48:63]\n"
    "v_add_f32 v120, v120, v204\n"
    "v_add_f32 v121, v121, v205\n"
    "v_add_f32 v122, v122, v206\n"
    "v_add_f32 v123, v123, v207\n"
    "v_add_f32 v144, v144, v120\n"
    "v_add_f32 v145, v145, v121\n"
    "v_add_f32 v146, v146, v122\n"
    "v_add_f32 v147, v147, v123\n"
    "ds_write_b128 v11, v[144:147] offset:4096\n"
    "s_waitcnt lgkmcnt(3)\n"
    "v_mfma_f32_32x32x16_bf16 v[32:47], a[92:95], v[68:71], v[32:47]\n"
    "s_add_u32 m0, s66, 24576\n"
    "s_nop 0\n"
    "buffer_load_dwordx4 v2, s[36:39], s67 offen sc1 lds\n"
    "v_mfma_f32_32x32x16_bf16 v[48:63], a[220:223], v[68:71], v[48:63]\n"
    "s_waitcnt lgkmcnt(2)\n"
    "v_mfma_f32_32x32x16_bf16 v[32:47], a[96:99], v[72:75], v[32:47]\n"
    "buffer_load_dwordx4 v2, s[36:39], s67 offen offset:1024 sc1 lds\n"
    "v_mfma_f32_32x32x16_bf16 v[48:63], a[224:227], v[72:75], v[48:63]\n"
    "s_waitcnt lgkmcnt(0)\n"
    "s_barrier\n"
    "ds_read_b128 v[148:151], v12 offset:4096\n"
    "ds_read_b128 v[64:67], v239 offset:26624\n"
    "ds_read_b128 v[68:71], v239 offset:27648\n"
    "v_mfma_f32_32x32x16_bf16 v[32:47], a[100:103], v[76:79], v[32:47]\n"
    "buffer_load_dwordx4 v[96:99], v7, s[36:39], s68 offen offset:0 sc1\n"
    "v_mfma_f32_32x32x16_bf16 v[48:63], a[228:231], v[76:79], v[48:63]\n"
    "buffer_load_dwordx4 v[100:103], v7, s[36:39], s68 offen offset:1024 sc1\n"
    "s_waitcnt lgkmcnt(2)\n"
    "v_add_f32 v144, v144, v148\n"
    "v_add_f32 v145, v145, v149\n"
    "v_add_f32 v146, v146, v150\n"
    "v_add_f32 v147, v147, v151\n"
    "v_max3_f32 v228, v144, v145, v146\n"
    "v_max3_f32 v229, v228, v147, v220\n"
    "v_max3_f32 v228, v228, v147, v220\n"
    "s_waitcnt lgkmcnt(1)\n"
    "v_mfma_f32_32x32x16_bf16 v[32:47], a[104:107], v[64:67], v[32:47]\n"
    "v_permlane32_swap_b32 v228, v229\n"
    "v_max_f32 v228, v228, v229\n"
    "v_mul_f32 v224, 0xbe0293ee, v228\n"
    "v_fma_f32 v144, v144, s18, v224\n"
    "v_fma_f32 v145, v145, s18, v224\n"
    "v_mfma_f32_32x32x16_bf16 v[48:63], a[232:235], v[64:67], v[48:63]\n"
    "buffer_load_dwordx4 v[104:107], v7, s[36:39], s68 offen offset:2048 sc1\n"
    "v_fma_f32 v146, v146, s18, v224\n"
    "v_fma_f32 v147, v147, s18, v224\n"
    "v_exp_f32 v144, v144\n"
    "v_exp_f32 v145, v145\n"
    "s_waitcnt lgkmcnt(0)\n"
    "v_mfma_f32_32x32x16_bf16 v[32:47], a[108:111], v[68:71], v[32:47]\n"
    "v_fma_f32 v226, v220, s18, v224\n"
    "v_exp_f32 v146, v146\n"
    "v_exp_f32 v147, v147\n"
    "v_add_f32 v230, v144, v145\n"
    "v_mfma_f32_32x32x16_bf16 v[48:63], a[236:239], v[68:71], v[48:63]\n"
    "buffer_load_dwordx4 v[108:111], v7, s[36:39], s68 offen offset:3072 sc1\n"
    "v_exp_f32 v226, v226\n"
    "v_add_f32 v231, v146, v147\n"
    "v_add_f32 v232, v230, v231\n"
    "v_add_f32 v230, v230, v231\n"
    "ds_read_b128 v[72:75], v239 offset:30720\n"
    "ds_read_b128 v[64:67], v239 offset:28672\n"
    "v_permlane32_swap_b32 v230, v232\n"
    "v_add_f32 v230, v230, v232\n"
    "v_fma_f32 v230, v226, v222, v230\n"
    "v_rcp_f32 v230, v230\n"
    "ds_read_b128 v[68:71], v239 offset:29696\n"
    "v_mul_f32 v226, v226, v230\n"
    "v_pk_mul_f32 v[144:145], v[144:145], v[230:231] op_sel_hi:[1,0]\n"
    "v_pk_mul_f32 v[146:147], v[146:147], v[230:231] op_sel_hi:[1,0]\n"
    "v_cvt_pk_bf16_f32 v212, v144, v145\n"
    "v_cvt_pk_bf16_f32 v213, v146, v147\n"
    "v_pk_mul_f32 v[160:161], v[160:161], v[226:227] op_sel_hi:[1,0]\n"
    "v_pk_mul_f32 v[162:163], v[162:163], v[226:227] op_sel_hi:[1,0]\n"
    "v_pk_mul_f32 v[164:165], v[164:165], v[226:227] op_sel_hi:[1,0]\n"
    "v_pk_mul_f32 v[166:167], v[166:167], v[226:227] op_sel_hi:[1,0]\n"
    "v_pk_mul_f32 v[168:169], v[168:169], v[226:227] op_sel_hi:[1,0]\n"
    "v_pk_mul_f32 v[170:171], v[170:171], v[226:227] op_sel_hi:[1,0]\n"
    "v_pk_mul_f32 v[172:173], v[172:173], v[226:227] op_sel_hi:[1,0]\n"
    "v_pk_mul_f32 v[174:175], v[174:175], v[226:227] op_sel_hi:[1,0]\n"
    "v_pk_mul_f32 v[176:177], v[176:177], v[226:227] op_sel_hi:[1,0]\n"
    "v_pk_mul_f32 v[178:179], v[178:179], v[226:227] op_sel_hi:[1,0]\n"
    "v_pk_mul_f32 v[180:181], v[180:181], v[226:227] op_sel_hi:[1,0]\n"
    "v_pk_mul_f32 v[182:183], v[182:183], v[226:227] op_sel_hi:[1,0]\n"
    "v_pk_mul_f32 v[184:185], v[184:185], v[226:227] op_sel_hi:[1,0]\n"
    "v_pk_mul_f32 v[186:187], v[186:187], v[226:227] op_sel_hi:[1,0]\n"
    "v_pk_mul_f32 v[188:189], v[188:189], v[226:227] op_sel_hi:[1,0]\n"
    "v_pk_mul_f32 v[190:191], v[190:191], v[226:227] op_sel_hi:[1,0]\n"
    "s_waitcnt vmcnt(12)\n"
    "v_mfma_f32_32x32x8_bf16 v[160:175], v[208:209], v[212:213], v[160:175]\n"
    "v_mfma_f32_32x32x8_bf16 v[176:191], v[210:211], v[212:213], v[176:191]\n"
    "s_waitcnt lgkmcnt(1)\n"
    "v_mfma_f32_32x32x16_bf16 v[32:47], a[112:115], v[64:67], v[32:47]\n"
    "ds_read_b128 v[76:79], v239 offset:31744\n"
    "s_nop 2\n"
    "v_cvt_pk_bf16_f32 v144, v160, v161\n"
    "v_cvt_pk_bf16_f32 v145, v162, v163\n"
    "v_cvt_pk_bf16_f32 v146, v164, v165\n"
    "v_cvt_pk_bf16_f32 v147, v166, v167\n"
    "v_mfma_f32_32x32x16_bf16 v[48:63], a[240:243], v[64:67], v[48:63]\n"
    "buffer_store_dwordx4 v[144:147], v6, s[36:39], s69 offen offset:0 sc1\n"
    "s_waitcnt lgkmcnt(1)\n"
    "v_mfma_f32_32x32x16_bf16 v[32:47], a[116:119], v[68:71], v[32:47]\n"
    "v_cvt_pk_bf16_f32 v148, v168, v169\n"
    "v_cvt_pk_bf16_f32 v149, v170, v171\n"
    "v_cvt_pk_bf16_f32 v150, v172, v173\n"
    "v_cvt_pk_bf16_f32 v151, v174, v175\n"
    "v_mfma_f32_32x32x16_bf16 v[48:63], a[244:247], v[68:71], v[48:63]\n"
    "buffer_store_dwordx4 v[148:151], v6, s[36:39], s69 offen offset:512 sc1\n"
    "v_mfma_f32_32x32x16_bf16 v[32:47], a[120:123], v[72:75], v[32:47]\n"
    "v_cvt_pk_bf16_f32 v152, v176, v177\n"
    "v_cvt_pk_bf16_f32 v153, v178, v179\n"
    "v_cvt_pk_bf16_f32 v154, v180, v181\n"
    "v_cvt_pk_bf16_f32 v155, v182, v183\n"
    "v_mfma_f32_32x32x16_bf16 v[48:63], a[248:251], v[72:75], v[48:63]\n"
    "buffer_store_dwordx4 v[152:155], v6, s[36:39], s69 offen offset:2048 sc1\n"
    "s_waitcnt lgkmcnt(0)\n"
    "v_mfma_f32_32x32x16_bf16 v[32:47], a[124:127], v[76:79], v[32:47]\n"
    "v_cvt_pk_bf16_f32 v156, v184, v185\n"
    "v_cvt_pk_bf16_f32 v157, v186, v187\n"
    "v_cvt_pk_bf16_f32 v158, v188, v189\n"
    "v_cvt_pk_bf16_f32 v159, v190, v191\n"
    "s_waitcnt vmcnt(3)\n"
    "s_barrier\n"
    "v_mfma_f32_32x32x16_bf16 v[48:63], a[252:255], v[76:79], v[48:63]\n"
    "buffer_store_dwordx4 v[156:159], v6, s[36:39], s69 offen offset:2560 sc1\n"
    "s_xor_b32 s62, s62, 0x8000\n"
    "s_xor_b32 s63, s63, 0x8000\n"
    "v_add_u32 v239, s62, v3\n"
    "v_mov_b32 v6, v243\n"
    "v_cvt_pk_bf16_f32 v80, v32, v33\n"
    "v_cvt_pk_bf16_f32 v81, v34, v35\n"
    "v_cvt_pk_bf16_f32 v82, v36, v37\n"
    "v_cvt_pk_bf16_f32 v83, v38, v39\n"
    "v_cvt_pk_bf16_f32 v84, v40, v41\n"
    "v_cvt_pk_bf16_f32 v85, v42, v43\n"
    "v_cvt_pk_bf16_f32 v86, v44, v45\n"
    "v_cvt_pk_bf16_f32 v87, v46, v47\n"
    "v_mfma_f32_32x32x16_bf16 v[128:143], v[96:99], v[80:83], v[16:31]\n"
    "v_cvt_pk_bf16_f32 v88, v48, v49\n"
    "v_cvt_pk_bf16_f32 v89, v50, v51\n"
    "v_cvt_pk_bf16_f32 v90, v52, v53\n"
    "v_cvt_pk_bf16_f32 v91, v54, v55\n"
    "v_mfma_f32_32x32x16_bf16 v[128:143], v[100:103], v[84:87], v[128:143]\n"
    "v_cvt_pk_bf16_f32 v92, v56, v57\n"
    "v_cvt_pk_bf16_f32 v93, v58, v59\n"
    "v_cvt_pk_bf16_f32 v94, v60, v61\n"
    "v_cvt_pk_bf16_f32 v95, v62, v63\n"
    "v_mfma_f32_32x32x16_bf16 v[128:143], v[104:107], v[88:91], v[128:143]\n"
    "buffer_load_dwordx4 v[112:115], v7, s[36:39], s99 offen offset:0 sc1\n"
    "ds_read_b128 v[64:67], v239 offset:0\n"
    "ds_read_b128 v[68:71], v239 offset:1024\n"
    "ds_read_b128 v[72:75], v239 offset:2048\n"
    "v_mfma_f32_32x32x16_bf16 v[128:143], v[108:111], v[92:95], v[128:143]\n"
    "s_waitcnt lgkmcnt(2)\n"
    "v_mfma_f32_32x32x16_bf16 v[32:47], a[0:3], v[64:67], 0\n"
    "ds_read_b128 v[76:79], v239 offset:3072\n"
    "buffer_load_dwordx4 v[116:119], v7, s[36:39], s99 offen offset:1024 sc1\n"
    "v_mfma_f32_32x32x16_bf16 v[48:63], a[128:131], v[64:67], 0\n"
    "ds_write_b128 v11, v[128:131] offset:0\n"
    "s_waitcnt lgkmcnt(3)\n"
    "v_mfma_f32_32x32x16_bf16 v[32:47], a[4:7], v[68:71], v[32:47]\n"
    "ds_read_b128 v[64:67], v239 offset:4096\n"
    "ds_write_b128 v11, v[132:135] offset:1024\n"
    "v_mfma_f32_32x32x16_bf16 v[48:63], a[132:135], v[68:71], v[48:63]\n"
    "ds_write_b128 v11, v[136:139] offset:2048\n"
    "s_waitcnt lgkmcnt(5)\n"
    "v_mfma_f32_32x32x16_bf16 v[32:47], a[8:11], v[72:75], v[32:47]\n"
    "ds_read_b128 v[68:71], v239 offset:5120\n"
    "ds_write_b128 v11, v[140:143] offset:3072\n"
    "v_mfma_f32_32x32x16_bf16 v[48:63], a[136:139], v[72:75], v[48:63]\n"
    "buffer_load_dwordx4 v[120:123], v7, s[36:39], s99 offen offset:2048 sc1\n"
    "v_add_u32 v237, s60, v8\n"
    "v_min_u32 v237, 64, v237\n"
    "s_lshr_b32 s71, s58, 8\n"
    "s_waitcnt lgkmcnt(6)\n"
    "v_mfma_f32_32x32x16_bf16 v[32:47], a[12:15], v[76:79], v[32:47]\n"
    "ds_read_b128 v[72:75], v239 offset:6144\n"
    "buffer_load_dwordx4 v[124:127], v7, s[36:39], s99 offen offset:3072 sc1\n"
    "v_mfma_f32_32x32x16_bf16 v[48:63], a[140:143], v[76:79], v[48:63]\n"
    "s_waitcnt vmcnt(4)\n"
    "s_waitcnt lgkmcnt(1)\n"
    "s_barrier\n"
    "ds_read_b128 v[144:147], v12 offset:0\n"
    "ds_read_b128 v[148:151], v12 offset:1024\n"
    "buffer_store_dword v240, v5, s[36:39], 0 offen sc1\n"
    "v_mfma_f32_32x32x16_bf16 v[32:47], a[16:19], v[64:67], v[32:47]\n"
    "ds_read_b128 v[76:79], v239 offset:7168\n"
    "ds_read_b128 v[152:155], v12 offset:2048\n"
    "ds_read_b128 v[156:159], v12 offset:3072\n"
    "v_mfma_f32_32x32x16_bf16 v[48:63], a[144:147], v[64:67], v[48:63]\n"
    "s_xor_b32 s71, s71, s52\n"
    "s_waitcnt lgkmcnt(4)\n"
    "v_add_f32 v128, v128, v144\n"
    "v_add_f32 v129, v129, v145\n"
    "v_add_f32 v130, v130, v146\n"
    "v_add_f32 v131, v131, v147\n"
    "s_waitcnt lgkmcnt(3)\n"
    "v_add_f32 v132, v132, v148\n"
    "v_add_f32 v133, v133, v149\n"
    "v_mfma_f32_32x32x16_bf16 v[32:47], a[20:23], v[68:71], v[32:47]\n"
    "ds_read_b128 v[64:67], v239 offset:8192\n"
    "v_add_f32 v134, v134, v150\n"
    "v_add_f32 v135, v135, v151\n"
    "s_waitcnt lgkmcnt(2)\n"
    "v_add_f32 v136, v136, v152\n"
    "v_add_f32 v137, v137, v153\n"
    "v_mfma_f32_32x32x16_bf16 v[48:63], a[148:151], v[68:71], v[48:63]\n"
    "s_and_b32 s71, s71, 0x2000\n"
    "v_add_f32 v138, v138, v154\n"
    "v_add_f32 v139, v139, v155\n"
    "s_waitcnt lgkmcnt(1)\n"
    "v_add_f32 v140, v140, v156\n"
    "v_add_f32 v141, v141, v157\n"
    "v_add_f32 v142, v142, v158\n"
    "v_add_f32 v143, v143, v159\n"
    "v_mfma_f32_32x32x16_bf16 v[32:47], a[24:27], v[72:75], v[32:47]\n"
    "ds_read_b128 v[68:71], v239 offset:9216\n"
    "v_max3_f32 v220, v128, v129, v130\n"
    "v_max3_f32 v221, v131, v132, v133\n"
    "v_max3_f32 v220, v220, v134, v135\n"
    "v_max3_f32 v221, v221, v136, v137\n"
    "v_mfma_f32_32x32x16_bf16 v[48:63], a[152:155], v[72:75], v[48:63]\n"
    "s_or_b32 s71, s71, s58\n"
    "v_max3_f32 v220, v220, v138, v139\n"
    "v_max3_f32 v221, v221, v140, v141\n"
    "v_max3_f32 v220, v220, v142, v143\n"
    "v_max_f32 v220, v220, v221\n"
    "v_mov_b32 v221, v220\n"
    "s_add_u32 s58, s58, 0x20000\n"
    "v_mfma_f32_32x32x16_bf16 v[32:47], a[28:31], v[76:79], v[32:47]\n"
    "ds_read_b128 v[72:75], v239 offset:10240\n"
    "v_permlane32_swap_b32 v220, v221\n"
    "v_max_f32 v220, v220, v221\n"
    "v_mul_f32 v224, 0xbe0293ee, v220\n"
    "v_mfma_f32_32x32x16_bf16 v[48:63], a[156:159], v[76:79], v[48:63]\n"
    "s_add_u32 s59, s71, 0x800000\n"
    "v_fma_f32 v128, v128, s18, v224\n"
    "v_fma_f32 v129, v129, s18, v224\n"
    "v_exp_f32 v128, v128\n"
    "v_exp_f32 v129, v129\n"
    "s_waitcnt lgkmcnt(2)\n"
    "v_mfma_f32_32x32x16_bf16 v[32:47], a[32:35], v[64:67], v[32:47]\n"
    "ds_read_b128 v[76:79], v239 offset:11264\n"
    "v_fma_f32 v130, v130, s18, v224\n"
    "v_fma_f32 v131, v131, s18, v224\n"
    "v_exp_f32 v130, v130\n"
    "v_mfma_f32_32x32x16_bf16 v[48:63], a[160:163], v[64:67], v[48:63]\n"
    "s_lshr_b32 s69, s48, 8\n"
    "v_exp_f32 v131, v131\n"
    "v_fma_f32 v132, v132, s18, v224\n"
    "v_fma_f32 v133, v133, s18, v224\n"
    "v_exp_f32 v132, v132\n"
    "s_waitcnt lgkmcnt(2)\n"
    "v_mfma_f32_32x32x16_bf16 v[32:47], a[36:39], v[68:71], v[32:47]\n"
    "ds_read_b128 v[64:67], v239 offset:12288\n"
    "v_exp_f32 v133, v133\n"
    "v_cvt_pk_bf16_f32 v212, v128, v129\n"
    "v_fma_f32 v134, v134, s18, v224\n"
    "v_mfma_f32_32x32x16_bf16 v[48:63], a[164:167], v[68:71], v[48:63]\n"
    "s_xor_b32 s69, s69, s52\n"
    "v_fma_f32 v135, v135, s18, v224\n"
    "v_exp_f32 v134, v134\n"
    "v_exp_f32 v135, v135\n"
    "v_cvt_pk_bf16_f32 v213, v130, v131\n"
    "s_waitcnt lgkmcnt(2)\n"
    "v_mfma_f32_32x32x16_bf16 v[32:47], a[40:43], v[72:75], v[32:47]\n"
    "ds_read_b128 v[68:71], v239 offset:13312\n"
    "v_fma_f32 v136, v136, s18, v224\n"
    "v_fma_f32 v137, v137, s18, v224\n"
    "v_exp_f32 v136, v136\n"
    "v_mfma_f32_32x32x16_bf16 v[48:63], a[168:171], v[72:75], v[48:63]\n"
    "s_and_b32 s69, s69, 0x2000\n"
    "v_exp_f32 v137, v137\n"
    "v_cvt_pk_bf16_f32 v214, v132, v133\n"
    "v_fma_f32 v138, v138, s18, v224\n"
    "v_fma_f32 v139, v139, s18, v224\n"
    "s_or_b32 s69, s69, s48\n"
    "s_waitcnt lgkmcnt(2)\n"
    "v_mfma_f32_32x32x16_bf16 v[32:47], a[44:47], v[76:79], v[32:47]\n"
    "s_add_u32 s48, s48, s49\n"
    "v_exp_f32 v138, v138\n"
    "v_exp_f32 v139, v139\n"
    "v_cvt_pk_bf16_f32 v215, v134, v135\n"
    "v_fma_f32 v140, v140, s18, v224\n"
    "v_mfma_f32_32x32x16_bf16 v[48:63], a[172:175], v[76:79], v[48:63]\n"
    "buffer_load_dword v236, v4, s[36:39], 0 offen sc1\n"
    "v_fma_f32 v141, v141, s18, v224\n"
    "v_exp_f32 v140, v140\n"
    "s_waitcnt lgkmcnt(1)\n"
    "v_mfma_f32_32x32x16_bf16 v[32:47], a[48:51], v[64:67], v[32:47]\n"
    "s_lshr_b32 s68, s57, 8\n"
    "v_exp_f32 v141, v141\n"
    "v_fma_f32 v142, v142, s18, v224\n"
    "v_fma_f32 v143, v143, s18, v224\n"
    "v_exp_f32 v142, v142\n"
    "v_mfma_f32_32x32x16_bf16 v[48:63], a[176:179], v[64:67], v[48:63]\n"
    "s_xor_b32 s68, s68, s52\n"
    "v_exp_f32 v143, v143\n"
    "s_and_b32 s68, s68, 0x2000\n"
    "s_or_b32 s68, s68, s57\n"
    "s_add_u32 s57, s57, 0x80000\n"
    "s_add_u32 s99, s68, 0x2000000\n"
    "s_waitcnt lgkmcnt(0)\n"
    "v_mfma_f32_32x32x16_bf16 v[32:47], a[52:55], v[68:71], v[32:47]\n"
    "s_add_u32 s60, s60, 1\n"
    "v_mov_b32 v240, s60\n"
    "s_lshr_b32 s67, s46, 8\n"
    "s_xor_b32 s67, s67, s51\n"
    "s_and_b32 s67, s67, 0x2000\n"
    "s_or_b32 s67, s67, s46\n"
    "s_add_u32 s66, s53, s63\n"
    "v_mfma_f32_32x32x16_bf16 v[48:63], a[180:183], v[68:71], v[48:63]\n"
    "s_add_u32 s46, s46, s47\n"
    "s_waitcnt vmcnt(1)\n"
    "v_mfma_f32_32x32x16_bf16 v[160:175], v[112:115], v[212:215], 0\n"
    "ds_read_b128 v[64:67], v239 offset:14336\n"
    "v_cvt_pk_bf16_f32 v216, v136, v137\n"
    "v_cvt_pk_bf16_f32 v217, v138, v139\n"
    "v_cvt_pk_bf16_f32 v218, v140, v141\n"
    "v_cvt_pk_bf16_f32 v219, v142, v143\n"
    "v_add_f32 v128, v128, v136\n"
    "v_mfma_f32_32x32x16_bf16 v[176:191], v[120:123], v[212:215], 0\n"
    "ds_read_b128 v[68:71], v239 offset:15360\n"
    "v_add_f32 v129, v129, v137\n"
    "v_add_f32 v130, v130, v138\n"
    "v_add_f32 v131, v131, v139\n"
    "v_add_f32 v132, v132, v140\n"
    "v_add_f32 v133, v133, v141\n"
    "v_mfma_f32_32x32x16_bf16 v[160:175], v[116:119], v[216:219], v[160:175]\n"
    "ds_read_b128 v[72:75], v239 offset:16384\n"
    "v_add_f32 v134, v134, v142\n"
    "v_add_f32 v135, v135, v143\n"
    "v_add_f32 v128, v128, v132\n"
    "v_add_f32 v129, v129, v133\n"
    "v_add_f32 v130, v130, v134\n"
    "v_mfma_f32_32x32x16_bf16 v[176:191], v[124:127], v[216:219], v[176:191]\n"
    "v_add_f32 v131, v131, v135\n"
    "v_add_f32 v128, v128, v130\n"
    "v_add_f32 v129, v129, v131\n"
    "v_add_f32 v222, v128, v129\n"
    "v_mov_b32 v223, v222\n"
    "s_nop 1\n"
    "v_permlane32_swap_b32 v222, v223\n"
    "v_add_f32 v222, v222, v223\n"
    "s_waitcnt vmcnt(0)\n"
    "s_branch L_QA_LOOP%=\n"
    "L_QA_W0f%=:\n"
    "s_nop 0\n"
    "s_cmp_lg_u32 s65, 0\n"
    "s_cbranch_scc1 L_QA_W0ok%=\n"
    "s_add_u32 s64, s64, 1\n"
    "s_and_b32 s70, s64, 255\n"
    "s_cbranch_scc1 L_QA_W0%=\n"
    "buffer_load_dword v238, off, s[36:39], s50 sc1\n"
    "s_waitcnt vmcnt(0)\n"
    "v_readfirstlane_b32 s65, v238\n"
    "s_cmp_lt_u32 s64, 0x40000\n"
    "s_cbranch_scc1 L_QA_W0%=\n"
    "s_mov_b32 s65, 1\n"
    "v_mov_b32 v238, 1\n"
    "buffer_store_dword v238, off, s[36:39], s50 sc1\n"
    "s_branch L_QA_W0%=\n"
    "L_QA_Vf%=:\n"
    "s_nop 0\n"
    "s_cmp_lg_u32 s65, 0\n"
    "s_cbranch_scc1 L_QA_Vok%=\n"
    "s_add_u32 s64, s64, 1\n"
    "s_and_b32 s70, s64, 255\n"
    "s_cbranch_scc1 L_QA_V%=\n"
    "buffer_load_dword v238, off, s[36:39], s50 sc1\n"
    "s_waitcnt vmcnt(0)\n"
    "v_readfirstlane_b32 s65, v238\n"
    "s_cmp_lt_u32 s64, 0x40000\n"
    "s_cbranch_scc1 L_QA_V%=\n"
    "s_mov_b32 s65, 1\n"
    "v_mov_b32 v238, 1\n"
    "buffer_store_dword v238, off, s[36:39], s50 sc1\n"
    "s_branch L_QA_V%=\n"
    "L_QA_Wf%=:\n"
    "s_nop 0\n"
    "s_cmp_lg_u32 s65, 0\n"
    "s_cbranch_scc1 L_QA_Wok%=\n"
    "s_add_u32 s64, s64, 1\n"
    "s_and_b32 s70, s64, 255\n"
    "s_cbranch_scc1 L_QA_W%=\n"
    "buffer_load_dword v238, off, s[36:39], s50 sc1\n"
    "s_waitcnt vmcnt(0)\n"
    "v_readfirstlane_b32 s65, v238\n"
    "s_cmp_lt_u32 s64, 0x40000\n"
    "s_cbranch_scc1 L_QA_W%=\n"
    "s_mov_b32 s65, 1\n"
    "v_mov_b32 v238, 1\n"
    "buffer_store_dword v238, off, s[36:39], s50 sc1\n"
    "s_branch L_QA_W%=\n"
    "L_QA_REDO%=:\n"
    "s_lshl_b32 s70, s70, 11\n"
    "s_lshl_b32 s71, s44, 6\n"
    "s_sub_i32 s70, s70, s71\n"
    "s_ashr_i32 s71, s70, 31\n"
    "s_add_u32 s40, s40, s70\n"
    "s_addc_u32 s41, s41, s71\n"
    "s_branch L_QA_PRO%=\n"
    "L_QA_TAIL%=:\n"
    "s_waitcnt vmcnt(0)\n"
    "s_nop 7\n"
    "v_mfma_f32_32x32x16_bf16 v[144:159], v[244:247], v[80:83], 0\n"
    "v_mfma_f32_32x32x16_bf16 v[128:143], v[244:247], v[84:87], 0\n"
    "v_mfma_f32_32x32x16_bf16 v[112:127], v[244:247], v[88:91], 0\n"
    "v_mfma_f32_32x32x16_bf16 v[192:207], v[244:247], v[92:95], 0\n"
    "v_add_f32 v144, v144, v132\n"
    "v_add_f32 v145, v145, v133\n"
    "v_add_f32 v146, v146, v134\n"
    "v_add_f32 v147, v147, v135\n"
    "s_nop 6\n"
    "v_add_f32 v120, v120, v204\n"
    "v_add_f32 v121, v121, v205\n"
    "v_add_f32 v122, v122, v206\n"
    "v_add_f32 v123, v123, v207\n"
    "v_add_f32 v144, v144, v120\n"
    "v_add_f32 v145, v145, v121\n"
    "v_add_f32 v146, v146, v122\n"
    "v_add_f32 v147, v147, v123\n"
    "ds_write_b128 v11, v[144:147] offset:4096\n"
    "s_waitcnt lgkmcnt(0)\n"
    "s_barrier\n"
    "ds_read_b128 v[148:151], v12 offset:4096\n"
    "s_waitcnt lgkmcnt(0)\n"
    "v_add_f32 v144, v144, v148\n"
    "v_add_f32 v145, v145, v149\n"
    "v_add_f32 v146, v146, v150\n"
    "v_add_f32 v147, v147, v151\n"
    "v_max3_f32 v228, v144, v145, v146\n"
    "v_max3_f32 v229, v228, v147, v220\n"
    "v_max3_f32 v228, v228, v147, v220\n"
    "s_nop 1\n"
    "v_permlane32_swap_b32 v228, v229\n"
    "v_max_f32 v228, v228, v229\n"
    "v_mul_f32 v224, 0xbe0293ee, v228\n"
    "v_fma_f32 v144, v144, s18, v224\n"
    "v_fma_f32 v145, v145, s18, v224\n"
    "v_fma_f32 v146, v146, s18, v224\n"
    "v_fma_f32 v147, v147, s18, v224\n"
    "v_exp_f32 v144, v144\n"
    "v_exp_f32 v145, v145\n"
    "v_fma_f32 v226, v220, s18, v224\n"
    "v_exp_f32 v146, v146\n"
    "v_exp_f32 v147, v147\n"
    "v_add_f32 v230, v144, v145\n"
    "v_exp_f32 v226, v226\n"
    "v_add_f32 v231, v146, v147\n"
    "v_add_f32 v232, v230, v231\n"
    "v_add_f32 v230, v230, v231\n"
    "s_nop 1\n"
    "v_permlane32_swap_b32 v230, v232\n"
    "v_add_f32 v230, v230, v232\n"
    "v_fma_f32 v230, v226, v222, v230\n"
    "v_rcp_f32 v230, v230\n"
    "s_nop 0\n"
    "v_mul_f32 v226, v226, v230\n"
    "v_pk_mul_f32 v[144:145], v[144:145], v[230:231] op_sel_hi:[1,0]\n"
    "v_pk_mul_f32 v[146:147], v[146:147], v[230:231] op_sel_hi:[1,0]\n"
    "v_cvt_pk_bf16_f32 v212, v144, v145\n"
    "v_cvt_pk_bf16_f32 v213, v146, v147\n"
    "v_pk_mul_f32 v[160:161], v[160:161], v[226:227] op_sel_hi:[1,0]\n"
    "v_pk_mul_f32 v[162:163], v[162:163], v[226:227] op_sel_hi:[1,0]\n"
    "v_pk_mul_f32 v[164:165], v[164:165], v[226:227] op_sel_hi:[1,0]\n"
    "v_pk_mul_f32 v[166:167], v[166:167], v[226:227] op_sel_hi:[1,0]\n"
    "v_pk_mul_f32 v[168:169], v[168:169], v[226:227] op_sel_hi:[1,0]\n"
    "v_pk_mul_f32 v[170:171], v[170:171], v[226:227] op_sel_hi:[1,0]\n"
    "v_pk_mul_f32 v[172:173], v[172:173], v[226:227] op_sel_hi:[1,0]\n"
    "v_pk_mul_f32 v[174:175], v[174:175], v[226:227] op_sel_hi:[1,0]\n"
    "v_pk_mul_f32 v[176:177], v[176:177], v[226:227] op_sel_hi:[1,0]\n"
    "v_pk_mul_f32 v[178:179], v[178:179], v[226:227] op_sel_hi:[1,0]\n"
    "v_pk_mul_f32 v[180:181], v[180:181], v[226:227] op_sel_hi:[1,0]\n"
    "v_pk_mul_f32 v[182:183], v[182:183], v[226:227] op_sel_hi:[1,0]\n"
    "v_pk_mul_f32 v[184:185], v[184:185], v[226:227] op_sel_hi:[1,0]\n"
    "v_pk_mul_f32 v[186:187], v[186:187], v[226:227] op_sel_hi:[1,0]\n"
    "v_pk_mul_f32 v[188:189], v[188:189], v[226:227] op_sel_hi:[1,0]\n"
    "v_pk_mul_f32 v[190:191], v[190:191], v[226:227] op_sel_hi:[1,0]\n"
    "v_mfma_f32_32x32x8_bf16 v[160:175], v[208:209], v[212:213], v[160:175]\n"
    "v_mfma_f32_32x32x8_bf16 v[176:191], v[210:211], v[212:213], v[176:191]\n"
    "s_nop 7\n"
    "v_cvt_pk_bf16_f32 v144, v160, v161\n"
    "v_cvt_pk_bf16_f32 v145, v162, v163\n"
    "v_cvt_pk_bf16_f32 v146, v164, v165\n"
    "v_cvt_pk_bf16_f32 v147, v166, v167\n"
    "buffer_store_dwordx4 v[144:147], v6, s[36:39], s69 offen offset:0 sc1\n"
    "v_cvt_pk_bf16_f32 v148, v168, v169\n"
    "v_cvt_pk_bf16_f32 v149, v170, v171\n"
    "v_cvt_pk_bf16_f32 v150, v172, v173\n"
    "v_cvt_pk_bf16_f32 v151, v174, v175\n"
    "buffer_store_dwordx4 v[148:151], v6, s[36:39], s69 offen offset:512 sc1\n"
    "v_cvt_pk_bf16_f32 v152, v176, v177\n"
    "v_cvt_pk_bf16_f32 v153, v178, v179\n"
    "v_cvt_pk_bf16_f32 v154, v180, v181\n"
    "v_cvt_pk_bf16_f32 v155, v182, v183\n"
    "buffer_store_dwordx4 v[152:155], v6, s[36:39], s69 offen offset:2048 sc1\n"
    "v_cvt_pk_bf16_f32 v156, v184, v185\n"
    "v_cvt_pk_bf16_f32 v157, v186, v187\n"
    "v_cvt_pk_bf16_f32 v158, v188, v189\n"
    "v_cvt_pk_bf16_f32 v159, v190, v191\n"
    "buffer_store_dwordx4 v[156:159], v6, s[36:39], s69 offen offset:2560 sc1\n"
    "s_waitcnt vmcnt(0)\n"
    "s_barrier\n"
    "buffer_store_dword v240, v5, s[36:39], 0 offen sc1\n"
    "s_waitcnt vmcnt(0)\n"
    "L_QA_FIN:\ns_endpgm\n"
      GEMM_OPS, "{v[8:15]}"(vb), "{s[74:75]}"(v2u{blockIdx.x, c.slot >> 5}) : ROLE_CLOB);
  }
}

template <bool SECOND> DI void role_RED(const Params& p, const Ctx& c, unsigned rg, v2u tag) {
  constexpr unsigned NP = SECOND ? 2 : 4;
  const unsigned tid = threadIdx.x, lane = tid & 63, w = UNI(tid >> 6), tk = lane & 7, c8 = lane >> 3, tok = 8 * rg + tk;
  const unsigned qn = SECOND ? c.q ^ 0x2000u : c.q;
  const v2u px = ptr2(SECOND ? (const u16*)p.out : p.x);
  v8u sa = {px.x, px.y, 0u, 0u, 0u, 0u, SECOND ? O_PD : O_PW, NP * 65536};
  v8u sb = {SECOND ? O_XM : O_X2, SECOND ? O_X2 : O_XM, O_ABORT, UNI(c.q), UNI(c.q), UNI(qn), 0u, UNI(O_STAMP + c.slot * 8192 + w * 2048)};
  v4u sc = {UNI(c.l ^ (SECOND ? 0u : 1u)), SECOND ? O_XN : O_XNM, UNI(qn), 0u};
  const v8u va = {w * 16384 + c8 * 512 + tok * 16, flag((SECOND ? F_DN : F_WO) + 4 * (lane & (NP - 1)) + w, c.q),
                  tid != 0 ? OOB : (SECOND && c.l ? (rg == 0 ? O_EPOCH : OOB) : flag((SECOND ? F_XN : F_R1) + rg, qn)), tok * 1024 + w * 256 + c8 * 16,
                  c.lb + (lane >> 5) * 128 + tk * 16 + w * 4, c.lb + tk * 16, tag.x, tag.y};
  if constexpr (SECOND) {
    asm volatile("s_mov_b64 exec, -1\ns_and_b32 s37, s37, 0xffff\ns_mov_b32 s39, 0x20000\ns_nop 4\n"
    "v_add_u32 v13, 0x1000, v0\n"
    "v_readfirstlane_b32 s17, v0\n"
    "s_lshr_b32 s17, s17, 14\n"
    "s_mov_b32 s64, 0\n"
    "s_mov_b32 s65, 0\n"
    "v_mov_b32 v8, 0\n"
    "s_mov_b32 s60, 0\n"
    "s_mov_b32 s61, 1\n"
    "s_mov_b32 s59, 0xffff0000\n"
    "L_RED2_LOOP%=:\n"
    "s_lshr_b32 s68, s46, 8\n"
    "s_xor_b32 s68, s68, s51\n"
    "s_and_b32 s68, s68, 0x2000\n"
    "s_or_b32 s68, s68, s46\n"
    "s_add_u32 s46, s46, s47\n"
    "s_lshr_b32 s69, s48, 8\n"
    "s_xor_b32 s69, s69, s52\n"
    "s_and_b32 s69, s69, 0x2000\n"
    "s_or_b32 s69, s69, s48\n"
    "s_add_u32 s48, s48, 0x10000\n"
    "s_lshr_b32 s62, s49, 8\n"
    "s_xor_b32 s62, s62, s53\n"
    "s_and_b32 s62, s62, 0x2000\n"
    "s_or_b32 s62, s62, s49\n"
    "s_add_u32 s49, s49, 0x10000\n"
    "s_lshr_b32 s63, s57, 8\n"
    "s_xor_b32 s63, s63, s58\n"
    "s_and_b32 s63, s63, 0x2000\n"
    "s_or_b32 s63, s63, s57\n"
    "s_add_u32 s57, s57, 0x10000\n"
    "v_mov_b32 v10, s61\n"
    "v_cmp_gt_i32 vcc, s61, v8\n"
    "s_cbranch_vccz L_RED2_Wok%=\n"
    "L_RED2_W%=:\n"
    "buffer_load_dword v8, v1, s[36:39], 0 offen sc1\n"
    "s_waitcnt vmcnt(0)\n"
    "v_cmp_gt_i32 vcc, s61, v8\n"
    "s_cbranch_vccnz L_RED2_Wf%=\n"
    "L_RED2_Wok%=:\n"
    "buffer_load_dwordx4 v[32:35], v0, s[36:39], s69 offen sc1\n"
    "buffer_load_dwordx4 v[36:39], v13, s[36:39], s69 offen sc1\n"
    "L_RED2_HASX%=:\n"
    "buffer_load_dwordx4 v[16:19], v0, s[36:39], s68 offen sc1\n"
    "buffer_load_dwordx4 v[20:23], v13, s[36:39], s68 offen sc1\n"
    "s_add_u32 s68, s68, 0x10000\n"
    "buffer_load_dwordx4 v[24:27], v0, s[36:39], s68 offen sc1\n"
    "buffer_load_dwordx4 v[28:31], v13, s[36:39], s68 offen sc1\n"
    "s_waitcnt vmcnt(4)\n"
    "v_lshlrev_b32 v64, 16, v32\n"
    "v_and_b32 v65, s59, v32\n"
    "v_lshlrev_b32 v66, 16, v33\n"
    "v_and_b32 v67, s59, v33\n"
    "v_lshlrev_b32 v68, 16, v34\n"
    "v_and_b32 v69, s59, v34\n"
    "v_lshlrev_b32 v70, 16, v35\n"
    "v_and_b32 v71, s59, v35\n"
    "v_lshlrev_b32 v72, 16, v36\n"
    "v_and_b32 v73, s59, v36\n"
    "v_lshlrev_b32 v74, 16, v37\n"
    "v_and_b32 v75, s59, v37\n"
    "v_lshlrev_b32 v76, 16, v38\n"
    "v_and_b32 v77, s59, v38\n"
    "v_lshlrev_b32 v78, 16, v39\n"
    "v_and_b32 v79, s59, v39\n"
    "s_waitcnt vmcnt(3)\n"
    "v_max3_u32 v9, v16, v17, v18\n"
    "v_max_u32 v9, v9, v19\n"
    "v_lshlrev_b32 v80, 16, v16\n"
    "v_and_b32 v81, s59, v16\n"
    "v_lshlrev_b32 v82, 16, v17\n"
    "v_and_b32 v83, s59, v17\n"
    "v_lshlrev_b32 v84, 16, v18\n"
    "v_and_b32 v85, s59, v18\n"
    "v_lshlrev_b32 v86, 16, v19\n"
    "v_and_b32 v87, s59, v19\n"
    "v_pk_add_f32 v[64:65], v[64:65], v[80:81]\n"
    "v_pk_add_f32 v[66:67], v[66:67], v[82:83]\n"
    "v_pk_add_f32 v[68:69], v[68:69], v[84:85]\n"
    "v_pk_add_f32 v[70:71], v[70:71], v[86:87]\n"
    "s_waitcnt vmcnt(2)\n"
    "v_max3_u32 v9, v9, v20, v21\n"
    "v_max3_u32 v9, v9, v22, v23\n"
    "v_lshlrev_b32 v80, 16, v20\n"
    "v_and_b32 v81, s59, v20\n"
    "v_lshlrev_b32 v82, 16, v21\n"
    "v_and_b32 v83, s59, v21\n"
    "v_lshlrev_b32 v84, 16, v22\n"
    "v_and_b32 v85, s59, v22\n"
    "v_lshlrev_b32 v86, 16, v23\n"
    "v_and_b32 v87, s59, v23\n"
    "v_pk_add_f32 v[72:73], v[72:73], v[80:81]\n"
    "v_pk_add_f32 v[74:75], v[74:75], v[82:83]\n"
    "v_pk_add_f32 v[76:77], v[76:77], v[84:85]\n"
    "v_pk_add_f32 v[78:79], v[78:79], v[86:87]\n"
    "s_waitcnt vmcnt(1)\n"
    "v_max3_u32 v9, v9, v24, v25\n"
    "v_max3_u32 v9, v9, v26, v27\n"
    "v_lshlrev_b32 v80, 16, v24\n"
    "v_and_b32 v81, s59, v24\n"
    "v_lshlrev_b32 v82, 16, v25\n"
    "v_and_b32 v83, s59, v25\n"
    "v_lshlrev_b32 v84, 16, v26\n"
    "v_and_b32 v85, s59, v26\n"
    "v_lshlrev_b32 v86, 16, v27\n"
    "v_and_b32 v87, s59, v27\n"
    "v_pk_add_f32 v[64:65], v[64:65], v[80:81]\n"
    "v_pk_add_f32 v[66:67], v[66:67], v[82:83]\n"
    "v_pk_add_f32 v[68:69], v[68:69], v[84:85]\n"
    "v_pk_add_f32 v[70:71], v[70:71], v[86:87]\n"
    "s_waitcnt vmcnt(0)\n"
    "v_max3_u32 v9, v9, v28, v29\n"
    "v_max3_u32 v9, v9, v30, v31\n"
    "v_cmp_eq_u32 vcc, -1, v9\n"
    "v_lshlrev_b32 v80, 16, v28\n"
    "v_and_b32 v81, s59, v28\n"
    "v_lshlrev_b32 v82, 16, v29\n"
    "v_and_b32 v83, s59, v29\n"
    "v_lshlrev_b32 v84, 16, v30\n"
    "v_and_b32 v85, s59, v30\n"
    "v_lshlrev_b32 v86, 16, v31\n"
    "v_and_b32 v87, s59, v31\n"
    "v_pk_add_f32 v[72:73], v[72:73], v[80:81]\n"
    "v_pk_add_f32 v[74:75], v[74:75], v[82:83]\n"
    "v_pk_add_f32 v[76:77], v[76:77], v[84:85]\n"
    "v_pk_add_f32 v[78:79], v[78:79], v[86:87]\n"
    "s_cbranch_vccnz L_RED2_AGAIN%=\n"
    "L_RED2_FULL%=:\n"
    "s_add_u32 s60, s60, 1\n"
    "s_add_u32 s61, s61, 1\n"
    "s_cmp_eq_u32 s56, 0\n"
    "s_cbranch_scc1 L_RED2_MID%=\n"
    "v_cvt_pk_bf16_f32 v96, v64, v65\n"
    "v_cvt_pk_bf16_f32 v97, v66, v67\n"
    "v_cvt_pk_bf16_f32 v98, v68, v69\n"
    "v_cvt_pk_bf16_f32 v99, v70, v71\n"
    "v_cvt_pk_bf16_f32 v100, v72, v73\n"
    "v_cvt_pk_bf16_f32 v101, v74, v75\n"
    "v_cvt_pk_bf16_f32 v102, v76, v77\n"
    "v_cvt_pk_bf16_f32 v103, v78, v79\n"
    "global_store_dwordx4 v3, v[96:99], s[40:41] sc1\n"
    "global_store_dwordx4 v3, v[100:103], s[40:41] offset:128 sc1\n"
    "s_add_u32 s40, s40, 0x8000\n"
    "s_addc_u32 s41, s41, 0\n"
    "s_cmp_lt_u32 s60, 64\n"
    "s_cbranch_scc1 L_RED2_LOOP%=\n"
    "s_branch L_RED2_END%=\n"
    "L_RED2_MID%=:\n"
    "v_pk_mul_f32 v[114:115], v[64:65], v[64:65]\n"
    "v_pk_add_f32 v[112:113], v[64:65], v[66:67]\n"
    "v_pk_fma_f32 v[114:115], v[66:67], v[66:67], v[114:115]\n"
    "v_pk_add_f32 v[112:113], v[112:113], v[68:69]\n"
    "v_pk_fma_f32 v[114:115], v[68:69], v[68:69], v[114:115]\n"
    "v_pk_add_f32 v[112:113], v[112:113], v[70:71]\n"
    "v_pk_fma_f32 v[114:115], v[70:71], v[70:71], v[114:115]\n"
    "v_pk_add_f32 v[112:113], v[112:113], v[72:73]\n"
    "v_pk_fma_f32 v[114:115], v[72:73], v[72:73], v[114:115]\n"
    "v_pk_add_f32 v[112:113], v[112:113], v[74:75]\n"
    "v_pk_fma_f32 v[114:115], v[74:75], v[74:75], v[114:115]\n"
    "v_pk_add_f32 v[112:113], v[112:113], v[76:77]\n"
    "v_pk_fma_f32 v[114:115], v[76:77], v[76:77], v[114:115]\n"
    "v_pk_add_f32 v[112:113], v[112:113], v[78:79]\n"
    "v_pk_fma_f32 v[114:115], v[78:79], v[78:79], v[114:115]\n"
    "v_add_f32 v112, v112, v113\n"
    "v_add_f32 v114, v114, v115\n"
    "v_cvt_pk_bf16_f32 v96, v64, v65\n"
    "v_cvt_pk_bf16_f32 v97, v66, v67\n"
    "v_permlane32_swap_b32 v112, v114\n"
    "v_add_f32 v112, v112, v114\n"
    "v_mov_b32 v113, v112\n"
    "v_cvt_pk_bf16_f32 v98, v68, v69\n"
    "v_cvt_pk_bf16_f32 v99, v70, v71\n"
    "v_permlane16_swap_b32 v112, v113\n"
    "v_add_f32 v112, v112, v113\n"
    "v_cvt_pk_bf16_f32 v100, v72, v73\n"
    "v_cvt_pk_bf16_f32 v101, v74, v75\n"
    "v_add_f32_dpp v112, v112, v112 row_ror:8 row_mask:0xf bank_mask:0xf\n"
    "ds_write_b32 v4, v112\n"
    "v_cvt_pk_bf16_f32 v102, v76, v77\n"
    "v_cvt_pk_bf16_f32 v103, v78, v79\n"
    "buffer_store_dwordx4 v[96:99], v0, s[36:39], s62 offen sc1\n"
    "buffer_store_dwordx4 v[100:103], v13, s[36:39], s62 offen sc1\n"
    "v_mov_b32 v9, 0\n"
    "s_nop 1\n"
    "v_mov_b32 v9, 0\n"
    "s_nop 1\n"
    "v_mov_b32 v9, 0\n"
    "s_nop 1\n"
    "s_waitcnt lgkmcnt(0)\n"
    "s_barrier\n"
    "ds_read_b128 v[80:83], v5\n"
    "ds_read_b128 v[84:87], v5 offset:128\n"
    "v_mov_b32 v9, 0\n"
    "s_nop 2\n"
    "v_mov_b32 v9, 0\n"
    "s_nop 2\n"
    "v_mov_b32 v9, 0\n"
    "s_nop 2\n"
    "s_waitcnt lgkmcnt(1)\n"
    "v_pk_add_f32 v[80:81], v[80:81], v[82:83]\n"
    "s_waitcnt lgkmcnt(0)\n"
    "v_pk_add_f32 v[84:85], v[84:85], v[86:87]\n"
    "v_add_f32 v80, v80, v81\n"
    "v_add_f32 v84, v84, v85\n"
    "v_mul_f32 v80, 0x3b000000, v80\n"
    "v_mul_f32 v84, 0x3b000000, v84\n"
    "v_fma_f32 v84, -v80, v80, v84\n"
    "v_max_f32 v84, 0, v84\n"
    "v_add_f32 v84, 0x3727c5ac, v84\n"
    "v_rsq_f32 v84, v84\n"
    "s_nop 0\n"
    "v_mul_f32_e64 v80, -v80, v84\n"
    "v_pk_fma_f32 v[64:65], v[64:65], v[84:85], v[80:81] op_sel_hi:[1,0,0]\n"
    "v_pk_fma_f32 v[66:67], v[66:67], v[84:85], v[80:81] op_sel_hi:[1,0,0]\n"
    "v_pk_fma_f32 v[68:69], v[68:69], v[84:85], v[80:81] op_sel_hi:[1,0,0]\n"
    "v_pk_fma_f32 v[70:71], v[70:71], v[84:85], v[80:81] op_sel_hi:[1,0,0]\n"
    "v_cvt_pk_bf16_f32 v104, v64, v65\n"
    "v_cvt_pk_bf16_f32 v105, v66, v67\n"
    "v_cvt_pk_bf16_f32 v106, v68, v69\n"
    "v_cvt_pk_bf16_f32 v107, v70, v71\n"
    "buffer_store_dwordx4 v[104:107], v0, s[36:39], s63 offen sc1\n"
    "v_pk_fma_f32 v[72:73], v[72:73], v[84:85], v[80:81] op_sel_hi:[1,0,0]\n"
    "v_pk_fma_f32 v[74:75], v[74:75], v[84:85], v[80:81] op_sel_hi:[1,0,0]\n"
    "v_pk_fma_f32 v[76:77], v[76:77], v[84:85], v[80:81] op_sel_hi:[1,0,0]\n"
    "v_pk_fma_f32 v[78:79], v[78:79], v[84:85], v[80:81] op_sel_hi:[1,0,0]\n"
    "v_cvt_pk_bf16_f32 v108, v72, v73\n"
    "v_cvt_pk_bf16_f32 v109, v74, v75\n"
    "v_cvt_pk_bf16_f32 v110, v76, v77\n"
    "v_cvt_pk_bf16_f32 v111, v78, v79\n"
    "buffer_store_dwordx4 v[108:111], v13, s[36:39], s63 offen sc1\n"
    "s_waitcnt vmcnt(0)\n"
    "s_barrier\n"
    "buffer_store_dword v10, v2, s[36:39], 0 offen sc1\n"
    "s_cmp_lt_u32 s60, 64\n"
    "s_cbranch_scc1 L_RED2_LOOP%=\n"
    "s_branch L_RED2_END%=\n"
    "L_RED2_Wf%=:\n"
    "s_nop 0\n"
    "s_cmp_lg_u32 s65, 0\n"
    "s_cbranch_scc1 L_RED2_Wok%=\n"
    "s_add_u32 s64, s64, 1\n"
    "s_and_b32 s70, s64, 255\n"
    "s_cbranch_scc1 L_RED2_W%=\n"
    "buffer_load_dword v9, off, s[36:39], s50 sc1\n"
    "s_waitcnt vmcnt(0)\n"
    "v_readfirstlane_b32 s65, v9\n"
    "s_cmp_lt_u32 s64, 0x40000\n"
    "s_cbranch_scc1 L_RED2_W%=\n"
    "s_mov_b32 s65, 1\n"
    "v_mov_b32 v9, 1\n"
    "buffer_store_dword v9, off, s[36:39], s50 sc1\n"
    "s_branch L_RED2_W%=\n"
    "L_RED2_AGAIN%=:\n"
    "s_nop 7\n"
    "s_cmp_lg_u32 s65, 0\n"
    "s_cbranch_scc1 L_RED2_FULL%=\n"
    "s_sub_u32 s68, s68, 0x10000\n"
    "s_add_u32 s64, s64, 1\n"
    "s_sleep 1\n"
    "s_and_b32 s70, s64, 255\n"
    "s_cbranch_scc1 L_RED2_HASX%=\n"
    "buffer_load_dword v9, off, s[36:39], s50 sc1\n"
    "s_waitcnt vmcnt(0)\n"
    "v_readfirstlane_b32 s65, v9\n"
    "s_cmp_lt_u32 s64, 0x40000\n"
    "s_cbranch_scc1 L_RED2_HASX%=\n"
    "s_mov_b32 s65, 1\n"
    "v_mov_b32 v9, 1\n"
    "buffer_store_dword v9, off, s[36:39], s50 sc1\n"
    "s_branch L_RED2_HASX%=\n"
    "L_RED2_END%=:\n"
    "s_cmp_eq_u32 s56, 0\n"
    "s_cbranch_scc1 L_RED2_FIN%=\n"
    "buffer_store_dwordx2 v[6:7], v2, s[36:39], 0 offen sc1\n"
    "L_RED2_FIN%=:\n"
    "s_waitcnt vmcnt(0)\n"
    "s_endpgm\n"
      GEMM_OPS : ROLE_CLOB);
  } else {
    asm volatile("s_mov_b64 exec, -1\ns_and_b32 s37, s37, 0xffff\ns_mov_b32 s39, 0x20000\ns_nop 4\n"
    "v_add_u32 v13, 0x1000, v0\n"
    "v_readfirstlane_b32 s17, v0\n"
    "s_lshr_b32 s17, s17, 14\n"
    "s_mov_b32 s64, 0\n"
    "s_mov_b32 s65, 0\n"
    "v_mov_b32 v8, 0\n"
    "s_mov_b32 s60, 0\n"
    "s_mov_b32 s61, 1\n"
    "s_mov_b32 s59, 0xffff0000\n"
    "L_RED4_LOOP%=:\n"
    "s_lshr_b32 s68, s46, 8\n"
    "s_xor_b32 s68, s68, s51\n"
    "s_and_b32 s68, s68, 0x2000\n"
    "s_or_b32 s68, s68, s46\n"
    "s_add_u32 s46, s46, s47\n"
    "s_lshr_b32 s69, s48, 8\n"
    "s_xor_b32 s69, s69, s52\n"
    "s_and_b32 s69, s69, 0x2000\n"
    "s_or_b32 s69, s69, s48\n"
    "s_add_u32 s48, s48, 0x10000\n"
    "s_lshr_b32 s62, s49, 8\n"
    "s_xor_b32 s62, s62, s53\n"
    "s_and_b32 s62, s62, 0x2000\n"
    "s_or_b32 s62, s62, s49\n"
    "s_add_u32 s49, s49, 0x10000\n"
    "s_lshr_b32 s63, s57, 8\n"
    "s_xor_b32 s63, s63, s58\n"
    "s_and_b32 s63, s63, 0x2000\n"
    "s_or_b32 s63, s63, s57\n"
    "s_add_u32 s57, s57, 0x10000\n"
    "v_mov_b32 v10, s61\n"
    "s_cmp_eq_u32 s56, 0\n"
    "s_cbranch_scc1 L_RED4_NOX%=\n"
    "global_load_dwordx4 v[48:51], v3, s[40:41]\n"
    "global_load_dwordx4 v[52:55], v3, s[40:41] offset:128\n"
    "s_add_u32 s40, s40, 0x8000\n"
    "s_addc_u32 s41, s41, 0\n"
    "L_RED4_NOX%=:\n"
    "v_cmp_gt_i32 vcc, s61, v8\n"
    "s_cbranch_vccz L_RED4_Wok%=\n"
    "L_RED4_W%=:\n"
    "buffer_load_dword v8, v1, s[36:39], 0 offen sc1\n"
    "s_waitcnt vmcnt(0)\n"
    "v_cmp_gt_i32 vcc, s61, v8\n"
    "s_cbranch_vccnz L_RED4_Wf%=\n"
    "L_RED4_Wok%=:\n"
    "s_cmp_lg_u32 s56, 0\n"
    "s_cbranch_scc1 L_RED4_HASX%=\n"
    "buffer_load_dwordx4 v[48:51], v0, s[36:39], s69 offen sc1\n"
    "buffer_load_dwordx4 v[52:55], v13, s[36:39], s69 offen sc1\n"
    "L_RED4_HASX%=:\n"
    "buffer_load_dwordx4 v[16:19], v0, s[36:39], s68 offen sc1\n"
    "buffer_load_dwordx4 v[20:23], v13, s[36:39], s68 offen sc1\n"
    "s_add_u32 s68, s68, 0x10000\n"
    "buffer_load_dwordx4 v[24:27], v0, s[36:39], s68 offen sc1\n"
    "buffer_load_dwordx4 v[28:31], v13, s[36:39], s68 offen sc1\n"
    "s_add_u32 s68, s68, 0x10000\n"
    "buffer_load_dwordx4 v[32:35], v0, s[36:39], s68 offen sc1\n"
    "buffer_load_dwordx4 v[36:39], v13, s[36:39], s68 offen sc1\n"
    "s_add_u32 s68, s68, 0x10000\n"
    "buffer_load_dwordx4 v[40:43], v0, s[36:39], s68 offen sc1\n"
    "buffer_load_dwordx4 v[44:47], v13, s[36:39], s68 offen sc1\n"
    "s_waitcnt vmcnt(8)\n"
    "v_lshlrev_b32 v64, 16, v48\n"
    "v_and_b32 v65, s59, v48\n"
    "v_lshlrev_b32 v66, 16, v49\n"
    "v_and_b32 v67, s59, v49\n"
    "v_lshlrev_b32 v68, 16, v50\n"
    "v_and_b32 v69, s59, v50\n"
    "v_lshlrev_b32 v70, 16, v51\n"
    "v_and_b32 v71, s59, v51\n"
    "v_lshlrev_b32 v72, 16, v52\n"
    "v_and_b32 v73, s59, v52\n"
    "v_lshlrev_b32 v74, 16, v53\n"
    "v_and_b32 v75, s59, v53\n"
    "v_lshlrev_b32 v76, 16, v54\n"
    "v_and_b32 v77, s59, v54\n"
    "v_lshlrev_b32 v78, 16, v55\n"
    "v_and_b32 v79, s59, v55\n"
    "s_waitcnt vmcnt(7)\n"
    "v_max3_u32 v9, v16, v17, v18\n"
    "v_max_u32 v9, v9, v19\n"
    "v_lshlrev_b32 v80, 16, v16\n"
    "v_and_b32 v81, s59, v16\n"
    "v_lshlrev_b32 v82, 16, v17\n"
    "v_and_b32 v83, s59, v17\n"
    "v_lshlrev_b32 v84, 16, v18\n"
    "v_and_b32 v85, s59, v18\n"
    "v_lshlrev_b32 v86, 16, v19\n"
    "v_and_b32 v87, s59, v19\n"
    "v_pk_add_f32 v[64:65], v[64:65], v[80:81]\n"
    "v_pk_add_f32 v[66:67], v[66:67], v[82:83]\n"
    "v_pk_add_f32 v[68:69], v[68:69], v[84:85]\n"
    "v_pk_add_f32 v[70:71], v[70:71], v[86:87]\n"
    "s_waitcnt vmcnt(6)\n"
    "v_max3_u32 v9, v9, v20, v21\n"
    "v_max3_u32 v9, v9, v22, v23\n"
    "v_lshlrev_b32 v80, 16, v20\n"
    "v_and_b32 v81, s59, v20\n"
    "v_lshlrev_b32 v82, 16, v21\n"
    "v_and_b32 v83, s59, v21\n"
    "v_lshlrev_b32 v84, 16, v22\n"
    "v_and_b32 v85, s59, v22\n"
    "v_lshlrev_b32 v86, 16, v23\n"
    "v_and_b32 v87, s59, v23\n"
    "v_pk_add_f32 v[72:73], v[72:73], v[80:81]\n"
    "v_pk_add_f32 v[74:75], v[74:75], v[82:83]\n"
    "v_pk_add_f32 v[76:77], v[76:77], v[84:85]\n"
    "v_pk_add_f32 v[78:79], v[78:79], v[86:87]\n"
    "s_waitcnt vmcnt(5)\n"
    "v_max3_u32 v9, v9, v24, v25\n"
    "v_max3_u32 v9, v9, v26, v27\n"
    "v_lshlrev_b32 v80, 16, v24\n"
    "v_and_b32 v81, s59, v24\n"
    "v_lshlrev_b32 v82, 16, v25\n"
    "v_and_b32 v83, s59, v25\n"
    "v_lshlrev_b32 v84, 16, v26\n"
    "v_and_b32 v85, s59, v26\n"
    "v_lshlrev_b32 v86, 16, v27\n"
    "v_and_b32 v87, s59, v27\n"
    "v_pk_add_f32 v[64:65], v[64:65], v[80:81]\n"
    "v_pk_add_f32 v[66:67], v[66:67], v[82:83]\n"
    "v_pk_add_f32 v[68:69], v[68:69], v[84:85]\n"
    "v_pk_add_f32 v[70:71], v[70:71], v[86:87]\n"
    "s_waitcnt vmcnt(4)\n"
    "v_max3_u32 v9, v9, v28, v29\n"
    "v_max3_u32 v9, v9, v30, v31\n"
    "v_lshlrev_b32 v80, 16, v28\n"
    "v_and_b32 v81, s59, v28\n"
    "v_lshlrev_b32 v82, 16, v29\n"
    "v_and_b32 v83, s59, v29\n"
    "v_lshlrev_b32 v84, 16, v30\n"
    "v_and_b32 v85, s59, v30\n"
    "v_lshlrev_b32 v86, 16, v31\n"
    "v_and_b32 v87, s59, v31\n"
    "v_pk_add_f32 v[72:73], v[72:73], v[80:81]\n"
    "v_pk_add_f32 v[74:75], v[74:75], v[82:83]\n"
    "v_pk_add_f32 v[76:77], v[76:77], v[84:85]\n"
    "v_pk_add_f32 v[78:79], v[78:79], v[86:87]\n"
    "s_waitcnt vmcnt(3)\n"
    "v_max3_u32 v9, v9, v32, v33\n"
    "v_max3_u32 v9, v9, v34, v35\n"
    "v_lshlrev_b32 v80, 16, v32\n"
    "v_and_b32 v81, s59, v32\n"
    "v_lshlrev_b32 v82, 16, v33\n"
    "v_and_b32 v83, s59, v33\n"
    "v_lshlrev_b32 v84, 16, v34\n"
    "v_and_b32 v85, s59, v34\n"
    "v_lshlrev_b32 v86, 16, v35\n"
    "v_and_b32 v87, s59, v35\n"
    "v_pk_add_f32 v[64:65], v[64:65], v[80:81]\n"
    "v_pk_add_f32 v[66:67], v[66:67], v[82:83]\n"
    "v_pk_add_f32 v[68:69], v[68:69], v[84:85]\n"
    "v_pk_add_f32 v[70:71], v[70:71], v[86:87]\n"
    "s_waitcnt vmcnt(2)\n"
    "v_max3_u32 v9, v9, v36, v37\n"
    "v_max3_u32 v9, v9, v38, v39\n"
    "v_lshlrev_b32 v80, 16, v36\n"
    "v_and_b32 v81, s59, v36\n"
    "v_lshlrev_b32 v82, 16, v37\n"
    "v_and_b32 v83, s59, v37\n"
    "v_lshlrev_b32 v84, 16, v38\n"
    "v_and_b32 v85, s59, v38\n"
    "v_lshlrev_b32 v86, 16, v39\n"
    "v_and_b32 v87, s59, v39\n"
    "v_pk_add_f32 v[72:73], v[72:73], v[80:81]\n"
    "v_pk_add_f32 v[74:75], v[74:75], v[82:83]\n"
    "v_pk_add_f32 v[76:77], v[76:77], v[84:85]\n"
    "v_pk_add_f32 v[78:79], v[78:79], v[86:87]\n"
    "s_waitcnt vmcnt(1)\n"
    "v_max3_u32 v9, v9, v40, v41\n"
    "v_max3_u32 v9, v9, v42, v43\n"
    "v_lshlrev_b32 v80, 16, v40\n"
    "v_and_b32 v81, s59, v40\n"
    "v_lshlrev_b32 v82, 16, v41\n"
    "v_and_b32 v83, s59, v41\n"
    "v_lshlrev_b32 v84, 16, v42\n"
    "v_and_b32 v85, s59, v42\n"
    "v_lshlrev_b32 v86, 16, v43\n"
    "v_and_b32 v87, s59, v43\n"
    "v_pk_add_f32 v[64:65], v[64:65], v[80:81]\n"
    "v_pk_add_f32 v[66:67], v[66:67], v[82:83]\n"
    "v_pk_add_f32 v[68:69], v[68:69], v[84:85]\n"
    "v_pk_add_f32 v[70:71], v[70:71], v[86:87]\n"
    "s_waitcnt vmcnt(0)\n"
    "v_max3_u32 v9, v9, v44, v45\n"
    "v_max3_u32 v9, v9, v46, v47\n"
    "v_cmp_eq_u32 vcc, -1, v9\n"
    "v_lshlrev_b32 v80, 16, v44\n"
    "v_and_b32 v81, s59, v44\n"
    "v_lshlrev_b32 v82, 16, v45\n"
    "v_and_b32 v83, s59, v45\n"
    "v_lshlrev_b32 v84, 16, v46\n"
    "v_and_b32 v85, s59, v46\n"
    "v_lshlrev_b32 v86, 16, v47\n"
    "v_and_b32 v87, s59, v47\n"
    "v_pk_add_f32 v[72:73], v[72:73], v[80:81]\n"
    "v_pk_add_f32 v[74:75], v[74:75], v[82:83]\n"
    "v_pk_add_f32 v[76:77], v[76:77], v[84:85]\n"
    "v_pk_add_f32 v[78:79], v[78:79], v[86:87]\n"
    "s_cbranch_vccnz L_RED4_AGAIN%=\n"
    "L_RED4_FULL%=:\n"
    "s_add_u32 s60, s60, 1\n"
    "s_add_u32 s61, s61, 1\n"
    "v_pk_mul_f32 v[114:115], v[64:65], v[64:65]\n"
    "v_pk_add_f32 v[112:113], v[64:65], v[66:67]\n"
    "v_pk_fma_f32 v[114:115], v[66:67], v[66:67], v[114:115]\n"
    "v_pk_add_f32 v[112:113], v[112:113], v[68:69]\n"
    "v_pk_fma_f32 v[114:115], v[68:69], v[68:69], v[114:115]\n"
    "v_pk_add_f32 v[112:113], v[112:113], v[70:71]\n"
    "v_pk_fma_f32 v[114:115], v[70:71], v[70:71], v[114:115]\n"
    "v_pk_add_f32 v[112:113], v[112:113], v[72:73]\n"
    "v_pk_fma_f32 v[114:115], v[72:73], v[72:73], v[114:115]\n"
    "v_pk_add_f32 v[112:113], v[112:113], v[74:75]\n"
    "v_pk_fma_f32 v[114:115], v[74:75], v[74:75], v[114:115]\n"
    "v_pk_add_f32 v[112:113], v[112:113], v[76:77]\n"
    "v_pk_fma_f32 v[114:115], v[76:77], v[76:77], v[114:115]\n"
    "v_pk_add_f32 v[112:113], v[112:113], v[78:79]\n"
    "v_pk_fma_f32 v[114:115], v[78:79], v[78:79], v[114:115]\n"
    "v_add_f32 v112, v112, v113\n"
    "v_add_f32 v114, v114, v115\n"
    "v_cvt_pk_bf16_f32 v96, v64, v65\n"
    "v_cvt_pk_bf16_f32 v97, v66, v67\n"
    "v_permlane32_swap_b32 v112, v114\n"
    "v_add_f32 v112, v112, v114\n"
    "v_mov_b32 v113, v112\n"
    "v_cvt_pk_bf16_f32 v98, v68, v69\n"
    "v_cvt_pk_bf16_f32 v99, v70, v71\n"
    "v_permlane16_swap_b32 v112, v113\n"
    "v_add_f32 v112, v112, v113\n"
    "v_cvt_pk_bf16_f32 v100, v72, v73\n"
    "v_cvt_pk_bf16_f32 v101, v74, v75\n"
    "v_add_f32_dpp v112, v112, v112 row_ror:8 row_mask:0xf bank_mask:0xf\n"
    "ds_write_b32 v4, v112\n"
    "v_cvt_pk_bf16_f32 v102, v76, v77\n"
    "v_cvt_pk_bf16_f32 v103, v78, v79\n"
    "buffer_store_dwordx4 v[96:99], v0, s[36:39], s62 offen sc1\n"
    "buffer_store_dwordx4 v[100:103], v13, s[36:39], s62 offen sc1\n"
    "v_mov_b32 v9, 0\n"
    "s_nop 1\n"
    "v_mov_b32 v9, 0\n"
    "s_nop 1\n"
    "v_mov_b32 v9, 0\n"
    "s_nop 1\n"
    "s_waitcnt lgkmcnt(0)\n"
    "s_barrier\n"
    "ds_read_b128 v[80:83], v5\n"
    "ds_read_b128 v[84:87], v5 offset:128\n"
    "v_mov_b32 v9, 0\n"
    "s_nop 2\n"
    "v_mov_b32 v9, 0\n"
    "s_nop 2\n"
    "v_mov_b32 v9, 0\n"
    "s_nop 2\n"
    "s_waitcnt lgkmcnt(1)\n"
    "v_pk_add_f32 v[80:81], v[80:81], v[82:83]\n"
    "s_waitcnt lgkmcnt(0)\n"
    "v_pk_add_f32 v[84:85], v[84:85], v[86:87]\n"
    "v_add_f32 v80, v80, v81\n"
    "v_add_f32 v84, v84, v85\n"
    "v_mul_f32 v80, 0x3b000000, v80\n"
    "v_mul_f32 v84, 0x3b000000, v84\n"
    "v_fma_f32 v84, -v80, v80, v84\n"
    "v_max_f32 v84, 0, v84\n"
    "v_add_f32 v84, 0x3727c5ac, v84\n"
    "v_rsq_f32 v84, v84\n"
    "s_nop 0\n"
    "v_mul_f32_e64 v80, -v80, v84\n"
    "v_pk_fma_f32 v[64:65], v[64:65], v[84:85], v[80:81] op_sel_hi:[1,0,0]\n"
    "v_pk_fma_f32 v[66:67], v[66:67], v[84:85], v[80:81] op_sel_hi:[1,0,0]\n"
    "v_pk_fma_f32 v[68:69], v[68:69], v[84:85], v[80:81] op_sel_hi:[1,0,0]\n"
    "v_pk_fma_f32 v[70:71], v[70:71], v[84:85], v[80:81] op_sel_hi:[1,0,0]\n"
    "v_cvt_pk_bf16_f32 v104, v64, v65\n"
    "v_cvt_pk_bf16_f32 v105, v66, v67\n"
    "v_cvt_pk_bf16_f32 v106, v68, v69\n"
    "v_cvt_pk_bf16_f32 v107, v70, v71\n"
    "buffer_store_dwordx4 v[104:107], v0, s[36:39], s63 offen sc1\n"
    "v_pk_fma_f32 v[72:73], v[72:73], v[84:85], v[80:81] op_sel_hi:[1,0,0]\n"
    "v_pk_fma_f32 v[74:75], v[74:75], v[84:85], v[80:81] op_sel_hi:[1,0,0]\n"
    "v_pk_fma_f32 v[76:77], v[76:77], v[84:85], v[80:81] op_sel_hi:[1,0,0]\n"
    "v_pk_fma_f32 v[78:79], v[78:79], v[84:85], v[80:81] op_sel_hi:[1,0,0]\n"
    "v_cvt_pk_bf16_f32 v108, v72, v73\n"
    "v_cvt_pk_bf16_f32 v109, v74, v75\n"
    "v_cvt_pk_bf16_f32 v110, v76, v77\n"
    "v_cvt_pk_bf16_f32 v111, v78, v79\n"
    "buffer_store_dwordx4 v[108:111], v13, s[36:39], s63 offen sc1\n"
    "s_waitcnt vmcnt(0)\n"
    "s_barrier\n"
    "buffer_store_dword v10, v2, s[36:39], 0 offen sc1\n"
    "s_cmp_lt_u32 s60, 64\n"
    "s_cbranch_scc1 L_RED4_LOOP%=\n"
    "s_branch L_RED4_END%=\n"
    "L_RED4_Wf%=:\n"
    "s_nop 0\n"
    "s_cmp_lg_u32 s65, 0\n"
    "s_cbranch_scc1 L_RED4_Wok%=\n"
    "s_add_u32 s64, s64, 1\n"
    "s_and_b32 s70, s64, 255\n"
    "s_cbranch_scc1 L_RED4_W%=\n"
    "buffer_load_dword v9, off, s[36:39], s50 sc1\n"
    "s_waitcnt vmcnt(0)\n"
    "v_readfirstlane_b32 s65, v9\n"
    "s_cmp_lt_u32 s64, 0x40000\n"
    "s_cbranch_scc1 L_RED4_W%=\n"
    "s_mov_b32 s65, 1\n"
    "v_mov_b32 v9, 1\n"
    "buffer_store_dword v9, off, s[36:39], s50 sc1\n"
    "s_branch L_RED4_W%=\n"
    "L_RED4_AGAIN%=:\n"
    "s_nop 7\n"
    "s_cmp_lg_u32 s65, 0\n"
    "s_cbranch_scc1 L_RED4_FULL%=\n"
    "s_sub_u32 s68, s68, 0x30000\n"
    "s_add_u32 s64, s64, 1\n"
    "s_sleep 1\n"
    "s_and_b32 s70, s64, 255\n"
    "s_cbranch_scc1 L_RED4_HASX%=\n"
    "buffer_load_dword v9, off, s[36:39], s50 sc1\n"
    "s_waitcnt vmcnt(0)\n"
    "v_readfirstlane_b32 s65, v9\n"
    "s_cmp_lt_u32 s64, 0x40000\n"
    "s_cbranch_scc1 L_RED4_HASX%=\n"
    "s_mov_b32 s65, 1\n"
    "v_mov_b32 v9, 1\n"
    "buffer_store_dword v9, off, s[36:39], s50 sc1\n"
    "s_branch L_RED4_HASX%=\n"
    "L_RED4_END%=:\n"
    "s_waitcnt vmcnt(0)\n"
    "s_endpgm\n"
      GEMM_OPS : ROLE_CLOB);
  }
}

DI void role_PACE(const Params& p, const Ctx& c) {
  volatile LAS unsigned* seen_at = (volatile LAS unsigned*)(uintptr_t)c.lb;
  seen_at[threadIdx.x] = 0u;
  __syncthreads();
  if (threadIdx.x >= 128) return;
  const __amdgpu_buffer_rsrc_t rs = __builtin_amdgcn_make_buffer_rsrc(p.ws, 0, NREC, 0x00020000);
  if (threadIdx.x >= 64) {
    for (unsigned i = 0, n = 0; c.l && n < NSEG && i < (1u << 16); ++i) {
      asm volatile("" ::: "memory");
      const unsigned v = __builtin_amdgcn_raw_buffer_load_b32(rs, flag(F_XN + (threadIdx.x & 3), c.q), 0, 16);
      while (n < NSEG && !__any(v <= n)) seen_at[n++] = (unsigned)__builtin_amdgcn_s_memrealtime() | 1u;
      __builtin_amdgcn_s_sleep(1);
    }
    return;
  }
  const unsigned base = O_COMM + c.q + MB, l16 = threadIdx.x * 16;
  const unsigned LM = (p.dly2 & 0xffffu), B0 = (p.dly2 >> 16), LMAX = p.dly3 & 0xffffu, MBIG = (p.dly3 >> 16) & 255u;
  unsigned due = (unsigned)__builtin_amdgcn_s_memrealtime(), spins = 0;
  v2u k0 = {0u, 0u}, k1 = k0, k2 = k0;
  const unsigned go = threadIdx.x < 32 ? flag(F_GO + 64 * (threadIdx.x >> 3) + (threadIdx.x & 7), c.q) : OOB, g2 = threadIdx.x & 4 ? OOB : go;
  __builtin_amdgcn_raw_buffer_store_b32(3u, rs, go, 0, 16);
  int adj = 0, calm = 1;
  const unsigned G2 = p.dly3 >> 24;
  __builtin_amdgcn_raw_buffer_store_b32(G2 ? 0u : 64u, rs, g2, (F_G2 - F_GO) * 128, 16);
  float span = SPAN_CYC / SPAN0;
  if (c.l) {
    for (unsigned i = 0; i < (1u << 16) && seen_at[1] == 0u; ++i) __builtin_amdgcn_s_sleep(1);
    due = (unsigned)__builtin_amdgcn_s_memrealtime() + START1;
    if (threadIdx.x == 0) __builtin_amdgcn_raw_buffer_store_b64(v2u{due & 0x7fffffffu, (unsigned)span + (p.ex2 & 255u) + ((B0 + 4) << 8)}, rs, 24, base, 16);
    due += B0 + 4;
  } else
  for (unsigned i = 0, seen = 0; i < 256; ++i) {
    asm volatile("" ::: "memory");
    int v = (int)__builtin_amdgcn_raw_buffer_load_b32(rs, (threadIdx.x & 7) * 4 + (seen && (threadIdx.x & 8) ? 63 * 128 : 0), base, 16);
#pragma unroll
    for (int o = 1; o < 16; o <<= 1) v = max(v, __shfl_xor(v, o));
    const unsigned now = (unsigned)__builtin_amdgcn_s_memrealtime(), d = (UNI(v) - now) << 1;
    if (v >= 0 && (seen || (int)d < 0)) { due = now + min((int)d >> 1, 400) + B0 + B_FIRST; break; }
    if (v >= 0 && d < 800u) { while ((int)((unsigned)__builtin_amdgcn_s_memrealtime() - now - (d >> 1)) < 0) {} seen = 1; }
    else __builtin_amdgcn_s_sleep(16);
  }
  for (int s = 1; s < NSEG - 1; ++s) {
    while ((int)((unsigned)__builtin_amdgcn_s_memrealtime() - due) < 0) {}
    for (;;) {
      asm volatile("" ::: "memory");
      unsigned m = 0;
#pragma unroll
      for (int i = 0; i < 8; ++i) { const v4u v = __builtin_amdgcn_raw_buffer_load_b128(rs, l16 + i * 1024, base + s * 16384, 16); m = max(max(m, max(v.x, v.y)), max(v.z, v.w)); }
      if (!__any(m == SENT)) break;
      if (++spins > (1u << 16)) break;
      if (s == 1) __builtin_amdgcn_s_sleep(4);
    }
    const unsigned t = (unsigned)__builtin_amdgcn_s_memrealtime();
    if (t - due > LMAX) { if (s > 3 && calm) adj = min(adj + ADJ_UP, 8 * 16); calm = 0; } else { adj = max(adj - ADJ_DN, ADJ_MIN * 16); calm = 1; }
    const unsigned B = B0 + (adj >> 4);
    unsigned plan = (t - due > LMAX ? t + MBIG : due + LM + (adj >> 4) + (s == 1 ? L_FIRST : 0)) + (unsigned)span;
    if (c.l && s < NSEG - 2) {
      const unsigned a = UNI(seen_at[s + 1]), need = (a ? a : t) + START1 + 2u * (unsigned)span - 184u;
      if ((int)(need - plan) > 0) plan = need;
    }
    if (threadIdx.x == 0) __builtin_amdgcn_raw_buffer_store_b64(v2u{plan & 0x7fffffffu, (unsigned)span + (p.ex2 & 255u) + (B << 8)}, rs, 24, base + s * 128, 16);
    for (unsigned todo = G2 ? 3 : 1; todo;) {
      const unsigned now = (unsigned)__builtin_amdgcn_s_memrealtime() - plan;
      if ((todo & 1) && (int)(now + (c.l ? G1L1 : p.stag >> 24)) >= 0) { todo &= ~1u; __builtin_amdgcn_raw_buffer_store_b32(s + 3u, rs, go, 0, 16); }
      if ((todo & 2) && (int)(now + G2) >= 0) { todo &= ~2u; __builtin_amdgcn_raw_buffer_store_b32(s < (c.l ? REL1 : REL0) ? s : 64u, rs, g2, (F_G2 - F_GO) * 128, 16); }
    }
    {
      const v2u k = __builtin_amdgcn_raw_buffer_load_b64(rs, flag(F_R1 + 16 + (threadIdx.x & 3), c.q), 0, 16);
      float v = k0.x != 0u && k.x != k0.x ? SPAN_CYC * (float)(k.y - k0.y) / (float)(k.x - k0.x) + 0.5f : 0.f;
#pragma unroll
      for (int o = 1; o < 4; o <<= 1) v = fmaxf(v, __shfl_xor(v, o));
      if (v > 60.f && v < 160.f) span = v + (s < RAMP_N ? RAMP_B : 0.f);
      k0 = WIN > 2 ? k1 : k2; k1 = k2; k2 = k;
    }
    while ((int)((unsigned)__builtin_amdgcn_s_memrealtime() - plan) < 0) {}
    asm volatile("" ::: "memory");
    unsigned late = (__builtin_amdgcn_raw_buffer_load_b32(rs, (threadIdx.x & 7) * 4, base + s * 128, 16) - plan) & 0x7fffffffu;
    late = late < 200u ? late : 0u;
#pragma unroll
    for (int o = 1; o < 8; o <<= 1) late = max(late, (unsigned)__shfl_xor((int)late, o));
    due = plan + UNI(late) + B;
  }
}

__global__ void __launch_bounds__(256, 1) k_main(Params p) {
  extern __shared__ __attribute__((aligned(16))) u8 smem[];
  LAS unsigned* lds = (LAS unsigned*)smem;
  if ((blockIdx.x >> 4) == 8) {
    u64 pc; unsigned o = ((blockIdx.x & 8u) * 32u + threadIdx.x) * 128u;
    asm volatile("s_getpc_b64 %0\nL_TOP:\n\ts_max_i32 m0, 0, L_GU_FIN-L_TOP\n\ts_max_i32 m0, m0, L_R_FIN-L_TOP\n\ts_max_i32 m0, m0, L_QA_FIN-L_TOP\n\ts_max_i32 m0, m0, L_KV_FIN-L_TOP\n\t"
                 "v_min_u32 %1, m0, %1\n\ts_mov_b32 m0, %2\n\ts_nop 0\n\tglobal_load_lds_dword %1, %0"
                 : "=&s"(pc), "+v"(o) : "s"((unsigned)(uintptr_t)lds + L_DUMP) : "memory", "scc");
  }
  if ((blockIdx.x >> 4) == 9) {
    const unsigned w = UNI(threadIdx.x >> 6), l = threadIdx.x & 63, tl = (unsigned)(uintptr_t)lds + L_DUMP;
    const u8* a = blockIdx.x & 8u ? p.ws + 0x7000 + (w == 0 ? (l < 32 ? O_INB : O_OF - 2 * MB) : (w == 1 ? O_XN0 : (w == 2 ? (l < 32 ? O_KF : O_VF - 2 * MB) : O_COMM)))
                                  : (const u8*)(w == 0 ? p.wk : (w == 1 ? p.wv : (w == 2 ? p.wq : p.wo)));
    asm volatile("s_mov_b32 m0, %1\n\ts_nop 0\n\tglobal_load_lds_dword %0, off sc1" ::"v"(a + l * 65536), "s"(tl) : "memory");
  }
  unsigned x = (unsigned)__builtin_amdgcn_s_getreg(20 | (3 << 11)) & 7u;
  const unsigned r = blockIdx.x >> 3;
  Ctx c;
  c.lb = (unsigned)(uintptr_t)lds; c.l = 0; c.q = 0; c.slot = x * 32 + r;
  if (x >= 4 && r < 16) {
    const unsigned tid = threadIdx.x, xi = x & 3, tl = c.lb + L_DUMP;
    const size_t o = (size_t)((r >> 2) * 128 + (r & 3) * 16 + (tid >> 4)) * HD + xi * 1024 + (tid & 15) * 64;
    TOUCH(p.wk + o); TOUCH(p.wv + o);
    TOUCH(p.wk + o + 64 * HD); TOUCH(p.wv + o + 64 * HD); TOUCH(p.wq + o); TOUCH(p.wq + o + 64 * HD);
    const size_t o2 = (size_t)(xi * 1024 + (r >> 1) * 128 + (r & 1) * 32) * DM + tid * 64;
    TOUCH(p.wo + o2); TOUCH(p.wo + o2 + 64 * DM);
  }
  if (r < 8) role_R(p, c, x);
  else if (r < 16) role_gemm<0>(p, c, 8 * (x & 3) + (r & 7));
  else {
    v2u tag;
    const unsigned g = setup(p, lds, x, tag);
    if (r < 20) { role_gemm<4>(p, c, 4 * (x & 3) + (r & 3)); return; }
    if (g == ~0u) return;
    x = g & 7u;
    const unsigned xi = x & 3;
    c.l = x >> 2; c.q = ((g >> 3) ^ c.l) << 13; c.slot = x * 32 + r;
    if (r < 24) role_gemm<2>(p, c, 4 * xi + (r & 3));
    else if (r < 26) role_gemm<3>(p, c, 2 * xi + (r & 1));
    else if (r == 26) role_RED<false>(p, c, xi, tag);
    else if (r == 27) role_RED<true>(p, c, xi, tag);
    else if (4 * xi + r - 28 < 14) role_gemm<1>(p, c, 4 * xi + r - 28);
    else if (r == 30) role_PACE(p, c);
  }
}

extern "C" void kernel_launch(void* const* d_in, const int* in_sizes, int n_in, void* d_out, int out_size, void* d_ws, size_t ws_size,
                              hipStream_t stream) {
  Params p;
  p.x = (const u16*)d_in[0]; p.mem0 = (const u16*)d_in[1]; p.wq = (const u16*)d_in[2]; p.wk = (const u16*)d_in[3]; p.wv = (const u16*)d_in[4];
  p.wo = (const u16*)d_in[5]; p.wg = (const u16*)d_in[8]; p.wu = (const u16*)d_in[9]; p.wd = (const u16*)d_in[10];
  p.out = (u16*)d_out;
  p.ws = (u8*)(((uintptr_t)d_ws + (2 * MB - 1)) & ~(uintptr_t)(2 * MB - 1));
  p.dly = g_dly; p.stag = g_stag; p.dly2 = g_dly2; p.dly3 = g_dly3; p.ex = g_ex; p.ex2 = g_ex2;
  (void)hipFuncSetAttribute((const void*)k_main, hipFuncAttributeMaxDynamicSharedMemorySize, LDS_BYTES);
  k_main<<<256, 256, LDS_BYTES, stream>>>(p);
}
```
